```python
import torch

def _softmax(x, axis=-1):
    return torch.softmax(x, dim=axis)
def _gelu(x):
    return torch.nn.functional.gelu(x, approximate="tanh")
def _silu(x):
    return torch.nn.functional.silu(x)
def _sigmoid(x):
    return torch.sigmoid(x)
def _layernorm(x, axis=-1, eps=1e-5):
    m = x.mean(dim=axis, keepdim=True)
    v = x.var(dim=axis, keepdim=True, unbiased=False)
    return (x - m) / torch.sqrt(v + eps)
def _scatter_add(tgt, idx, src, axis=0):
    return tgt.clone().index_add_(axis, idx, src)
def _segment_sum(src, seg, n):
    out = torch.zeros((n,) + src.shape[1:], dtype=src.dtype)
    return out.index_add_(0, seg, src)
def _pad(x, pw):
    flat = tuple(v for pair in reversed(pw) for v in pair)
    return torch.nn.functional.pad(x, flat)

kernel_name = 'sequential_preLN__sliding_local_dilated__geglu_ffn__prompt'

def setup_inputs():

    _w = (lambda a: (lambda t: t.to(torch.bfloat16) if t.is_floating_point() else t)(torch.as_tensor(a)))
    def _A_setup():
        B, S, D = 1, 2048, 576
        return {
            "x": _w((lambda *s: torch.randn(*s, dtype=torch.bfloat16) * 0.05)(B, S, D)),
            "wqkv": _w((lambda *s: torch.randn(*s, dtype=torch.bfloat16) * 0.05)(D, 3 * D)),
            "wo": _w((lambda *s: torch.randn(*s, dtype=torch.bfloat16) * 0.05)(D, D)),
        }
    _A_in = _A_setup()

    def _B_setup():
        B, S, D, F = 1, 2048, 576, 4608
        return {
            "x": _w((lambda *s: torch.randn(*s, dtype=torch.bfloat16) * 0.05)(B, S, D)),
            "w_gate": _w((lambda *s: torch.randn(*s, dtype=torch.bfloat16) * 0.05)(D, F)),
            "w_up": _w((lambda *s: torch.randn(*s, dtype=torch.bfloat16) * 0.05)(D, F)),
            "w_down": _w((lambda *s: torch.randn(*s, dtype=torch.bfloat16) * 0.05)(F, D)),
        }
    _B_in = _B_setup()

    return {
        "A_x": _A_in["x"],
        "A_wqkv": _A_in["wqkv"],
        "A_wo": _A_in["wo"],
        "B_x": _B_in["x"],
        "B_w_gate": _B_in["w_gate"],
        "B_w_up": _B_in["w_up"],
        "B_w_down": _B_in["w_down"],
    }

def reference(A_x, A_wqkv, A_wo, B_x, B_w_gate, B_w_up, B_w_down):
    def _A_ref(x, wqkv, wo):
        B, S, D = x.shape
        H = 32; Dh = D // H
        W, dil = 8, 2
        qkv = (lambda x, *d: x.permute(*d))((x @ wqkv).reshape(B, S, 3, H, Dh), 2, 0, 3, 1, 4)
        q, k, v = qkv[0], qkv[1], qkv[2]
        scale = Dh ** -0.5
        pos = torch.arange(S)
        off = torch.arange(W) * dil
        idx = pos[:, None] - off[None, :]
        mask = idx >= 0
        idx_c = torch.where(mask, idx, 0).reshape(-1)
        kg = k[:, :, idx_c, :].reshape(B, H, S, W, Dh)
        vg = v[:, :, idx_c, :].reshape(B, H, S, W, Dh)
        logits = torch.einsum("bhsd,bhswd->bhsw", q, kg) * scale
        logits = torch.where(mask[None, None, :, :], logits, -1e9)
        attn = _softmax(logits, axis=-1)
        o = torch.einsum("bhsw,bhswd->bhsd", attn, vg)
        return (lambda x, *d: x.permute(*d))(o, 0, 2, 1, 3).reshape(B, S, D) @ wo

    def _B_ref(x, w_gate, w_up, w_down):
        return (_gelu(x @ w_gate) * (x @ w_up)) @ w_down

    x = A_x
    h = x + _A_ref(_layernorm(x, axis=-1), A_wqkv, A_wo)
    h2 = h + _B_ref(_layernorm(h, axis=-1), B_w_gate, B_w_up, B_w_down)
    return h2
```

```cpp
#include <hip/hip_runtime.h>
#include <hip/hip_bf16.h>
#include <stdint.h>

#define LAS __attribute__((address_space(3)))
#define GAS __attribute__((address_space(1)))
#define DI __device__ __forceinline__
typedef unsigned short u16;
typedef short s16x4 __attribute__((ext_vector_type(4)));
typedef short s16x8 __attribute__((ext_vector_type(8)));
typedef __bf16 bf16x2 __attribute__((ext_vector_type(2)));
typedef __bf16 bf16x8 __attribute__((ext_vector_type(8)));
typedef float f32x2 __attribute__((ext_vector_type(2)));
typedef float f32x4 __attribute__((ext_vector_type(4)));
typedef float f32x16 __attribute__((ext_vector_type(16)));
typedef unsigned u32x2 __attribute__((ext_vector_type(2)));
typedef unsigned u32x4 __attribute__((ext_vector_type(4)));

constexpr int S_ = 2048, D_ = 576, F_ = 4608;

constexpr size_t WS_O    = 0;
constexpr size_t WS_H    = WS_O + (size_t)S_ * D_ * 2;
constexpr size_t WS_ST   = WS_H + (size_t)S_ * D_ * 2;
constexpr size_t WS_CS   = WS_ST + (size_t)S_ * 8 * 8;
constexpr size_t WS_CNT  = WS_CS + (size_t)2 * F_ * 4;
constexpr size_t WS_ACT  = WS_CNT + 256 * 256;
constexpr size_t WS_PART = WS_ACT + (size_t)S_ * F_ * 2;
constexpr size_t PART_SLAB = 18 * 1024;

DI float bf_lo(unsigned v) { return __uint_as_float(v << 16); }
DI float bf_hi(unsigned v) { return __uint_as_float(v & 0xffff0000u); }
DI unsigned pk_bf16(float a, float b) { f32x2 v = {a, b}; return __builtin_bit_cast(unsigned, __builtin_convertvector(v, bf16x2)); }
DI float rbf(float a) { return bf_lo(pk_bf16(a, 0.f)); }
DI float dot2bf(unsigned a, unsigned b, float c) { return __builtin_amdgcn_fdot2_f32_bf16(__builtin_bit_cast(bf16x2, a), __builtin_bit_cast(bf16x2, b), c, false); }
DI s16x4 tr_read(LAS char* p) { return __builtin_amdgcn_ds_read_tr16_b64_v4i16((LAS s16x4*)p); }
DI bf16x8 cat8(s16x4 a, s16x4 b) { return __builtin_bit_cast(bf16x8, __builtin_shufflevector(a, b, 0, 1, 2, 3, 4, 5, 6, 7)); }
DI void glds16(const void* g, LAS char* l) {
  unsigned keep; const unsigned la = __builtin_amdgcn_readfirstlane((unsigned)(size_t)l);
  asm volatile("s_mov_b32 %0, m0\n\ts_mov_b32 m0, %2\n\ts_nop 0\n\tglobal_load_lds_dwordx4 %1, off\n\ts_mov_b32 m0, %0" : "=&s"(keep) : "v"(g), "s"(la) : "memory");
}
#define MFMA16(a, b, c) __builtin_amdgcn_mfma_f32_16x16x32_bf16((a), (b), (c), 0, 0, 0)
#define MFMA32(a, b, c) __builtin_amdgcn_mfma_f32_32x32x16_bf16((a), (b), (c), 0, 0, 0)
#define BAR() __builtin_amdgcn_s_barrier()
#define SCHED0() __builtin_amdgcn_sched_barrier(0)
#define WAITV(n) asm volatile("s_waitcnt vmcnt(" #n ")" ::: "memory")
#define WAITVL(n) asm volatile("s_waitcnt vmcnt(" #n ") lgkmcnt(0)" ::: "memory")
#define WAITL0() asm volatile("s_waitcnt lgkmcnt(0)" ::: "memory")

DI unsigned fm32_piece(int tok, int c4, int KB16) {
  const int pi = (c4 & 15) >> 2;
  return (unsigned)(((tok >> 5) * KB16 + (c4 >> 4)) * 1024 + (((pi & 1) * 32 + (tok & 31)) * 16) + (pi >> 1) * 8);
}

constexpr int KA_WROW = 240;
constexpr int KA_LDS = 576 * KA_WROW;
constexpr int KA_QROW = 304;
constexpr int KA_OOFF = 144 * KA_QROW;

__global__ __launch_bounds__(576, 1) void kA(const u16* __restrict__ x, const u16* __restrict__ wqkv, char* __restrict__ ws) {
  extern __shared__ __attribute__((aligned(16))) char smem[];
  LAS char* lds = (LAS char*)smem;
  const int tid = threadIdx.x, lane = tid & 63, w = __builtin_amdgcn_readfirstlane(tid >> 6);
  const int bx = blockIdx.x, xcd = bx & 7, jj = bx >> 3;
  const int mt = (xcd >> 1) * 4 + (jj & 3), hp = (xcd & 1) * 8 + (jj >> 2);
  const int g = lane >> 4, r = lane & 15;
  if (bx == 0 && tid < 256) *(unsigned*)(ws + WS_CNT + (size_t)tid * 256) = 0u;

  int row = mt * 128 - 16 + w * 16 + r; row = row < 0 ? 0 : row;
  const char* xp = (const char*)x + (size_t)row * (D_ * 2) + g * 16;
  u32x4 xr[18];
#pragma unroll
  for (int s = 0; s < 18; ++s) asm volatile("global_load_dwordx4 %0, %1, off offset:%2" : "=v"(xr[s]) : "v"(xp), "i"(s * 64) : "memory");

  const int segoff = (72 * hp) & ~15;
#pragma unroll
  for (int c = 0; c < 3; ++c)
#pragma unroll
    for (int i = 0; i < 5; ++i) {
      const int inst = c * 45 + w * 5 + i;
      const int P = inst * 64 + lane, pr = P / 15, ch = P - pr * 15, seg = ch / 5, c5 = ch - seg * 5;
      const int p16 = pr & 15, k = (pr & ~15) + 8 * (p16 >> 3) + 4 * (p16 & 1) + ((p16 >> 1) & 3);
      glds16((const char*)wqkv + (size_t)k * 3456 + seg * 1152 + segoff + c5 * 16, lds + inst * 1024);
    }
  asm volatile("s_waitcnt vmcnt(15)"
               : "+v"(xr[0]), "+v"(xr[1]), "+v"(xr[2]), "+v"(xr[3]), "+v"(xr[4]), "+v"(xr[5]), "+v"(xr[6]), "+v"(xr[7]), "+v"(xr[8]),
                 "+v"(xr[9]), "+v"(xr[10]), "+v"(xr[11]), "+v"(xr[12]), "+v"(xr[13]), "+v"(xr[14]), "+v"(xr[15]), "+v"(xr[16]), "+v"(xr[17])
               :: "memory");
  {
    float sum = 0.f;
#pragma unroll
    for (int s = 0; s < 18; ++s)
#pragma unroll
      for (int e = 0; e < 4; ++e) sum = dot2bf(xr[s][e], 0x3f803f80u, sum);
    sum += __shfl_xor(sum, 16); sum += __shfl_xor(sum, 32);
    const float mean = sum * (1.0f / D_), mb = rbf(mean);
    float q0 = 0.f, q1 = 0.f;
#pragma unroll
    for (int s = 0; s < 18; ++s) {
      asm volatile("" : "+v"(xr[s]) : "v"(mb));
#pragma unroll
      for (int e = 0; e < 4; ++e) {
        const float a = bf_lo(xr[s][e]) - mb, b = bf_hi(xr[s][e]) - mb;
        q0 = fmaf(a, a, q0); q1 = fmaf(b, b, q1);
        xr[s][e] = pk_bf16(a, b);
      }
      asm volatile("" : "+v"(xr[s]), "+v"(q0), "+v"(q1));
    }
    float q = q0 + q1;
    q += __shfl_xor(q, 16); q += __shfl_xor(q, 32);
    const float dm = mean - mb;
    const float var = rbf(fmaxf(q * (1.0f / D_) - dm * dm, 0.f));
    const float sd = rbf(sqrtf(rbf(var + 1e-5f)));
    const float rs = 1.0f / sd;
#pragma unroll
    for (int s = 0; s < 18; ++s) {
      asm volatile("" : "+v"(xr[s]) : "v"(rs));
#pragma unroll
      for (int e = 0; e < 4; ++e) xr[s][e] = pk_bf16(bf_lo(xr[s][e]) * rs, bf_hi(xr[s][e]) * rs);
      asm volatile("" : "+v"(xr[s]));
    }
  }
  f32x4 acc[7];
#pragma unroll
  for (int b = 0; b < 7; ++b) acc[b] = (f32x4){0.f, 0.f, 0.f, 0.f};
  {
    const int q = r >> 2, p = r & 3;
    const int rowpos = 16 * (g >> 1) + 2 * (4 * (g & 1) + q);
    unsigned adr[7];
#pragma unroll
    for (int b = 0; b < 7; ++b) {
      int pc = 4 * b + p; pc = pc > 26 ? 26 : pc;
      const int seg = pc / 9, wi = pc - seg * 9;
      adr[b] = (unsigned)(rowpos * KA_WROW + seg * 80 + ((72 * hp) & 15) + wi * 8);
    }
#pragma unroll
    for (int c = 0; c < 3; ++c) {
      if (c == 0) WAITV(10); else if (c == 1) WAITV(5); else WAITV(0);
      BAR();
      asm volatile("" ::: "memory");
#pragma unroll
      for (int s6 = 0; s6 < 6; ++s6) {
        const int s = c * 6 + s6;
        const bf16x8 xf = __builtin_bit_cast(bf16x8, xr[s]);
#pragma unroll
        for (int b = 0; b < 7; ++b) {
          if (b < 2 && w == 0) continue;
          LAS char* a = lds + adr[b] + c * 46080;
          const bf16x8 wf = cat8(tr_read(a + s6 * 7680), tr_read(a + s6 * 7680 + KA_WROW));
          acc[b] = MFMA16(wf, xf, acc[b]);
        }
      }
    }
  }
  WAITL0();
  BAR();
  asm volatile("" ::: "memory");
  {
    LAS char* rowp = lds + (w * 16 + r) * KA_QROW;
#pragma unroll
    for (int b = 0; b < 7; ++b) {
      const int pc = 4 * b + g;
      if (pc < 27) {
        const int seg = pc / 9, c0 = (pc - seg * 9) * 4;
#pragma unroll
        for (int e = 0; e < 2; ++e) {
          const int c = c0 + 2 * e, hd = c >= 18 ? 1 : 0, d = c - 18 * hd;
          *(LAS unsigned*)(rowp + (seg * 2 + hd) * 48 + d * 2) = pk_bf16(acc[b][2 * e], acc[b][2 * e + 1]);
        }
      }
    }
  }
  __syncthreads();
  if (tid < 256) {
    const int t = tid & 127, hd = tid >> 7, T = mt * 128 + t;
    LAS char* base = lds + (t + 16) * KA_QROW + hd * 48;
    unsigned qv[9];
    { const u32x4 a = *(LAS u32x4*)(base), b = *(LAS u32x4*)(base + 16); const unsigned c = *(LAS unsigned*)(base + 32);
      qv[0] = a[0]; qv[1] = a[1]; qv[2] = a[2]; qv[3] = a[3]; qv[4] = b[0]; qv[5] = b[1]; qv[6] = b[2]; qv[7] = b[3]; qv[8] = c; }
    float lg[8]; float mx = -3.0e38f;
#pragma unroll
    for (int wv = 0; wv < 8; ++wv) {
      LAS char* kp = base - wv * 2 * KA_QROW + 96;
      const u32x4 a = *(LAS u32x4*)(kp), b = *(LAS u32x4*)(kp + 16); const unsigned c = *(LAS unsigned*)(kp + 32);
      float s = 0.f;
      s = dot2bf(qv[0], a[0], s); s = dot2bf(qv[1], a[1], s); s = dot2bf(qv[2], a[2], s); s = dot2bf(qv[3], a[3], s);
      s = dot2bf(qv[4], b[0], s); s = dot2bf(qv[5], b[1], s); s = dot2bf(qv[6], b[2], s); s = dot2bf(qv[7], b[3], s);
      s = dot2bf(qv[8], c, s);
      s = rbf(rbf(s) * 0.23570226039551584f);
      s = (T - 2 * wv >= 0) ? s : -1.0e9f;
      lg[wv] = s; mx = fmaxf(mx, s);
    }
    float sum = 0.f;
#pragma unroll
    for (int wv = 0; wv < 8; ++wv) { lg[wv] = __builtin_amdgcn_exp2f((lg[wv] - mx) * 1.4426950408889634f); sum += lg[wv]; }
    const float inv = 1.0f / sum;
    float o[18];
#pragma unroll
    for (int d = 0; d < 18; ++d) o[d] = 0.f;
#pragma unroll
    for (int wv = 0; wv < 8; ++wv) {
      const float p = rbf(lg[wv] * inv);
      LAS char* vp = base - wv * 2 * KA_QROW + 192;
      const u32x4 a = *(LAS u32x4*)(vp), b = *(LAS u32x4*)(vp + 16); const unsigned c = *(LAS unsigned*)(vp + 32);
      const unsigned vv[9] = {a[0], a[1], a[2], a[3], b[0], b[1], b[2], b[3], c};
#pragma unroll
      for (int i = 0; i < 9; ++i) { o[2 * i] = fmaf(p, bf_lo(vv[i]), o[2 * i]); o[2 * i + 1] = fmaf(p, bf_hi(vv[i]), o[2 * i + 1]); }
    }
    LAS char* op = lds + KA_OOFF + t * 80 + hd * 36;
#pragma unroll
    for (int i = 0; i < 9; ++i) *(LAS unsigned*)(op + 4 * i) = pk_bf16(o[2 * i], o[2 * i + 1]);
  }
  __syncthreads();
#pragma unroll
  for (int i = 0; i < 2; ++i) {
    const int id = tid + i * 576;
    const int pc = id >> 7, t = id & 127;
    const u32x2 v = *(LAS u32x2*)(lds + KA_OOFF + t * 80 + pc * 8);
    *(u32x2*)(ws + WS_O + fm32_piece(mt * 128 + t, hp * 36 + pc * 4, 36)) = v;
  }
}

constexpr int KB_WROW = 160;
constexpr int KB_CSOFF = 576 * KB_WROW + 2048;
constexpr int KB_LDS = KB_CSOFF + 28 * 36 * 4;

__global__ __launch_bounds__(512, 1) void kB(const u16* __restrict__ x, const u16* __restrict__ wo, const u16* __restrict__ wg,
                                             const u16* __restrict__ wu, char* __restrict__ ws) {
  extern __shared__ __attribute__((aligned(16))) char smem[];
  LAS char* lds = (LAS char*)smem;
  const int tid = threadIdx.x, lane = tid & 63, w = __builtin_amdgcn_readfirstlane(tid >> 6);
  const int bx = blockIdx.x, xcd = bx & 7, jj = bx >> 3;
  const int mt = xcd * 4 + (jj & 3), nt = jj >> 2;
  const int g = lane >> 4, r = lane & 15;
  if (w < 4) {
    const int tok = mt * 64 + w * 16 + r;
    const char* op = ws + WS_O + (size_t)((tok >> 5) * 36 + (g >> 1)) * 1024 + ((g & 1) * 32 + (tok & 31)) * 16;
    u32x4 of[18];
#pragma unroll
    for (int s = 0; s < 18; ++s) asm volatile("global_load_dwordx4 %0, %1, off" : "=v"(of[s]) : "v"(op + s * 2048) : "memory");
    u32x2 xres[5];
    const char* xp = (const char*)x + ((size_t)tok * D_ + nt * 72 + 4 * g) * 2;
#pragma unroll
    for (int b = 0; b < 5; ++b) { const char* pp = xp + ((b == 4 && g >= 2) ? 0 : b * 32);
      asm volatile("global_load_dwordx2 %0, %1, off" : "=v"(xres[b]) : "v"(pp) : "memory"); }
#pragma unroll
    for (int c = 0; c < 3; ++c)
#pragma unroll
      for (int i = 0; i < 8; ++i) {
        int sl = w * 8 + i;
        const int inst = c * 30 + (sl < 30 ? sl : 29);
        const int P = inst * 64 + lane, k = P / 10; int ch = P - k * 10;
        if (nt == 7 && ch == 9) ch = 8;
        glds16((const char*)wo + (size_t)k * (D_ * 2) + nt * 144 + ch * 16, lds + inst * 1024);
      }
    f32x4 acc[5];
#pragma unroll
    for (int b = 0; b < 5; ++b) acc[b] = (f32x4){0.f, 0.f, 0.f, 0.f};
    const unsigned adr = (unsigned)((16 * (g >> 1) + 4 * (g & 1) + (r >> 2)) * KB_WROW + (r & 3) * 8);
#pragma unroll
    for (int c = 0; c < 3; ++c) {
      if (c == 0) {
        asm volatile("s_waitcnt vmcnt(16)"
                     : "+v"(of[0]), "+v"(of[1]), "+v"(of[2]), "+v"(of[3]), "+v"(of[4]), "+v"(of[5]), "+v"(of[6]), "+v"(of[7]), "+v"(of[8]),
                       "+v"(of[9]), "+v"(of[10]), "+v"(of[11]), "+v"(of[12]), "+v"(of[13]), "+v"(of[14]), "+v"(of[15]), "+v"(of[16]), "+v"(of[17]),
                       "+v"(xres[0]), "+v"(xres[1]), "+v"(xres[2]), "+v"(xres[3]), "+v"(xres[4])
                     :: "memory");
      } else if (c == 1) WAITV(8); else WAITV(0);
      BAR();
      asm volatile("" ::: "memory");
#pragma unroll
      for (int s6 = 0; s6 < 6; ++s6) {
        const int s = c * 6 + s6;
        const bf16x8 xf = __builtin_bit_cast(bf16x8, of[s]);
        LAS char* a = lds + adr + c * (192 * KB_WROW) + s6 * (32 * KB_WROW);
#pragma unroll
        for (int b = 0; b < 5; ++b) {
          const bf16x8 wf = cat8(tr_read(a + b * 32), tr_read(a + b * 32 + 8 * KB_WROW));
          acc[b] = MFMA16(wf, xf, acc[b]);
        }
      }
    }
    float hv[5][4]; float sum = 0.f;
#pragma unroll
    for (int b = 0; b < 5; ++b) {
      const bool ok = !(b == 4 && g >= 2);
      const unsigned a0 = pk_bf16(acc[b][0], acc[b][1]), a1 = pk_bf16(acc[b][2], acc[b][3]);
      const unsigned h0 = pk_bf16(bf_lo(xres[b][0]) + bf_lo(a0), bf_hi(xres[b][0]) + bf_hi(a0));
      const unsigned h1 = pk_bf16(bf_lo(xres[b][1]) + bf_lo(a1), bf_hi(xres[b][1]) + bf_hi(a1));
      hv[b][0] = bf_lo(h0); hv[b][1] = bf_hi(h0); hv[b][2] = bf_lo(h1); hv[b][3] = bf_hi(h1);
      if (ok) {
        *(u32x2*)(ws + WS_H + fm32_piece(tok, nt * 72 + b * 16 + g * 4, 36)) = (u32x2){h0, h1};
        sum += (hv[b][0] + hv[b][1]) + (hv[b][2] + hv[b][3]);
      }
    }
    sum += __shfl_xor(sum, 16); sum += __shfl_xor(sum, 32);
    const float mean = sum * (1.0f / 72.0f);
    float q = 0.f;
#pragma unroll
    for (int b = 0; b < 5; ++b) {
      const bool ok = !(b == 4 && g >= 2);
      float qq = 0.f;
#pragma unroll
      for (int e = 0; e < 4; ++e) { const float d = hv[b][e] - mean; qq = fmaf(d, d, qq); }
      q += ok ? qq : 0.f;
    }
    q += __shfl_xor(q, 16); q += __shfl_xor(q, 32);
    if (g == 0) *(f32x2*)(ws + WS_ST + ((size_t)tok * 8 + nt) * 8) = (f32x2){mean, q};
    BAR();
  } else {
    const int t = tid - 256, rg = t / 9, c9 = t - rg * 9;
    const int mat = xcd & 1, f0 = (xcd >> 1) * 1152 + jj * 36;
    const u16* wsrc = mat ? wu : wg;
    float s0 = 0.f, s1 = 0.f, s2 = 0.f, s3 = 0.f;
    const int rgc = rg < 28 ? rg : 27;
    const char* p = (const char*)wsrc + ((size_t)rgc * F_ + f0 + c9 * 4) * 2;
    u32x2 v[21];
#pragma unroll
    for (int i = 0; i < 21; ++i) { const int k = rgc + 28 * i; v[i] = *(const u32x2*)(p + (size_t)(k < D_ ? i : 0) * 28 * F_ * 2); }
    asm volatile("" ::: "memory");
    BAR(); BAR(); BAR();
    asm volatile("" ::: "memory");
#pragma unroll
    for (int i = 0; i < 21; ++i) { if (rgc + 28 * i < D_) { s0 += bf_lo(v[i][0]); s1 += bf_hi(v[i][0]); s2 += bf_lo(v[i][1]); s3 += bf_hi(v[i][1]); } }
    if (t < 252) *(LAS f32x4*)(lds + KB_CSOFF + (rg * 36 + c9 * 4) * 4) = (f32x4){s0, s1, s2, s3};
    WAITL0();
    BAR();
    asm volatile("" ::: "memory");
    if (t < 36) {
      float s = 0.f;
#pragma unroll
      for (int i = 0; i < 28; ++i) s += *(LAS float*)(lds + KB_CSOFF + (i * 36 + t) * 4);
      *(float*)(ws + WS_CS + ((size_t)mat * F_ + f0 + t) * 4) = s;
    }
  }
}

constexpr int KC_UPOFF = 16384 + 9216 + 128;
constexpr int KC_STAGE = 35072;
constexpr int KC_STOFF = 4 * KC_STAGE;
constexpr int KC_CSOFF = KC_STOFF + 16384;
constexpr int KC_RSOFF = KC_CSOFF + 2048;
constexpr int KC_LDS = KC_RSOFF + 2048;

__global__ __launch_bounds__(768, 1) void kC(const u16* __restrict__ wg, const u16* __restrict__ wu, char* __restrict__ ws) {
  extern __shared__ __attribute__((aligned(16))) char smem[];
  LAS char* lds = (LAS char*)smem;
  const int tid = threadIdx.x, lane = tid & 63, w = __builtin_amdgcn_readfirstlane(tid >> 6);
  const int bx = blockIdx.x, xcd = bx & 7, jj = bx >> 3;
  const int pm = (xcd & 1) * 4 + (jj & 3), pn = (xcd >> 1) * 8 + (jj >> 2);
  const int wm = w & 3, wn = w >> 2;
  const int hh = lane >> 5;

#pragma unroll
  for (int i = 0; i < 2; ++i) {
    const int sl = w * 2 + i;
    if (sl < 16) glds16(ws + WS_ST + (size_t)pm * 16384 + sl * 1024 + lane * 16, lds + KC_STOFF + sl * 1024);
    else { const int m = (sl - 16) & 1; const int l2 = lane < 36 ? lane : 35;
      glds16(ws + WS_CS + ((size_t)m * F_ + pn * 144) * 4 + l2 * 16, lds + KC_CSOFF + m * 1024); }
  }
  const char* src[3]; unsigned dst[3]; unsigned step[3];
#pragma unroll
  for (int i = 0; i < 3; ++i) {
    int sl = w * 3 + i; sl = sl > 33 ? 33 : sl;
    if (sl < 16) {
      src[i] = ws + WS_H + (size_t)((pm * 8 + (sl >> 1)) * 36 + (sl & 1)) * 1024 + lane * 16;
      dst[i] = sl * 1024; step[i] = 2048;
    } else {
      const int c = sl - 16, m = c >= 9 ? 1 : 0, cc = c - 9 * m, q = cc * 64 + lane, rr = q / 18, ch = q - rr * 18;
      src[i] = (const char*)(m ? wu : wg) + ((size_t)rr * F_ + pn * 144) * 2 + ch * 16;
      dst[i] = (m ? KC_UPOFF : 16384) + cc * 1024; step[i] = 32 * F_ * 2;
    }
  }
#define KC_ISSUE(st) do { _Pragma("unroll") for (int _i = 0; _i < 3; ++_i) { glds16(src[_i], lds + ((st) & 3) * KC_STAGE + dst[_i]); src[_i] += step[_i]; } } while (0)
  KC_ISSUE(0); KC_ISSUE(1); KC_ISSUE(2);

  f32x16 acc[3][2];
#pragma unroll
  for (int a = 0; a < 3; ++a)
#pragma unroll
    for (int b = 0; b < 2; ++b)
#pragma unroll
      for (int i = 0; i < 16; ++i) acc[a][b][i] = 0.f;
  const unsigned xadr = (unsigned)(wm * 4096 + lane * 16);
  const unsigned wadr = (unsigned)((((lane >> 4) & 1) ? KC_UPOFF : 16384) + (4 * hh + ((lane & 15) >> 2)) * 288 + wn * 96 + (lane & 3) * 8);
  bf16x8 xfA[2], wfA[3], xfB[2], wfB[3];
#define KC_READ(XF, WF, st, kk) do { LAS char* _b = lds + ((st) & 3) * KC_STAGE; \
    _Pragma("unroll") for (int _f = 0; _f < 3; ++_f) WF[_f] = cat8(tr_read(_b + wadr + (kk) * 4608 + _f * 32), tr_read(_b + wadr + (kk) * 4608 + 2304 + _f * 32)); \
    _Pragma("unroll") for (int _c = 0; _c < 2; ++_c) XF[_c] = *(LAS bf16x8*)(_b + xadr + _c * 2048 + (kk) * 1024); } while (0)
#define KC_MMA(XF, WF) do { _Pragma("unroll") for (int _f = 0; _f < 3; ++_f) _Pragma("unroll") for (int _c = 0; _c < 2; ++_c) acc[_f][_c] = MFMA32(WF[_f], XF[_c], acc[_f][_c]); } while (0)

  WAITV(6); BAR(); asm volatile("" ::: "memory");
  KC_ISSUE(3);
  KC_READ(xfA, wfA, 0, 0);
  KC_READ(xfB, wfB, 0, 1);
  SCHED0();
  KC_MMA(xfA, wfA);
  SCHED0();
  if (tid < 256) {
    const LAS f32x4* sp = (const LAS f32x4*)(lds + KC_STOFF + tid * 64);
    const f32x4 a = sp[0], b = sp[1], c = sp[2], d = sp[3];
    const float mean = ((a[0] + a[2]) + (b[0] + b[2]) + (c[0] + c[2]) + (d[0] + d[2])) * 0.125f;
    float m2 = (a[1] + a[3]) + (b[1] + b[3]) + (c[1] + c[3]) + (d[1] + d[3]);
    float dd = 0.f;
    { float e;
      e = a[0] - mean; dd = fmaf(e, e, dd); e = a[2] - mean; dd = fmaf(e, e, dd); e = b[0] - mean; dd = fmaf(e, e, dd); e = b[2] - mean; dd = fmaf(e, e, dd);
      e = c[0] - mean; dd = fmaf(e, e, dd); e = c[2] - mean; dd = fmaf(e, e, dd); e = d[0] - mean; dd = fmaf(e, e, dd); e = d[2] - mean; dd = fmaf(e, e, dd); }
    m2 = fmaf(72.0f, dd, m2);
    const float var = rbf(m2 * (1.0f / D_));
    const float sd = rbf(sqrtf(rbf(var + 1e-5f)));
    *(LAS f32x2*)(lds + KC_RSOFF + tid * 8) = (f32x2){rbf(mean), 1.0f / sd};
  }
#pragma unroll 1
  for (int t = 1; t < 18; ++t) {
    if (t <= 15) WAITVL(6); else if (t == 16) WAITVL(3); else WAITVL(0);
    BAR(); asm volatile("" ::: "memory");
    if (t + 3 < 18) KC_ISSUE(t + 3);
    KC_READ(xfA, wfA, t, 0);
    SCHED0();
    KC_MMA(xfB, wfB);
    SCHED0();
    KC_READ(xfB, wfB, t, 1);
    SCHED0();
    KC_MMA(xfA, wfA);
    SCHED0();
  }
  KC_MMA(xfB, wfB);

#pragma unroll
  for (int c = 0; c < 2; ++c) {
    const f32x2 st = *(LAS f32x2*)(lds + KC_RSOFF + (wm * 64 + c * 32 + (lane & 31)) * 8);
    const float mneg = -st[0], rs = st[1];
    const float k0 = -2.3022081981f * rs;
    const float k1 = -0.1029432397f * rs * rs * rs;
    const float k2 = rs * rs;
#pragma unroll
    for (int f = 0; f < 3; ++f) {
      const LAS char* cp = lds + KC_CSOFF + ((wn * 3 + f) * 16 + 4 * hh) * 4;
      const f32x4 g0 = *(const LAS f32x4*)(cp), g1 = *(const LAS f32x4*)(cp + 32), u0 = *(const LAS f32x4*)(cp + 1024), u1 = *(const LAS f32x4*)(cp + 1024 + 32);
      float o[8];
#pragma unroll
      for (int j = 0; j < 8; ++j) {
        const float cg = j < 4 ? g0[j & 3] : g1[j & 3], cu = j < 4 ? u0[j & 3] : u1[j & 3];
        const float a = fmaf(mneg, cg, acc[f][c][j]), b = fmaf(mneg, cu, acc[f][c][j + 8]);
        const float e = __builtin_amdgcn_exp2f(a * fmaf(a * a, k1, k0));
        o[j] = (a * b) * k2 * __builtin_amdgcn_rcpf(1.0f + e);
      }
      const u32x4 v = {pk_bf16(o[0], o[1]), pk_bf16(o[2], o[3]), pk_bf16(o[4], o[5]), pk_bf16(o[6], o[7])};
      *(u32x4*)(ws + WS_ACT + (size_t)((pm * 8 + wm * 2 + c) * 288 + pn * 9 + wn * 3 + f) * 1024 + lane * 16) = v;
    }
  }
#undef KC_ISSUE
#undef KC_READ
#undef KC_MMA
}

constexpr int KD_STAGE = 16384 + 18432;
constexpr int KD_LDS = 2 * 72 * 1024;

__global__ __launch_bounds__(512, 1) void kD(const u16* __restrict__ wd, char* __restrict__ ws, u16* __restrict__ out) {
  extern __shared__ __attribute__((aligned(16))) char smem[];
  LAS char* lds = (LAS char*)smem;
  const int tid = threadIdx.x, lane = tid & 63, w = __builtin_amdgcn_readfirstlane(tid >> 6);
  const int bx = blockIdx.x, xcd = bx & 7, jj = bx >> 3;
  const int ks = xcd >> 1, mt = (xcd & 1) * 8 + (jj & 7), nt = jj >> 3;
  const int gk = w >> 2, wm = w & 1, wn = ((w >> 1) & 1) ^ gk;
  const int g = lane >> 4, r = lane & 15;

  const char* src[5]; unsigned dst[5]; unsigned step[5];
#pragma unroll
  for (int i = 0; i < 5; ++i) {
    int sl = i * 8 + w; sl = sl > 33 ? sl - 8 : sl;
    if (sl < 16) {
      src[i] = ws + WS_ACT + (size_t)((mt * 4 + (sl >> 2)) * 288 + ks * 72 + (sl & 3)) * 1024 + lane * 16;
      dst[i] = sl * 1024; step[i] = 4096;
    } else {
      const int cc = sl - 16, q = cc * 64 + lane, rr = q / 18, ch = q - rr * 18;
      src[i] = (const char*)wd + ((size_t)(ks * 1152 + rr) * D_ + nt * 144) * 2 + ch * 16;
      dst[i] = 16384 + cc * 1024; step[i] = 64 * D_ * 2;
    }
  }
#define KD_ISSUE(st) do { _Pragma("unroll") for (int _i = 0; _i < 5; ++_i) { glds16(src[_i], lds + ((st) & 3) * KD_STAGE + dst[_i]); src[_i] += step[_i]; } } while (0)
  KD_ISSUE(0); KD_ISSUE(1); KD_ISSUE(2); KD_ISSUE(3);

  f32x4 acc[4][5];
#pragma unroll
  for (int a = 0; a < 4; ++a)
#pragma unroll
    for (int b = 0; b < 5; ++b) acc[a][b] = (f32x4){0.f, 0.f, 0.f, 0.f};
  const unsigned xadr = (unsigned)(wm * 8192 + (gk * 2 + (g >> 1)) * 1024 + ((g & 1) * 32 + r) * 16);
  const unsigned wadr = (unsigned)(16384 + (gk * 32 + 16 * (g >> 1) + 4 * (g & 1) + (r >> 2)) * 288 + wn * 160 + (r & 3) * 8);
  bf16x8 xfA[4], wfA[5], xfB[4], wfB[5];
#define KD_READ(XF, WF, st) do { LAS char* _b = lds + ((st) & 3) * KD_STAGE; \
    _Pragma("unroll") for (int _f = 0; _f < 5; ++_f) { const int _o = (_f == 4 && wn) ? 96 : _f * 32; WF[_f] = cat8(tr_read(_b + wadr + _o), tr_read(_b + wadr + 2304 + _o)); } \
    _Pragma("unroll") for (int _c = 0; _c < 4; ++_c) XF[_c] = *(LAS bf16x8*)(_b + xadr + (_c >> 1) * 4096 + (_c & 1) * 256); } while (0)
#define KD_MMA(XF, WF) do { _Pragma("unroll") for (int _f = 0; _f < 4; ++_f) _Pragma("unroll") for (int _c = 0; _c < 4; ++_c) acc[_c][_f] = MFMA16(WF[_f], XF[_c], acc[_c][_f]); \
    if (wn == 0) { _Pragma("unroll") for (int _c = 0; _c < 4; ++_c) acc[_c][4] = MFMA16(WF[4], XF[_c], acc[_c][4]); } } while (0)

  WAITV(15); BAR(); asm volatile("" ::: "memory");
  KD_READ(xfA, wfA, 0);
#pragma unroll 1
  for (int t = 0; t < 18; t += 2) {
    if (t <= 14) WAITVL(10); else WAITVL(0);
    BAR(); asm volatile("" ::: "memory");
    if (t + 4 < 18) KD_ISSUE(t + 4);
    KD_READ(xfB, wfB, t + 1);
    SCHED0();
    KD_MMA(xfA, wfA);
    SCHED0();
    if (t + 2 < 18) {
      if (t <= 12) WAITVL(10); else WAITVL(5);
      BAR(); asm volatile("" ::: "memory");
      if (t + 5 < 18) KD_ISSUE(t + 5);
      KD_READ(xfA, wfA, t + 2);
    }
    SCHED0();
    KD_MMA(xfB, wfB);
    SCHED0();
  }
  WAITL0(); BAR(); asm volatile("" ::: "memory");
#pragma unroll
  for (int a = 0; a < 4; ++a)
#pragma unroll
    for (int b = 0; b < 5; ++b)
      if (b < 4 || wn == 0) *(LAS f32x4*)(lds + gk * 73728 + ((wm * 4 + a) * 9 + wn * 5 + b) * 1024 + lane * 16) = acc[a][b];
  __syncthreads();
  const int tile = mt * 4 + nt;
  const auto prs = __builtin_amdgcn_make_buffer_rsrc((void*)(ws + WS_PART), 0, (int)(64 * 4 * 3 * PART_SLAB), 0x00020000);
  for (int i = w; i < 54; i += 8) {
    const int qi = i / 18, rem = i - qi * 18, q = qi + (qi >= ks ? 1 : 0);
    const int sidx = ks - (ks > q ? 1 : 0);
    const unsigned lo = (unsigned)((q * 18 + rem) * 1024 + lane * 16);
    const f32x4 v = *(LAS f32x4*)(lds + lo) + *(LAS f32x4*)(lds + 73728 + lo);
    __builtin_amdgcn_raw_buffer_store_b128(__builtin_bit_cast(u32x4, v), prs, (int)(((tile * 4 + q) * 3 + sidx) * PART_SLAB + rem * 1024 + lane * 16), 0, 16);
  }
  WAITV(0);
  __syncthreads();
  GAS unsigned* cnt = (GAS unsigned*)(ws + WS_CNT);
  if (tid < 4 && tid != ks) __hip_atomic_fetch_add(cnt + (tile * 4 + tid) * 64, 1u, __ATOMIC_RELAXED, __HIP_MEMORY_SCOPE_AGENT);
  if (w == 0) {
    unsigned spins = 0;
    while (__builtin_amdgcn_readfirstlane(__hip_atomic_load(cnt + (tile * 4 + ks) * 64, __ATOMIC_RELAXED, __HIP_MEMORY_SCOPE_AGENT)) < 3u) {
      if (++spins > 2000000u) break;
      __builtin_amdgcn_s_sleep(1);
    }
  }
  __syncthreads();
  for (int i = w; i < 18; i += 8) {
    const unsigned lo = (unsigned)((ks * 18 + i) * 1024 + lane * 16);
    const int po = (int)((tile * 4 + ks) * 3 * PART_SLAB + i * 1024 + lane * 16);
    const u32x4 p0 = __builtin_amdgcn_raw_buffer_load_b128(prs, po, 0, 16);
    const u32x4 p1 = __builtin_amdgcn_raw_buffer_load_b128(prs, po + (int)PART_SLAB, 0, 16);
    const u32x4 p2 = __builtin_amdgcn_raw_buffer_load_b128(prs, po + 2 * (int)PART_SLAB, 0, 16);
    const int mb = i / 9, nb = i - mb * 9;
    const int tok = mt * 128 + (ks * 2 + mb) * 16 + r, col = nt * 144 + nb * 16 + g * 4;
    const u32x2 hres = *(const u32x2*)(ws + WS_H + fm32_piece(tok, col, 36));
    f32x4 v = *(LAS f32x4*)(lds + lo) + *(LAS f32x4*)(lds + 73728 + lo);
    v += __builtin_bit_cast(f32x4, p0); v += __builtin_bit_cast(f32x4, p1); v += __builtin_bit_cast(f32x4, p2);
    const unsigned a0 = pk_bf16(v[0], v[1]), a1 = pk_bf16(v[2], v[3]);
    const unsigned o0 = pk_bf16(bf_lo(hres[0]) + bf_lo(a0), bf_hi(hres[0]) + bf_hi(a0));
    const unsigned o1 = pk_bf16(bf_lo(hres[1]) + bf_lo(a1), bf_hi(hres[1]) + bf_hi(a1));
    *(u32x2*)(out + (size_t)tok * D_ + col) = (u32x2){o0, o1};
  }
#undef KD_ISSUE
#undef KD_READ
#undef KD_MMA
}

extern "C" void kernel_launch(void* const* d_in, const int* in_sizes, int n_in, void* d_out, int out_size, void* d_ws, size_t ws_size,
                              hipStream_t stream) {
  const u16* x = (const u16*)d_in[0];
  const u16* wqkv = (const u16*)d_in[1];
  const u16* wo = (const u16*)d_in[2];
  const u16* wg = (const u16*)d_in[4];
  const u16* wu = (const u16*)d_in[5];
  const u16* wd = (const u16*)d_in[6];
  char* ws = (char*)d_ws;
  (void)hipFuncSetAttribute((const void*)kA, hipFuncAttributeMaxDynamicSharedMemorySize, KA_LDS);
  (void)hipFuncSetAttribute((const void*)kB, hipFuncAttributeMaxDynamicSharedMemorySize, KB_LDS);
  (void)hipFuncSetAttribute((const void*)kC, hipFuncAttributeMaxDynamicSharedMemorySize, KC_LDS);
  (void)hipFuncSetAttribute((const void*)kD, hipFuncAttributeMaxDynamicSharedMemorySize, KD_LDS);
  kA<<<256, 576, KA_LDS, stream>>>(x, wqkv, ws);
  kB<<<256, 512, KB_LDS, stream>>>(x, wo, wg, wu, ws);
  kC<<<256, 768, KC_LDS, stream>>>(wg, wu, ws);
  kD<<<256, 512, KD_LDS, stream>>>(wd, ws, (u16*)d_out);
}
```

```cpp
#include <hip/hip_runtime.h>
#include <hip/hip_bf16.h>
#include <stdint.h>

#define LAS __attribute__((address_space(3)))
#define GAS __attribute__((address_space(1)))
#define DI __device__ __forceinline__
typedef unsigned short u16;
typedef short s16x4 __attribute__((ext_vector_type(4)));
typedef short s16x8 __attribute__((ext_vector_type(8)));
typedef __bf16 bf16x2 __attribute__((ext_vector_type(2)));
typedef __bf16 bf16x8 __attribute__((ext_vector_type(8)));
typedef float f32x2 __attribute__((ext_vector_type(2)));
typedef float f32x4 __attribute__((ext_vector_type(4)));
typedef float f32x16 __attribute__((ext_vector_type(16)));
typedef unsigned u32x2 __attribute__((ext_vector_type(2)));
typedef unsigned u32x4 __attribute__((ext_vector_type(4)));

constexpr int S_ = 2048, D_ = 576, F_ = 4608;

constexpr size_t WS_O    = 0;
constexpr size_t WS_H    = WS_O + (size_t)S_ * D_ * 2;
constexpr size_t WS_HC   = WS_H + (size_t)S_ * D_ * 2;
constexpr size_t WS_ST   = WS_HC + (size_t)S_ * D_ * 2;
constexpr size_t WS_XM   = WS_ST + (size_t)S_ * 12 * 4;
constexpr size_t WS_WM   = WS_XM + (size_t)S_ * 4;
constexpr size_t WS_CNT  = WS_WM + 8192;
constexpr size_t WS_ACT  = WS_CNT + 256 * 256;
constexpr size_t WS_PART = WS_ACT + (size_t)S_ * F_ * 2;
constexpr size_t PART_SLAB = 18 * 1024;

DI float bf_lo(unsigned v) { return __uint_as_float(v << 16); }
DI float bf_hi(unsigned v) { return __uint_as_float(v & 0xffff0000u); }
DI unsigned pk_bf16(float a, float b) { f32x2 v = {a, b}; return __builtin_bit_cast(unsigned, __builtin_convertvector(v, bf16x2)); }
DI float rbf(float a) { return bf_lo(pk_bf16(a, 0.f)); }
DI float dot2bf(unsigned a, unsigned b, float c) { return __builtin_amdgcn_fdot2_f32_bf16(__builtin_bit_cast(bf16x2, a), __builtin_bit_cast(bf16x2, b), c, false); }
DI s16x4 tr_read(LAS char* p) { return __builtin_amdgcn_ds_read_tr16_b64_v4i16((LAS s16x4*)p); }
DI bf16x8 cat8(s16x4 a, s16x4 b) { return __builtin_bit_cast(bf16x8, __builtin_shufflevector(a, b, 0, 1, 2, 3, 4, 5, 6, 7)); }
DI void glds16(const void* g, LAS char* l) {
  unsigned keep; const unsigned la = __builtin_amdgcn_readfirstlane((unsigned)(size_t)l);
  asm volatile("s_mov_b32 %0, m0\n\ts_mov_b32 m0, %2\n\ts_nop 0\n\tglobal_load_lds_dwordx4 %1, off\n\ts_mov_b32 m0, %0" : "=&s"(keep) : "v"(g), "s"(la) : "memory");
}
DI u32x4 tr_frag(LAS char* p, int second) {
  const u32x2 a = __builtin_bit_cast(u32x2, tr_read(p)), b = __builtin_bit_cast(u32x2, tr_read(p + second));
  return (u32x4){a[0], a[1], b[0], b[1]};
}
DI u32x4 tr_frag2(LAS char* p0, LAS char* p1) {
  const u32x2 a = __builtin_bit_cast(u32x2, tr_read(p0)), b = __builtin_bit_cast(u32x2, tr_read(p1));
  return (u32x4){a[0], a[1], b[0], b[1]};
}
#define BF8(v) __builtin_bit_cast(bf16x8, (v))
DI void touch4(const void* g, LAS char* l) {
  unsigned keep; const unsigned la = __builtin_amdgcn_readfirstlane((unsigned)(size_t)l);
  asm volatile("s_mov_b32 %0, m0\n\ts_mov_b32 m0, %2\n\ts_nop 0\n\tglobal_load_lds_dword %1, off\n\ts_mov_b32 m0, %0" : "=&s"(keep) : "v"(g), "s"(la) : "memory");
}
#define MFMA16(a, b, c) __builtin_amdgcn_mfma_f32_16x16x32_bf16((a), (b), (c), 0, 0, 0)
#define MFMA32(a, b, c) __builtin_amdgcn_mfma_f32_32x32x16_bf16((a), (b), (c), 0, 0, 0)
#define BAR() __builtin_amdgcn_s_barrier()
#define SCHED0() __builtin_amdgcn_sched_barrier(0)
#define WAITV(n) asm volatile("s_waitcnt vmcnt(" #n ")" ::: "memory")
#define WAITVL(n) asm volatile("s_waitcnt vmcnt(" #n ") lgkmcnt(0)" ::: "memory")
#define WAITL0() asm volatile("s_waitcnt lgkmcnt(0)" ::: "memory")

#define STAMP_DECL
#define STAMP(i) do {} while (0)
#define STAMP_OUT(kid) do {} while (0)
DI unsigned fm32_piece(int tok, int c4, int KB16) {
  const int pi = (c4 & 15) >> 2;
  return (unsigned)(((tok >> 5) * KB16 + (c4 >> 4)) * 1024 + (((pi & 1) * 32 + (tok & 31)) * 16) + (pi >> 1) * 8);
}

constexpr int KA_WROW = 240;
constexpr int KA_XB = 144 * 128;
constexpr int KA_STAGE = KA_XB + 64 * KA_WROW;
constexpr int KA_ONES = 4 * KA_STAGE;
constexpr int KA_SQ = KA_ONES + 64;
constexpr int KA_SUM = KA_SQ + 160 * 4;
constexpr int KA_CST = KA_SUM + 160 * 4;
constexpr int KA_SCR = KA_CST + 128 * 4;
constexpr int KA_LDS = KA_SCR + 256;
constexpr int KA_QROW = 304;
constexpr int KA_OOFF = 144 * KA_QROW;

template <int NT, int NG>
DI void kA_loop(LAS char* lds, const char* (&src)[5], int w, const unsigned (&xad)[3], const unsigned (&xmul)[3], unsigned wad, unsigned wmul,
                f32x16 (&acc)[3], float& sq) {
#define KA_G(i, st) do { glds16(src[i], lds + ((st) & 3) * KA_STAGE + ((i) * 8 + w) * 1024); src[i] += ((i) * 8 + w < 18) ? 128 : 64 * 3456; } while (0)
#define KA_G4(st) do { if (NG == 5) { glds16(src[4], lds + ((st) & 3) * KA_STAGE + 32 * 1024); src[4] += 64 * 3456; } } while (0)
  u32x4 xP[2][3], wP[2], xQ[2][3], wQ[2];
  unsigned xb[3], wb;
#define KA_BASE(st) do { const unsigned _s = (unsigned)(((st) & 3) * KA_STAGE); \
    _Pragma("unroll") for (int _i = 0; _i < NT; ++_i) xb[_i] = xad[_i] + (_s & xmul[_i]); wb = wad + (_s & wmul); } while (0)
#define KA_RX(XF, i, kk) XF[i] = *(LAS u32x4*)(lds + (xb[i] ^ (((kk) * 32) & xmul[i])))
#define KA_SQ2(a, b) do { sq = dot2bf(a, a, sq); sq = dot2bf(b, b, sq); } while (0)
#define KA_STEP(XC, WC, XN, WN, kk, G0) do { \
    acc[0] = MFMA32(BF8(WC), BF8(XC[0]), acc[0]); SCHED0(); WN = tr_frag2(lds + wb + (((kk) * (16 * KA_WROW)) & wmul), lds + wb + (((kk) * (16 * KA_WROW) + KA_WROW) & wmul)); KA_RX(XN, 0, kk); \
    KA_SQ2(XC[0][0], XC[0][1]); SCHED0(); \
    acc[1] = MFMA32(BF8(WC), BF8(XC[1]), acc[1]); SCHED0(); KA_RX(XN, 1, kk); G0; \
    KA_SQ2(XC[0][2], XC[0][3]); SCHED0(); \
    if (NT == 3) { acc[2] = MFMA32(BF8(WC), BF8(XC[2]), acc[2]); SCHED0(); KA_RX(XN, 2, kk); SCHED0(); } } while (0)
#define KA_HALF(XC, WC, XN, WN, hk, G0, G1) do { KA_STEP(XC[0], WC[0], XN[0], WN[0], 2 * (hk), G0); KA_STEP(XC[1], WC[1], XN[1], WN[1], 2 * (hk) + 1, G1); } while (0)
  KA_BASE(0);
#pragma unroll
  for (int kk = 0; kk < 2; ++kk) {
    wP[kk] = tr_frag2(lds + wb + ((kk * (16 * KA_WROW)) & wmul), lds + wb + ((kk * (16 * KA_WROW) + KA_WROW) & wmul));
#pragma unroll
    for (int i = 0; i < NT; ++i) KA_RX(xP[kk], i, kk);
  }
  KA_G(0, 3); KA_G(1, 3); KA_G(2, 3); KA_G(3, 3); KA_G4(3);
  KA_HALF(xP, wP, xQ, wQ, 1, (void)0, (void)0);
#pragma unroll 1
  for (int t = 1; t < 6; ++t) {
    if (NG == 5) WAITVL(10); else WAITVL(8);
    BAR(); asm volatile("" ::: "memory");
    KA_BASE(t);
    KA_HALF(xQ, wQ, xP, wP, 0, do { KA_G(0, t + 3); KA_G4(t + 3); } while (0), KA_G(1, t + 3));
    KA_HALF(xP, wP, xQ, wQ, 1, KA_G(2, t + 3), KA_G(3, t + 3));
  }
#pragma unroll 1
  for (int t = 6; t < 9; ++t) {
    if (t == 6) { if (NG == 5) WAITVL(10); else WAITVL(8); } else if (t == 7) { if (NG == 5) WAITVL(5); else WAITVL(4); } else WAITVL(0);
    BAR(); asm volatile("" ::: "memory");
    KA_BASE(t);
    KA_HALF(xQ, wQ, xP, wP, 0, (void)0, (void)0);
    KA_HALF(xP, wP, xQ, wQ, 1, (void)0, (void)0);
  }
#pragma unroll
  for (int kk = 0; kk < 2; ++kk) {
#pragma unroll
    for (int i = 0; i < NT; ++i) acc[i] = MFMA32(BF8(wQ[kk]), BF8(xQ[kk][i]), acc[i]);
#pragma unroll
    for (int e = 0; e < 4; ++e) sq = dot2bf(xQ[kk][0][e], xQ[kk][0][e], sq);
  }
}

__global__ __launch_bounds__(576, 1) void kA(const u16* __restrict__ x, const u16* __restrict__ wqkv, const u16* __restrict__ wo,
                                             const u16* __restrict__ wg, const u16* __restrict__ wu, char* __restrict__ ws) {
  extern __shared__ __attribute__((aligned(16))) char smem[];
  LAS char* lds = (LAS char*)smem;
  const int tid = threadIdx.x, lane = tid & 63, w = __builtin_amdgcn_readfirstlane(tid >> 6);
  const int bx = blockIdx.x, xcd = bx & 7, jj = bx >> 3;
  const int mt = (xcd >> 1) * 4 + (jj & 3), hp = (xcd & 1) * 8 + (jj >> 2);
  STAMP_DECL STAMP(0);
  if (w == 8) {
    if (lane < 4) *(LAS unsigned*)(lds + KA_ONES + lane * 4) = 0x3f803f80u;
#pragma unroll
    for (int i = 0; i < 3; ++i) {
      int ln = i * 64 + lane; ln = ln < 162 ? ln : 161;
      const int L = ln * 8 + (jj >> 2);
      int row = mt * 128 - 16 + L % 144; row = row < 0 ? 0 : row;
      touch4((const char*)x + (size_t)row * (D_ * 2) + (L / 144) * 128, lds + KA_SCR);
    }
#pragma unroll
    for (int i = 0; i < 5; ++i) {
      int ln = i * 64 + lane; ln = ln < 270 ? ln : 269;
      const int kr = (ln / 15) * 32 + jj, sl = ln % 15, seg = sl / 5, l5 = sl % 5;
      touch4((const char*)wqkv + (size_t)kr * 3456 + seg * 1152 + ((xcd & 1) * 4 + l5) * 128, lds + KA_SCR);
    }
    WAITL0();
    BAR(); BAR(); BAR(); BAR(); BAR(); BAR(); BAR();
    u32x4 wv[3][2];
#pragma unroll
    for (int i = 0; i < 3; ++i) {
      const int row = bx + 256 * i < D_ ? bx + 256 * i : bx;
      const char* rp = (const char*)wo + (size_t)row * (D_ * 2);
      wv[i][0] = *(const u32x4*)(rp + lane * 16);
      wv[i][1] = *(const u32x4*)(rp + 1024 + (lane & 7) * 16);
    }
    asm volatile("" ::: "memory");
#pragma unroll
    for (int i = 0; i < 3; ++i) {
      int ln = i * 64 + lane; ln = ln < 162 ? ln : 161;
      touch4((const char*)wo + (size_t)(jj * 162 + ln) * 128, lds + KA_SCR);
    }
#pragma unroll
    for (int i = 0; i < 6; ++i) {
      int ln = i * 64 + lane; ln = ln < 324 ? ln : 323;
      const int kr = (ln / 18) * 32 + jj;
      touch4((const char*)((xcd & 1) ? wu : wg) + (size_t)kr * (F_ * 2) + (xcd >> 1) * 2304 + (ln % 18) * 128, lds + KA_SCR);
    }
    BAR(); BAR(); BAR(); BAR();
    asm volatile("" : "+v"(wv[0][0]), "+v"(wv[0][1]), "+v"(wv[1][0]), "+v"(wv[1][1]), "+v"(wv[2][0]), "+v"(wv[2][1]) :: "memory");
#pragma unroll
    for (int i = 0; i < 3; ++i) {
      float t = 0.f, u = 0.f;
#pragma unroll
      for (int e = 0; e < 4; ++e) { t = dot2bf(wv[i][0][e], 0x3f803f80u, t); u = dot2bf(wv[i][1][e], 0x3f803f80u, u); }
      t += lane < 8 ? u : 0.f;
#pragma unroll
      for (int o = 32; o; o >>= 1) t += __shfl_xor(t, o);
      t *= (1.0f / D_);
      const float hi = rbf(t);
      if (lane == 0 && bx + 256 * i < D_) *(u32x2*)(ws + WS_WM + (size_t)(bx + 256 * i) * 8) = (u32x2){pk_bf16(hi, t - hi), 0u};
    }
    asm volatile("" ::: "memory");
    BAR();
    STAMP(9); STAMP_OUT(0);
    return;
  }
  const char* src[5];
#pragma unroll
  for (int i = 0; i < 3; ++i) {
    if (i < 2 || w < 2) {
      const int lr = (i * 8 + w) * 8 + (lane >> 3);
      int row = mt * 128 - 16 + lr; row = row < 0 ? 0 : row;
      src[i] = (const char*)x + (size_t)row * (D_ * 2) + (((lane & 7) ^ ((lr >> 1) & 7)) * 16);
      if (i < 2) KA_G(i, 0);
    }
  }
  {
    const char* wbase = (const char*)wqkv + ((72 * hp) & ~15);
#pragma unroll
    for (int i = 2; i < 5; ++i) {
      if (i > 2 || w >= 2) {
        const int sl = i < 4 ? i * 8 + w : 32;
        const int P = (sl - 18) * 64 + lane, pr = (P * 4370) >> 16, ch = P - pr * 15, seg = (ch * 13) >> 6, c5 = ch - seg * 5;
        const int p16 = pr & 15, k = (pr & ~15) + 4 * (p16 & 3) + (p16 >> 2);
        src[i] = wbase + (unsigned)(k * 3456 + seg * 1152 + c5 * 16);
      }
    }
  }
  const bool five = (w == 7);
  KA_G(2, 0); KA_G(3, 0); if (five) { glds16(src[4], lds + 32 * 1024); src[4] += 64 * 3456; }
#pragma unroll
  for (int st = 1; st < 3; ++st) { KA_G(0, st); KA_G(1, st); KA_G(2, st); KA_G(3, st); if (five) { glds16(src[4], lds + st * KA_STAGE + 32 * 1024); src[4] += 64 * 3456; } }
  STAMP(1);
  if (bx == 0 && tid < 256) *(unsigned*)(ws + WS_CNT + (size_t)tid * 256) = 0u;
  const int cb = w & 3, tg = w >> 2, hh = lane >> 5, r32 = lane & 31;
  int tl[3];
  unsigned xad[3], xmul[3];
#pragma unroll
  for (int i = 0; i < 3; ++i) {
    tl[i] = tg ? 3 + ((i + cb) & 1) : (i + cb) % 3;
    const int lr = tl[i] ? tl[i] * 32 - 16 + r32 : (r32 & 15);
    const bool one = tl[i] == 0 && r32 == 0;
    xad[i] = one ? (unsigned)KA_ONES : (unsigned)(lr * 128 + ((hh ^ ((lr >> 1) & 7)) * 16));
    xmul[i] = one ? 0u : 0xffffffffu;
  }
  unsigned wad, wmul;
  {
    const int pc = 8 * cb + 4 * ((lane >> 4) & 1) + (lane & 3), q = (lane & 15) >> 2;
    const int seg = (pc * 57) >> 9, wi = pc - seg * 9;
    const bool one = pc > 26;
    wad = one ? (unsigned)KA_ONES : (unsigned)(KA_XB + (4 * q + 2 * hh) * KA_WROW + seg * 80 + ((72 * hp) & 15) + wi * 8);
    wmul = one ? 0u : 0xffffffffu;
  }
  unsigned sto[4][2];
#pragma unroll
  for (int G = 0; G < 4; ++G) {
    const int pd = 8 * cb + 2 * G + hh, sg = (pd * 57) >> 9, c0 = (pd - sg * 9) * 4;
#pragma unroll
    for (int e = 0; e < 2; ++e) {
      const int c = c0 + 2 * e, hd = c >= 18 ? 1 : 0, d = c - 18 * hd;
      sto[G][e] = (unsigned)(pd < 27 ? (sg * 2 + hd) * 48 + d * 2 : 288 + 4 * e);
    }
  }
  f32x16 acc[3];
#pragma unroll
  for (int a = 0; a < 3; ++a)
#pragma unroll
    for (int i = 0; i < 16; ++i) acc[a][i] = 0.f;
  float sq = 0.f;
  if (five) WAITVL(10); else WAITVL(8);
  BAR(); asm volatile("" ::: "memory");
  STAMP(2);
  if (tg == 0) kA_loop<3, 4>(lds, src, w, xad, xmul, wad, wmul, acc, sq);
  else if (five) kA_loop<2, 5>(lds, src, w, xad, xmul, wad, wmul, acc, sq);
  else kA_loop<2, 4>(lds, src, w, xad, xmul, wad, wmul, acc, sq);
  STAMP(3);
  {
    sq += __shfl_xor(sq, 32);
    if (hh == 0) *(LAS float*)(lds + KA_SQ + (tl[0] * 32 + r32) * 4) = sq;
#pragma unroll
    for (int i = 0; i < 3; ++i) {
      if (i == 2 && tg) break;
      if (cb == 3 && hh == 0) *(LAS float*)(lds + KA_SUM + (tl[i] * 32 + r32) * 4) = acc[i][8];
      if (tl[i] == 0 && r32 == 0) {
#pragma unroll
        for (int G = 0; G < 4; ++G) *(LAS f32x4*)(lds + KA_CST + (cb * 32 + G * 8 + hh * 4) * 4) = (f32x4){acc[i][4 * G], acc[i][4 * G + 1], acc[i][4 * G + 2], acc[i][4 * G + 3]};
      }
    }
  }
  WAITL0();
  BAR(); asm volatile("" ::: "memory");
  STAMP(4);
  {
    f32x4 cs[4];
#pragma unroll
    for (int G = 0; G < 4; ++G) cs[G] = *(LAS f32x4*)(lds + KA_CST + (cb * 32 + G * 8 + hh * 4) * 4);
#pragma unroll
    for (int i = 0; i < 3; ++i) {
      if (i == 2 && tg) break;
      const float sm = *(LAS float*)(lds + KA_SUM + (tl[i] * 32 + r32) * 4), s2 = *(LAS float*)(lds + KA_SQ + (tl[i] * 32 + r32) * 4);
      const float mean = sm * (1.0f / D_);
      const float var = rbf(fmaxf(s2 * (1.0f / D_) - mean * mean, 0.f));
      const float rs = __builtin_amdgcn_rcpf(rbf(__builtin_amdgcn_sqrtf(rbf(var + 1e-5f))));
      const float mrs = -rbf(mean) * rs;
      if (hp == 0 && cb == 0 && hh == 0 && tl[i]) *(float*)(ws + WS_XM + (size_t)(mt * 128 + tl[i] * 32 - 32 + r32) * 4) = mean;
      const int lr = tl[i] ? tl[i] * 32 - 16 + r32 : (r32 & 15);
      LAS char* rowp = lds + lr * KA_QROW;
#pragma unroll
      for (int G = 0; G < 4; ++G) {
        const f32x4 a = {acc[i][4 * G], acc[i][4 * G + 1], acc[i][4 * G + 2], acc[i][4 * G + 3]};
        const f32x4 v = a * rs + cs[G] * mrs;
        *(LAS unsigned*)(rowp + sto[G][0]) = pk_bf16(v[0], v[1]); *(LAS unsigned*)(rowp + sto[G][1]) = pk_bf16(v[2], v[3]);
      }
    }
  }
  WAITL0();
  BAR(); asm volatile("" ::: "memory");
  STAMP(5);
  {
    const int hf = tid & 1, t = (tid >> 1) & 127, hd = tid >> 8, T = mt * 128 + t;
    LAS char* base = lds + (t + 16) * KA_QROW + hd * 48;
    const u32x4 qa = *(LAS u32x4*)(base), qb = *(LAS u32x4*)(base + 16); const unsigned qc = *(LAS unsigned*)(base + 32);
    float lg[8];
#pragma unroll
    for (int i = 0; i < 4; ++i) {
      const int wv = hf * 4 + i;
      LAS char* kp = base - wv * 2 * KA_QROW + 96;
      const u32x4 a = *(LAS u32x4*)(kp), b = *(LAS u32x4*)(kp + 16); const unsigned c = *(LAS unsigned*)(kp + 32);
      float s = 0.f;
      s = dot2bf(qa[0], a[0], s); s = dot2bf(qa[1], a[1], s); s = dot2bf(qa[2], a[2], s); s = dot2bf(qa[3], a[3], s);
      s = dot2bf(qb[0], b[0], s); s = dot2bf(qb[1], b[1], s); s = dot2bf(qb[2], b[2], s); s = dot2bf(qb[3], b[3], s);
      s = dot2bf(qc, c, s);
      s = rbf(rbf(s) * 0.23570226039551584f);
      s = (T - 2 * wv >= 0) ? s : -1.0e9f;
      const float o = __builtin_bit_cast(float, __builtin_amdgcn_mov_dpp(__builtin_bit_cast(int, s), 0xB1, 0xF, 0xF, true));
      lg[i] = hf ? o : s; lg[4 + i] = hf ? s : o;
    }
    const float mx = fmaxf(fmaxf(fmaxf(lg[0], lg[1]), fmaxf(lg[2], lg[3])), fmaxf(fmaxf(lg[4], lg[5]), fmaxf(lg[6], lg[7])));
    float sm = 0.f;
#pragma unroll
    for (int wv = 0; wv < 8; ++wv) { lg[wv] = __builtin_amdgcn_exp2f((lg[wv] - mx) * 1.4426950408889634f); sm += lg[wv]; }
    const float inv = 1.0f / sm;
    float o[10];
#pragma unroll
    for (int d = 0; d < 10; ++d) o[d] = 0.f;
#pragma unroll
    for (int wp = 0; wp < 4; ++wp) {
      const unsigned pp = pk_bf16(lg[2 * wp] * inv, lg[2 * wp + 1] * inv);
      LAS char* v0 = base - (2 * wp) * 2 * KA_QROW + 192 + hf * 16;
      LAS char* v1 = v0 - 2 * KA_QROW;
      const u32x4 a = *(LAS u32x4*)(v0), b = *(LAS u32x4*)(v1); const unsigned a4 = *(LAS unsigned*)(v0 + 16), b4 = *(LAS unsigned*)(v1 + 16);
      const unsigned av[5] = {a[0], a[1], a[2], a[3], a4}, bv[5] = {b[0], b[1], b[2], b[3], b4};
#pragma unroll
      for (int i = 0; i < 5; ++i) {
        o[2 * i]     = dot2bf(pp, __builtin_amdgcn_perm(bv[i], av[i], 0x05040100u), o[2 * i]);
        o[2 * i + 1] = dot2bf(pp, __builtin_amdgcn_perm(bv[i], av[i], 0x07060302u), o[2 * i + 1]);
      }
    }
    LAS char* op = lds + KA_OOFF + t * 80 + hd * 36 + hf * 16;
#pragma unroll
    for (int i = 0; i < 5; ++i) *(LAS unsigned*)(op + 4 * i) = pk_bf16(o[2 * i], o[2 * i + 1]);
  }
  WAITL0();
  BAR(); asm volatile("" ::: "memory");
  STAMP(8);
#pragma unroll
  for (int i = 0; i < 3; ++i) {
    const int id = tid + i * 512;
    if (id < 1152) {
      const int pc = id >> 7, t = id & 127;
      const u32x2 v = *(LAS u32x2*)(lds + KA_OOFF + t * 80 + pc * 8);
      *(u32x2*)(ws + WS_O + fm32_piece(mt * 128 + t, hp * 36 + pc * 4, 36)) = v;
    }
  }
  STAMP(9); STAMP_OUT(0);
}

constexpr int KB_WM = 576 * 128;
constexpr int KB_RED = KB_WM + 5120;
constexpr int KB_SCR = KB_RED + 25600;
constexpr int KB_LDS = KB_SCR + 256;

__global__ __launch_bounds__(768, 1) void kB(const u16* __restrict__ x, const u16* __restrict__ wo, const u16* __restrict__ wg,
                                             const u16* __restrict__ wu, char* __restrict__ ws) {
  extern __shared__ __attribute__((aligned(16))) char smem[];
  LAS char* lds = (LAS char*)smem;
  const int tid = threadIdx.x, lane = tid & 63, w = __builtin_amdgcn_readfirstlane(tid >> 6);
  const int bx = blockIdx.x, xcd = bx & 7, jj = bx >> 3;
  const int unit = xcd * 30 + jj;
  STAMP_DECL STAMP(0);
  if (jj >= 30 || unit >= 234) {
    return;
  }
  const int mt = (unit * 7282) >> 16, nt = unit - mt * 9;
  const int g = lane >> 4, r = lane & 15;
  if (w >= 10) {
    const int lw = w - 10, kr = lane >> 3;
    const char* wp = (const char*)wo + nt * 128 + (unsigned)((lw * 8 + kr) * (D_ * 2) + (((lane & 7) ^ (((kr >> 1) & 3) << 1)) * 16));
    { const int i = lw * 3;
#pragma unroll
      for (int q = 0; q < 3; ++q) if (i + q < 5) { const int o = (i + q) * 1024 + lane * 16; glds16(ws + WS_WM + (o < 4608 ? o : 0), lds + KB_WM + (i + q) * 1024); } }
#pragma unroll
    for (int i = 0; i < 12; ++i) glds16(wp + (size_t)i * (16 * D_ * 2), lds + (i * 2 + lw) * 1024);
    BAR();
#pragma unroll
    for (int i = 12; i < 24; ++i) glds16(wp + (size_t)i * (16 * D_ * 2), lds + (i * 2 + lw) * 1024);
    WAITV(12); BAR();
#pragma unroll
    for (int i = 24; i < 36; ++i) glds16(wp + (size_t)i * (16 * D_ * 2), lds + (i * 2 + lw) * 1024);
    WAITV(12); BAR();
    WAITV(0); BAR();
    {
#pragma unroll
      for (int i = 0; i < 3; ++i) {
        int ln = (jj * 2 + lw) * 173 + i * 64 + lane; const int lim = (jj * 2 + lw + 1) * 173; ln = ln < lim ? ln : lim - 1; ln = ln < 10368 ? ln : 10367;
        const int kr2 = (ln * 3641) >> 16;
        touch4((const char*)((xcd & 1) ? wg : wu) + (size_t)kr2 * (F_ * 2) + (xcd >> 1) * 2304 + (ln - kr2 * 18) * 128, lds + KB_SCR);
      }
    }
    BAR();
    STAMP(7); STAMP_OUT(1);
    return;
  }
  const int rb = w < 5 ? w : w - 5, kp = w < 5 ? 0 : 1;
  int tok = mt * 80 + rb * 16 + r; const bool valid = tok < S_; tok = valid ? tok : S_ - 1;
  const char* op = ws + WS_O + (size_t)((tok >> 5) * 36 + (g >> 1) + 2 * kp) * 1024 + ((g & 1) * 32 + (tok & 31)) * 16;
  u32x4 of[9];
#pragma unroll
  for (int i = 0; i < 3; ++i) of[i] = *(const u32x4*)(op + i * 4096);
  asm volatile("" ::: "memory");
  BAR();
  asm volatile("" ::: "memory");
#pragma unroll
  for (int i = 3; i < 6; ++i) of[i] = *(const u32x4*)(op + i * 4096);
  asm volatile("" ::: "memory");
  u32x2 xres[4]; float xm = 0.f;
  STAMP(1);
  f32x4 acc[5];
#pragma unroll
  for (int b = 0; b < 5; ++b) acc[b] = (f32x4){0.f, 0.f, 0.f, 0.f};
  const int krow = 32 * kp + 16 * (g >> 1) + 4 * (g & 1) + (r >> 2);
  unsigned adr[4];
#pragma unroll
  for (int b = 0; b < 4; ++b) adr[b] = (unsigned)(krow * 128 + ((b ^ ((krow >> 1) & 3)) * 32) + (r & 3) * 8);
  const unsigned adrm = (unsigned)(KB_WM + krow * 8);
#pragma unroll
  for (int c = 0; c < 3; ++c) {
    BAR();
    asm volatile("" ::: "memory");
    STAMP(2 + c);
    if (c == 0) {
#pragma unroll
      for (int i = 6; i < 9; ++i) of[i] = *(const u32x4*)(op + i * 4096);
      const char* xp = (const char*)x + ((size_t)tok * D_ + nt * 64 + 4 * g) * 2;
#pragma unroll
      for (int b = 0; b < 4; ++b) xres[b] = *(const u32x2*)(xp + b * 32);
      xm = *(const float*)(ws + WS_XM + (size_t)tok * 4);
      asm volatile("" ::: "memory");
    }
    u32x4 wf[3][5];
#pragma unroll
    for (int s3 = 0; s3 < 3; ++s3) {
#pragma unroll
      for (int b = 0; b < 4; ++b) wf[s3][b] = tr_frag(lds + adr[b] + c * (192 * 128) + s3 * (64 * 128), 8 * 128);
      wf[s3][4] = tr_frag(lds + adrm + c * (192 * 8) + s3 * (64 * 8), 64);
    }
#pragma unroll
    for (int s3 = 0; s3 < 3; ++s3)
#pragma unroll
      for (int b = 0; b < 5; ++b) acc[b] = MFMA16(BF8(wf[s3][b]), BF8(of[c * 3 + s3]), acc[b]);
  }
  STAMP(5);
  if (kp == 1) {
#pragma unroll
    for (int b = 0; b < 5; ++b) *(LAS f32x4*)(lds + KB_RED + (rb * 5 + b) * 1024 + lane * 16) = acc[b];
  }
  WAITL0();
  BAR();
  asm volatile("" ::: "memory");
  if (kp == 0) {
    const f32x4 mv = acc[4] + *(LAS f32x4*)(lds + KB_RED + (rb * 5 + 4) * 1024 + lane * 16);
    const float mean = xm + (mv[0] + mv[1]), mb = rbf(mean);
    f32x4 qv = {0.f, 0.f, 0.f, 0.f};
    u32x2 hp2[4], cp2[4];
#pragma unroll
    for (int b = 0; b < 4; ++b) {
      const f32x4 v = acc[b] + *(LAS f32x4*)(lds + KB_RED + (rb * 5 + b) * 1024 + lane * 16);
      const unsigned a0 = pk_bf16(v[0], v[1]), a1 = pk_bf16(v[2], v[3]);
      const unsigned h0 = pk_bf16(bf_lo(xres[b][0]) + bf_lo(a0), bf_hi(xres[b][0]) + bf_hi(a0));
      const unsigned h1 = pk_bf16(bf_lo(xres[b][1]) + bf_lo(a1), bf_hi(xres[b][1]) + bf_hi(a1));
      const f32x4 hv = {bf_lo(h0), bf_hi(h0), bf_lo(h1), bf_hi(h1)};
      const f32x4 d = hv - mean, dc = hv - mb;
      qv += d * d;
      hp2[b] = (u32x2){h0, h1}; cp2[b] = (u32x2){pk_bf16(dc[0], dc[1]), pk_bf16(dc[2], dc[3])};
    }
    const unsigned ho = (unsigned)(((tok >> 5) * 36 + nt * 4 + (g >> 1)) * 1024 + ((g & 1) * 32 + (tok & 31)) * 16);
#pragma unroll
    for (int b = 0; b < 4; b += 2) {
      u32x4 hq, cq;
#pragma unroll
      for (int e = 0; e < 2; ++e) {
        const auto sh = __builtin_amdgcn_permlane32_swap(hp2[b][e], hp2[b + 1][e], false, false);
        const auto sc = __builtin_amdgcn_permlane32_swap(cp2[b][e], cp2[b + 1][e], false, false);
        hq[e] = sh[0]; hq[2 + e] = sh[1]; cq[e] = sc[0]; cq[2 + e] = sc[1];
      }
      if (valid) { *(u32x4*)(ws + WS_H + ho + b * 1024) = hq; *(u32x4*)(ws + WS_HC + ho + b * 1024) = cq; }
    }
    float q = (qv[0] + qv[1]) + (qv[2] + qv[3]);
    q += __shfl_xor(q, 16); q += __shfl_xor(q, 32);
    if (g == 0 && valid) *(float*)(ws + WS_ST + ((size_t)tok * 12 + nt) * 4) = q;
  }
  STAMP(7); STAMP_OUT(1);
}

constexpr int KC_UPOFF = 16384 + 9216 + 128;
constexpr int KC_STAGE = 35072;
constexpr int KC_STOFF = 4 * KC_STAGE;
constexpr int KC_SCR = KC_STOFF + 12288;
constexpr int KC_LDS = KC_SCR + 256;

DI f32x2 geglu2(f32x2 a, f32x2 b, f32x2 k0, f32x2 k1, f32x2 ik2) {
  const f32x2 tt = a * __builtin_elementwise_fma(a * a, k1, k0);
  const f32x2 e = {__builtin_amdgcn_exp2f(tt[0]), __builtin_amdgcn_exp2f(tt[1])};
  const f32x2 d = __builtin_elementwise_fma(e, ik2, ik2);
  const f32x2 rr = {__builtin_amdgcn_rcpf(d[0]), __builtin_amdgcn_rcpf(d[1])};
  return (a * b) * rr;
}
DI void ln_consts(const LAS char* sp_, f32x2& k0, f32x2& k1, f32x2& ik2) {
  const LAS f32x4* sp = (const LAS f32x4*)sp_;
  const f32x4 s0 = sp[0], s1 = sp[1]; const float s8 = *(const LAS float*)(sp + 2);
  const float var = rbf((((s0[0] + s0[1]) + (s0[2] + s0[3])) + ((s1[0] + s1[1]) + (s1[2] + s1[3])) + s8) * (1.0f / D_));
  const float sd = rbf(__builtin_amdgcn_sqrtf(rbf(var + 1e-5f)));
  const float rs = __builtin_amdgcn_rcpf(sd);
  const float k0s = -2.3022081981f * rs;
  const float k1s = -0.1029432397f * rs * rs * rs;
  const float ik2s = sd * sd;
  k0 = (f32x2){k0s, k0s}; k1 = (f32x2){k1s, k1s}; ik2 = (f32x2){ik2s, ik2s};
}

__global__ __launch_bounds__(768, 1) void kC(const u16* __restrict__ wg, const u16* __restrict__ wu, const u16* __restrict__ wd, char* __restrict__ ws) {
  extern __shared__ __attribute__((aligned(16))) char smem[];
  LAS char* lds = (LAS char*)smem;
  const int tid = threadIdx.x, lane = tid & 63, w = __builtin_amdgcn_readfirstlane(tid >> 6);
  const int bx = blockIdx.x, xcd = bx & 7, jj = bx >> 3;
  const int pm = (xcd & 1) * 4 + (jj & 3), pn = (xcd >> 1) * 8 + (jj >> 2);
  const int wm = w & 3, wn = w >> 2;

  STAMP_DECL STAMP(0);
  const char* src[3]; unsigned dst[3]; unsigned step[3];
  src[0] = ws + WS_HC + (size_t)((pm * 8 + (w >> 1)) * 36 + (w & 1)) * 1024 + lane * 16; dst[0] = w * 1024; step[0] = 2048;
#define KC_G(i, st) do { glds16(src[i], lds + ((st) & 3) * KC_STAGE + dst[i]); src[i] += step[i]; } while (0)
  KC_G(0, 0);
#pragma unroll
  for (int i = 1; i < 3; ++i) {
    int sl = i * 12 + w; sl = sl > 33 ? 33 : sl;
    if (sl < 16) {
      src[i] = ws + WS_HC + (size_t)((pm * 8 + (sl >> 1)) * 36 + (sl & 1)) * 1024 + lane * 16;
      dst[i] = sl * 1024; step[i] = 2048;
    } else {
      const int c = sl - 16, m = c >= 9 ? 1 : 0, cc = c - 9 * m, q = cc * 64 + lane, rr = (q * 3641) >> 16, ch = q - rr * 18;
      src[i] = (const char*)(m ? wu : wg) + pn * 288 + (unsigned)(rr * (F_ * 2) + ch * 16);
      dst[i] = (m ? KC_UPOFF : 16384) + cc * 1024; step[i] = 32 * F_ * 2;
    }
    KC_G(i, 0);
  }
#define KC_ISSUE(st) do { KC_G(0, st); KC_G(1, st); KC_G(2, st); } while (0)
  KC_ISSUE(1); KC_ISSUE(2);
  glds16(ws + WS_ST + (size_t)pm * 12288 + w * 1024 + lane * 16, lds + KC_STOFF + w * 1024);
  const auto ars = __builtin_amdgcn_make_buffer_rsrc((void*)(ws + WS_ACT), 0, S_ * F_ * 2, 0x00020000);
#define KC_TOUCH_WD() do { if (w < 6) {     \
    int ln = w * 64 + lane; ln = ln < 324 ? ln : 323; \
    touch4((const char*)wd + (size_t)(xcd >> 1) * (1152 * D_ * 2) + (size_t)(jj * 324 + ln) * 128, lds + KC_SCR); } } while (0)

  const int g = lane >> 4, r = lane & 15;
  f32x4 acc[6][4];
#pragma unroll
  for (int a = 0; a < 6; ++a)
#pragma unroll
    for (int b = 0; b < 4; ++b) acc[a][b] = (f32x4){0.f, 0.f, 0.f, 0.f};
  const unsigned xadr = (unsigned)(wm * 4096 + (g >> 1) * 1024 + ((g & 1) * 32 + r) * 16);
  const unsigned wadr = (unsigned)(16384 + (16 * (g >> 1) + 4 * (g & 1) + (r >> 2)) * 288 + wn * 96 + (r & 3) * 8);
  u32x4 xf[4], wA[3], wB[3];
#define KC_WOFF(j) ((((j) & 1) ? (KC_UPOFF - 16384) : 0) + ((j) >> 1) * 32)
#define KC_RW(WF, i, j, base) WF[i] = tr_frag((base) + wadr + KC_WOFF(j), 2304)
#define KC_RX(c, base) xf[c] = *(LAS u32x4*)((base) + xadr + ((c) >> 1) * 2048 + ((c) & 1) * 256)
#define KC_M3(WF, j0, c) do { acc[j0][c] = MFMA16(BF8(WF[0]), BF8(xf[c]), acc[j0][c]); acc[j0 + 1][c] = MFMA16(BF8(WF[1]), BF8(xf[c]), acc[j0 + 1][c]); \
    acc[j0 + 2][c] = MFMA16(BF8(WF[2]), BF8(xf[c]), acc[j0 + 2][c]); } while (0)
#define KC_HALFA(base, G0, G1) do { \
    KC_M3(wA, 0, 0); SCHED0(); KC_RW(wB, 0, 3, base); SCHED0(); \
    KC_M3(wA, 0, 1); SCHED0(); KC_RW(wB, 1, 4, base); G0; SCHED0(); \
    KC_M3(wA, 0, 2); SCHED0(); KC_RW(wB, 2, 5, base); SCHED0(); \
    KC_M3(wA, 0, 3); SCHED0(); G1; SCHED0(); } while (0)
#define KC_HALFB(nb, G0) do { \
    KC_M3(wB, 3, 0); SCHED0(); KC_RX(0, nb); KC_RW(wA, 0, 0, nb); SCHED0(); \
    KC_M3(wB, 3, 1); SCHED0(); KC_RX(1, nb); KC_RW(wA, 1, 1, nb); SCHED0(); \
    KC_M3(wB, 3, 2); SCHED0(); KC_RX(2, nb); KC_RW(wA, 2, 2, nb); G0; SCHED0(); \
    KC_M3(wB, 3, 3); SCHED0(); KC_RX(3, nb); SCHED0(); } while (0)
  STAMP(1);
  WAITV(7); BAR(); asm volatile("" ::: "memory");
  STAMP(2);
  KC_RX(0, lds); KC_RW(wA, 0, 0, lds); KC_RW(wA, 1, 1, lds); KC_RW(wA, 2, 2, lds); KC_RX(1, lds); KC_RX(2, lds); KC_RX(3, lds);
  KC_ISSUE(3);
  KC_HALFA(lds, (void)0, (void)0);
  STAMP(3);
#pragma unroll 1
  for (int t = 1; t < 15; ++t) {
    WAITVL(6);
    BAR(); asm volatile("" ::: "memory");
    LAS char* sb = lds + (t & 3) * KC_STAGE;
    KC_HALFB(sb, KC_G(0, t + 3));
    KC_HALFA(sb, KC_G(1, t + 3), KC_G(2, t + 3));
  }
  WAITVL(0);
  BAR(); asm volatile("" ::: "memory");
  KC_TOUCH_WD();
#pragma unroll 1
  for (int t = 15; t < 18; ++t) {
    LAS char* sb = lds + (t & 3) * KC_STAGE;
    KC_HALFB(sb, (void)0);
    KC_HALFA(sb, (void)0, (void)0);
  }
  KC_M3(wB, 3, 0); KC_M3(wB, 3, 1); KC_M3(wB, 3, 2); KC_M3(wB, 3, 3);
  STAMP(4);
#pragma unroll
  for (int cp = 0; cp < 2; ++cp) {
    f32x2 k0[2], k1[2], ik2[2];
#pragma unroll
    for (int e = 0; e < 2; ++e) ln_consts(lds + KC_STOFF + (wm * 64 + cp * 32 + e * 16 + r) * 48, k0[e], k1[e], ik2[e]);
#pragma unroll
    for (int f = 0; f < 3; ++f) {
      u32x2 pc[2];
#pragma unroll
      for (int e = 0; e < 2; ++e) {
        const f32x4 a = acc[2 * f][2 * cp + e], b = acc[2 * f + 1][2 * cp + e];
        const f32x2 o0 = geglu2((f32x2){a[0], a[1]}, (f32x2){b[0], b[1]}, k0[e], k1[e], ik2[e]);
        const f32x2 o1 = geglu2((f32x2){a[2], a[3]}, (f32x2){b[2], b[3]}, k0[e], k1[e], ik2[e]);
        pc[e] = (u32x2){pk_bf16(o0[0], o0[1]), pk_bf16(o1[0], o1[1])};
      }
      const auto s0 = __builtin_amdgcn_permlane32_swap(pc[0][0], pc[1][0], false, false);
      const auto s1 = __builtin_amdgcn_permlane32_swap(pc[0][1], pc[1][1], false, false);
      const u32x4 v = {s0[0], s1[0], s0[1], s1[1]};
      __builtin_amdgcn_raw_buffer_store_b128(v, ars, ((pm * 8 + wm * 2 + cp) * 288 + pn * 9 + wn * 3 + f) * 1024 + ((g & 1) * 32 + (g >> 1) * 16 + r) * 16, 0, 16);
    }
  }
  STAMP(5); STAMP_OUT(2);
}

constexpr int KD_STAGE = 16384 + 18432;
constexpr int KD_SCR = 4 * KD_STAGE;
constexpr int KD_LDS = KD_SCR + 256;

template <int NB, int NG>
DI void kD_loop(LAS char* lds, const char* srcA, const char* const (&srcW)[3], int w, int lane, f32x16 (&acc)[3]) {
  const int tt = w & 3, hh = lane >> 5;
#define KD_GA(i, st) glds16(srcA + (size_t)(i) * (2 * 288 * 1024) + (st) * 4096, lds + ((st) & 3) * KD_STAGE + (i) * 8192 + w * 1024)
#define KD_GW(i, st) glds16(srcW[i] + (size_t)(st) * (64 * D_ * 2), lds + ((st) & 3) * KD_STAGE + 16384 + (i) * 8192 + w * 1024)
#define KD_GW2(st) do { if (NG == 5) glds16(srcW[2] + (size_t)(st) * (64 * D_ * 2), lds + ((st) & 3) * KD_STAGE + 32768 + (w & 1) * 1024); } while (0)
#define KD_WAIT2() do { if (NG == 5) WAITVL(10); else WAITVL(8); } while (0)
  const unsigned xadr = (unsigned)(tt * 4096 + lane * 16);
  const unsigned wrow = (unsigned)(16384 + (4 * hh + ((lane & 15) >> 2)) * 288 + (lane & 3) * 8);
  const unsigned wadr0 = wrow + ((lane >> 4) & 1) * 128 + (NB == 3 ? 0 : 96);
  const unsigned wadr1 = NB == 3 ? wadr0 + 32 : wrow + 256;
  u32x4 xP[2], wP[2][3], xQ[2], wQ[2][3];
#define KD_RW(WF, f, ad, base, kk) WF[f] = tr_frag((base) + (ad) + (kk) * 4608, 2304)
#define KD_RX(XF, base, kk) XF = *(LAS u32x4*)((base) + xadr + (kk) * 1024)
#define KD_STEP(XC, WC, XN, WN, base, kk, G0) do { \
    acc[0] = MFMA32(BF8(WC[0]), BF8(XC), acc[0]); SCHED0(); KD_RW(WN, 0, wadr0, base, kk); KD_RX(XN, base, kk); SCHED0(); \
    acc[1] = MFMA32(BF8(WC[1]), BF8(XC), acc[1]); SCHED0(); KD_RW(WN, 1, wadr1, base, kk); G0; SCHED0(); \
    if (NB == 3) { acc[2] = MFMA32(BF8(WC[2]), BF8(XC), acc[2]); SCHED0(); KD_RW(WN, 2, wadr0 + 64, base, kk); SCHED0(); } } while (0)
#define KD_HALF(XC, WC, XN, WN, base, hk, G0, G1) do { KD_STEP(XC[0], WC[0], XN[0], WN[0], base, 2 * (hk), G0); KD_STEP(XC[1], WC[1], XN[1], WN[1], base, 2 * (hk) + 1, G1); } while (0)
#pragma unroll
  for (int kk = 0; kk < 2; ++kk) { KD_RW(wP[kk], 0, wadr0, lds, kk); KD_RX(xP[kk], lds, kk); KD_RW(wP[kk], 1, wadr1, lds, kk); if (NB == 3) KD_RW(wP[kk], 2, wadr0 + 64, lds, kk); }
  KD_GA(0, 3); KD_GA(1, 3); KD_GW(0, 3); KD_GW(1, 3); KD_GW2(3);
  KD_HALF(xP, wP, xQ, wQ, lds, 1, (void)0, (void)0);
#pragma unroll 1
  for (int t = 1; t < 15; ++t) {
    KD_WAIT2();
    BAR(); asm volatile("" ::: "memory");
    LAS char* sb = lds + (t & 3) * KD_STAGE;
    KD_HALF(xQ, wQ, xP, wP, sb, 0, do { KD_GA(0, t + 3); KD_GW2(t + 3); } while (0), KD_GA(1, t + 3));
    KD_HALF(xP, wP, xQ, wQ, sb, 1, KD_GW(0, t + 3), KD_GW(1, t + 3));
  }
#pragma unroll 1
  for (int t = 15; t < 18; ++t) {
    if (t == 15) KD_WAIT2(); else if (t == 16) { if (NG == 5) WAITVL(5); else WAITVL(4); } else WAITVL(0);
    BAR(); asm volatile("" ::: "memory");
    LAS char* sb = lds + (t & 3) * KD_STAGE;
    KD_HALF(xQ, wQ, xP, wP, sb, 0, (void)0, (void)0);
    KD_HALF(xP, wP, xQ, wQ, sb, 1, (void)0, (void)0);
  }
#pragma unroll
  for (int kk = 0; kk < 2; ++kk) {
    acc[0] = MFMA32(BF8(wQ[kk][0]), BF8(xQ[kk]), acc[0]); acc[1] = MFMA32(BF8(wQ[kk][1]), BF8(xQ[kk]), acc[1]);
    if (NB == 3) acc[2] = MFMA32(BF8(wQ[kk][2]), BF8(xQ[kk]), acc[2]);
  }
}

__global__ __launch_bounds__(512, 1) void kD(const u16* __restrict__ wd, char* __restrict__ ws, u16* __restrict__ out) {
  extern __shared__ __attribute__((aligned(16))) char smem[];
  LAS char* lds = (LAS char*)smem;
  const int tid = threadIdx.x, lane = tid & 63, w = __builtin_amdgcn_readfirstlane(tid >> 6);
  const int bx = blockIdx.x, xcd = bx & 7, jj = bx >> 3;
  const int ks = xcd >> 1, mt = (xcd & 1) * 8 + (jj & 7), nt = jj >> 3;
  const int tt = w & 3, cg = w >> 2;
  const int hh = lane >> 5;

  STAMP_DECL STAMP(0);
  const char* srcA = ws + WS_ACT + (size_t)((mt * 4 + (w >> 2)) * 288 + ks * 72 + (w & 3)) * 1024 + lane * 16;
  KD_GA(0, 0); KD_GA(1, 0);
  const char* srcW[3];
#pragma unroll
  for (int i = 0; i < 3; ++i) {
    const int cc = i < 2 ? i * 8 + w : 16 + (w & 1);
    const int q = cc * 64 + lane, rr = (q * 3641) >> 16, ch = q - rr * 18;
    srcW[i] = (const char*)wd + (size_t)(ks * 1152 * D_ + nt * 144) * 2 + (unsigned)(rr * (D_ * 2) + ch * 16);
  }
  const bool extra = (w == 4 || w == 5);
#define KD_GX(st) do { if (extra) glds16(srcW[2] + (size_t)(st) * (64 * D_ * 2), lds + ((st) & 3) * KD_STAGE + 32768 + (w & 1) * 1024); } while (0)
  KD_GW(0, 0); KD_GW(1, 0); KD_GX(0);
  KD_GA(0, 1); KD_GA(1, 1); KD_GW(0, 1); KD_GW(1, 1); KD_GX(1);
  KD_GA(0, 2); KD_GA(1, 2); KD_GW(0, 2); KD_GW(1, 2); KD_GX(2);
  const u32x4 hres0 = *(const u32x4*)(ws + WS_H + (size_t)((mt * 4 + ks) * 36 + nt * 9 + w) * 1024 + lane * 16);
  const u32x4 hres1 = *(const u32x4*)(ws + WS_H + (size_t)((mt * 4 + ks) * 36 + nt * 9 + 8) * 1024 + lane * 16);
  asm volatile("" ::: "memory");
  f32x16 acc[3];
#pragma unroll
  for (int a = 0; a < 3; ++a)
#pragma unroll
    for (int i = 0; i < 16; ++i) acc[a][i] = 0.f;
  STAMP(1);
  if (extra) WAITVL(10); else WAITVL(8);
  BAR(); asm volatile("" ::: "memory");
  STAMP(2);
  if (cg == 0) kD_loop<3, 4>(lds, srcA, srcW, w, lane, acc);
  else if (extra) kD_loop<2, 5>(lds, srcA, srcW, w, lane, acc);
  else kD_loop<2, 4>(lds, srcA, srcW, w, lane, acc);
  STAMP(3);
  const int tile = mt * 4 + nt;
  const auto prs = __builtin_amdgcn_make_buffer_rsrc((void*)(ws + WS_PART), 0, (int)(64 * 4 * 3 * PART_SLAB), 0x00020000);
  {
    const int sidx = ks - (ks > tt ? 1 : 0);
    const int po = (int)(((tile * 4 + tt) * 3 + sidx) * PART_SLAB) + lane * 16;
    LAS char* lo = lds + 2 * KD_STAGE + lane * 16;
#pragma unroll
    for (int a = 0; a < 3; ++a)
#pragma unroll
      for (int G = 0; G < 4; ++G) {
        const f32x4 v = {acc[a][4 * G], acc[a][4 * G + 1], acc[a][4 * G + 2], acc[a][4 * G + 3]};
        const int u0 = (a + 4 * (G >> 1)) * 2048 + (G & 1) * 1024;
        const int u1 = (a == 0 ? (3 + 4 * (G >> 1)) : 8) * 2048 + (G & 1) * 1024;
        const bool ok = cg == 0 || a == 0 || (a == 1 && G < 2);
        const int u = cg ? u1 : u0;
        if (ok) {
          if (tt != ks) __builtin_amdgcn_raw_buffer_store_b128(__builtin_bit_cast(u32x4, v), prs, po + u, 0, 16);
          else *(LAS f32x4*)(lo + u) = v;
        }
      }
  }
  STAMP(5);
  WAITVL(0);
  BAR(); asm volatile("" ::: "memory");
  STAMP(6);
  GAS unsigned* cnt = (GAS unsigned*)(ws + WS_CNT);
  if (tid < 4 && tid != ks) __hip_atomic_fetch_add(cnt + (tile * 4 + tid) * 64, 1u, __ATOMIC_RELAXED, __HIP_MEMORY_SCOPE_AGENT);
  if (w == 0) {
    unsigned spins = 0;
    while (__builtin_amdgcn_readfirstlane(__hip_atomic_load(cnt + (tile * 4 + ks) * 64, __ATOMIC_RELAXED, __HIP_MEMORY_SCOPE_AGENT)) < 3u) {
      if (++spins > 2000000u) break;
      __builtin_amdgcn_s_sleep(1);
    }
  }
  asm volatile("" ::: "memory");
  BAR(); asm volatile("" ::: "memory");
  STAMP(7);
#pragma unroll
  for (int i = 0; i < 2; ++i) {
    if (i == 1 && w != 1) break;
    const int grp = i ? 8 : w;
    const int po = (int)((tile * 4 + ks) * 3 * PART_SLAB) + grp * 2048 + lane * 16;
    u32x4 pp[3][2];
#pragma unroll
    for (int q = 0; q < 3; ++q) {
      pp[q][0] = __builtin_amdgcn_raw_buffer_load_b128(prs, po + q * (int)PART_SLAB, 0, 16);
      pp[q][1] = __builtin_amdgcn_raw_buffer_load_b128(prs, po + q * (int)PART_SLAB + 1024, 0, 16);
    }
    const u32x4 hr = i ? hres1 : hres0;
    u16* op = out + (size_t)(mt * 128 + ks * 32 + (lane & 31)) * D_ + nt * 144 + grp * 16 + 4 * hh;
#pragma unroll
    for (int hf = 0; hf < 2; ++hf) {
      f32x4 v = *(LAS f32x4*)(lds + 2 * KD_STAGE + grp * 2048 + hf * 1024 + lane * 16);
      v += __builtin_bit_cast(f32x4, pp[0][hf]); v += __builtin_bit_cast(f32x4, pp[1][hf]); v += __builtin_bit_cast(f32x4, pp[2][hf]);
      const unsigned a0 = pk_bf16(v[0], v[1]), a1 = pk_bf16(v[2], v[3]);
      const unsigned o0 = pk_bf16(bf_lo(hr[2 * hf]) + bf_lo(a0), bf_hi(hr[2 * hf]) + bf_hi(a0));
      const unsigned o1 = pk_bf16(bf_lo(hr[2 * hf + 1]) + bf_lo(a1), bf_hi(hr[2 * hf + 1]) + bf_hi(a1));
      *(u32x2*)(op + 8 * hf) = (u32x2){o0, o1};
    }
  }
  STAMP(8); STAMP_OUT(3);
}

extern "C" void kernel_launch(void* const* d_in, const int* in_sizes, int n_in, void* d_out, int out_size, void* d_ws, size_t ws_size,
                              hipStream_t stream) {
  const u16* x = (const u16*)d_in[0];
  const u16* wqkv = (const u16*)d_in[1];
  const u16* wo = (const u16*)d_in[2];
  const u16* wg = (const u16*)d_in[4];
  const u16* wu = (const u16*)d_in[5];
  const u16* wd = (const u16*)d_in[6];
  char* ws = (char*)d_ws;
  (void)hipFuncSetAttribute((const void*)kA, hipFuncAttributeMaxDynamicSharedMemorySize, KA_LDS);
  (void)hipFuncSetAttribute((const void*)kB, hipFuncAttributeMaxDynamicSharedMemorySize, KB_LDS);
  (void)hipFuncSetAttribute((const void*)kC, hipFuncAttributeMaxDynamicSharedMemorySize, KC_LDS);
  (void)hipFuncSetAttribute((const void*)kD, hipFuncAttributeMaxDynamicSharedMemorySize, KD_LDS);
  kA<<<256, 576, KA_LDS, stream>>>(x, wqkv, wo, wg, wu, ws);
  kB<<<256, 768, KB_LDS, stream>>>(x, wo, wg, wu, ws);
  kC<<<256, 768, KC_LDS, stream>>>(wg, wu, wd, ws);
  kD<<<256, 512, KD_LDS, stream>>>(wd, ws, (u16*)d_out);
}
```

```cpp
#include <hip/hip_runtime.h>
#include <hip/hip_bf16.h>
#include <stdint.h>

#define LAS __attribute__((address_space(3)))
#define GAS __attribute__((address_space(1)))
#define DI __device__ __forceinline__
typedef unsigned short u16;
typedef short s16x4 __attribute__((ext_vector_type(4)));
typedef short s16x8 __attribute__((ext_vector_type(8)));
typedef __bf16 bf16x2 __attribute__((ext_vector_type(2)));
typedef __bf16 bf16x8 __attribute__((ext_vector_type(8)));
typedef float f32x2 __attribute__((ext_vector_type(2)));
typedef float f32x4 __attribute__((ext_vector_type(4)));
typedef float f32x16 __attribute__((ext_vector_type(16)));
typedef unsigned u32x2 __attribute__((ext_vector_type(2)));
typedef unsigned u32x4 __attribute__((ext_vector_type(4)));

constexpr int S_ = 2048, D_ = 576, F_ = 4608;

constexpr size_t WS_O    = 0;
constexpr size_t WS_H    = WS_O + (size_t)S_ * D_ * 2;
constexpr size_t WS_HC   = WS_H + (size_t)S_ * D_ * 2;
constexpr size_t WS_ST   = WS_HC + (size_t)S_ * D_ * 2;
constexpr size_t WS_XM   = WS_ST + (size_t)S_ * 12 * 4;
constexpr size_t WS_WM   = WS_XM + (size_t)S_ * 4;
constexpr size_t WS_CNT  = WS_WM + 8192;
constexpr size_t WS_ACT  = WS_CNT + 256 * 256;
constexpr size_t WS_PART = WS_ACT + (size_t)S_ * F_ * 2;
constexpr size_t PART_SLAB = 18 * 1024;

DI float bf_lo(unsigned v) { return __uint_as_float(v << 16); }
DI float bf_hi(unsigned v) { return __uint_as_float(v & 0xffff0000u); }
DI unsigned pk_bf16(float a, float b) { f32x2 v = {a, b}; return __builtin_bit_cast(unsigned, __builtin_convertvector(v, bf16x2)); }
DI float rbf(float a) { return bf_lo(pk_bf16(a, 0.f)); }
DI float dot2bf(unsigned a, unsigned b, float c) { return __builtin_amdgcn_fdot2_f32_bf16(__builtin_bit_cast(bf16x2, a), __builtin_bit_cast(bf16x2, b), c, false); }
DI s16x4 tr_read(LAS char* p) { return __builtin_amdgcn_ds_read_tr16_b64_v4i16((LAS s16x4*)p); }
DI bf16x8 cat8(s16x4 a, s16x4 b) { return __builtin_bit_cast(bf16x8, __builtin_shufflevector(a, b, 0, 1, 2, 3, 4, 5, 6, 7)); }
DI void glds16(const void* g, LAS char* l) {
  unsigned keep; const unsigned la = __builtin_amdgcn_readfirstlane((unsigned)(size_t)l);
  asm volatile("s_mov_b32 %0, m0\n\ts_mov_b32 m0, %2\n\ts_nop 0\n\tglobal_load_lds_dwordx4 %1, off\n\ts_mov_b32 m0, %0" : "=&s"(keep) : "v"(g), "s"(la) : "memory");
}
DI u32x4 tr_frag(LAS char* p, int second) {
  const u32x2 a = __builtin_bit_cast(u32x2, tr_read(p)), b = __builtin_bit_cast(u32x2, tr_read(p + second));
  return (u32x4){a[0], a[1], b[0], b[1]};
}
DI u32x4 tr_frag2(LAS char* p0, LAS char* p1) {
  const u32x2 a = __builtin_bit_cast(u32x2, tr_read(p0)), b = __builtin_bit_cast(u32x2, tr_read(p1));
  return (u32x4){a[0], a[1], b[0], b[1]};
}
#define BF8(v) __builtin_bit_cast(bf16x8, (v))
DI void touch4(const void* g, LAS char* l) {
  unsigned keep; const unsigned la = __builtin_amdgcn_readfirstlane((unsigned)(size_t)l);
  asm volatile("s_mov_b32 %0, m0\n\ts_mov_b32 m0, %2\n\ts_nop 0\n\tglobal_load_lds_dword %1, off\n\ts_mov_b32 m0, %0" : "=&s"(keep) : "v"(g), "s"(la) : "memory");
}
#define CODE_ADDR(sym) ({ unsigned long long _pc; int _off; asm volatile("s_getpc_b64 %0\n\ts_mov_b32 %1, " sym "@rel32@lo+4" : "=s"(_pc), "=s"(_off)); (const char*)(_pc + (long long)_off); })
DI void touch_code(const char* lo, const char* hi, int lane, LAS char* scr) {
  const long long n = (hi - lo) >> 7;
  if (n > 0 && n <= 512) {
#pragma unroll 1
    for (int i = 0; i < (int)n; i += 64) { const int l = i + lane < (int)n ? i + lane : (int)n - 1; touch4(lo + (size_t)l * 128, scr); }
  }
}
#define MFMA16(a, b, c) __builtin_amdgcn_mfma_f32_16x16x32_bf16((a), (b), (c), 0, 0, 0)
#define MFMA32(a, b, c) __builtin_amdgcn_mfma_f32_32x32x16_bf16((a), (b), (c), 0, 0, 0)
#define BAR() __builtin_amdgcn_s_barrier()
#define SCHED0() __builtin_amdgcn_sched_barrier(0)
#define WAITV(n) asm volatile("s_waitcnt vmcnt(" #n ")" ::: "memory")
#define WAITVL(n) asm volatile("s_waitcnt vmcnt(" #n ") lgkmcnt(0)" ::: "memory")
#define WAITL0() asm volatile("s_waitcnt lgkmcnt(0)" ::: "memory")

#define STAMP_DECL
#define STAMP(i) do {} while (0)
#define STAMP_OUT(kid) do {} while (0)
DI unsigned fm32_piece(int tok, int c4, int KB16) {
  const int pi = (c4 & 15) >> 2;
  return (unsigned)(((tok >> 5) * KB16 + (c4 >> 4)) * 1024 + (((pi & 1) * 32 + (tok & 31)) * 16) + (pi >> 1) * 8);
}

constexpr int KA_WROW = 240;
constexpr int KA_XB = 144 * 128;
constexpr int KA_STAGE = KA_XB + 64 * KA_WROW;
constexpr int KA_ONES = 4 * KA_STAGE;
constexpr int KA_SQ = KA_ONES + 64;
constexpr int KA_SUM = KA_SQ + 160 * 4;
constexpr int KA_CST = KA_SUM + 160 * 4;
constexpr int KA_SCR = KA_CST + 128 * 4;
constexpr int KA_LDS = KA_SCR + 256;
constexpr int KA_QROW = 304;
constexpr int KA_OOFF = 144 * KA_QROW;

template <int NT, int NG>
DI void kA_loop(LAS char* lds, const char* (&src)[5], int w, const unsigned (&xad)[3], const unsigned (&xmul)[3], unsigned wad, unsigned wmul,
                f32x16 (&acc)[3], float& sq) {
#define KA_G(i, st) do { glds16(src[i], lds + ((st) & 3) * KA_STAGE + ((i) * 8 + w) * 1024); src[i] += ((i) * 8 + w < 18) ? 128 : 64 * 3456; } while (0)
#define KA_G4(st) do { if (NG == 5) { glds16(src[4], lds + ((st) & 3) * KA_STAGE + 32 * 1024); src[4] += 64 * 3456; } } while (0)
  u32x4 xP[2][3], wP[2], xQ[2][3], wQ[2];
  unsigned xb[3], wb;
#define KA_BASE(st) do { const unsigned _s = (unsigned)(((st) & 3) * KA_STAGE); \
    _Pragma("unroll") for (int _i = 0; _i < NT; ++_i) xb[_i] = xad[_i] + (_s & xmul[_i]); wb = wad + (_s & wmul); } while (0)
#define KA_RX(XF, i, kk) XF[i] = *(LAS u32x4*)(lds + (xb[i] ^ (((kk) * 32) & xmul[i])))
#define KA_SQ2(a, b) do { sq = dot2bf(a, a, sq); sq = dot2bf(b, b, sq); } while (0)
#define KA_STEP(XC, WC, XN, WN, kk, G0) do { \
    acc[0] = MFMA32(BF8(WC), BF8(XC[0]), acc[0]); SCHED0(); WN = tr_frag2(lds + wb + (((kk) * (16 * KA_WROW)) & wmul), lds + wb + (((kk) * (16 * KA_WROW) + KA_WROW) & wmul)); KA_RX(XN, 0, kk); \
    KA_SQ2(XC[0][0], XC[0][1]); SCHED0(); \
    acc[1] = MFMA32(BF8(WC), BF8(XC[1]), acc[1]); SCHED0(); KA_RX(XN, 1, kk); G0; \
    KA_SQ2(XC[0][2], XC[0][3]); SCHED0(); \
    if (NT == 3) { acc[2] = MFMA32(BF8(WC), BF8(XC[2]), acc[2]); SCHED0(); KA_RX(XN, 2, kk); SCHED0(); } } while (0)
#define KA_HALF(XC, WC, XN, WN, hk, G0, G1) do { KA_STEP(XC[0], WC[0], XN[0], WN[0], 2 * (hk), G0); KA_STEP(XC[1], WC[1], XN[1], WN[1], 2 * (hk) + 1, G1); } while (0)
  KA_BASE(0);
#pragma unroll
  for (int kk = 0; kk < 2; ++kk) {
    wP[kk] = tr_frag2(lds + wb + ((kk * (16 * KA_WROW)) & wmul), lds + wb + ((kk * (16 * KA_WROW) + KA_WROW) & wmul));
#pragma unroll
    for (int i = 0; i < NT; ++i) KA_RX(xP[kk], i, kk);
  }
  KA_G(0, 3); KA_G(1, 3); KA_G(2, 3); KA_G(3, 3); KA_G4(3);
  KA_HALF(xP, wP, xQ, wQ, 1, (void)0, (void)0);
#pragma unroll 1
  for (int t = 1; t < 6; ++t) {
    if (NG == 5) WAITVL(10); else WAITVL(8);
    BAR(); asm volatile("" ::: "memory");
    KA_BASE(t);
    KA_HALF(xQ, wQ, xP, wP, 0, do { KA_G(0, t + 3); KA_G4(t + 3); } while (0), KA_G(1, t + 3));
    KA_HALF(xP, wP, xQ, wQ, 1, KA_G(2, t + 3), KA_G(3, t + 3));
  }
#pragma unroll 1
  for (int t = 6; t < 9; ++t) {
    if (t == 6) { if (NG == 5) WAITVL(10); else WAITVL(8); } else if (t == 7) { if (NG == 5) WAITVL(5); else WAITVL(4); } else WAITVL(0);
    BAR(); asm volatile("" ::: "memory");
    KA_BASE(t);
    KA_HALF(xQ, wQ, xP, wP, 0, (void)0, (void)0);
    KA_HALF(xP, wP, xQ, wQ, 1, (void)0, (void)0);
  }
#pragma unroll
  for (int kk = 0; kk < 2; ++kk) {
#pragma unroll
    for (int i = 0; i < NT; ++i) acc[i] = MFMA32(BF8(wQ[kk]), BF8(xQ[kk][i]), acc[i]);
#pragma unroll
    for (int e = 0; e < 4; ++e) sq = dot2bf(xQ[kk][0][e], xQ[kk][0][e], sq);
  }
}

extern "C" __global__ __launch_bounds__(576, 1) void kA(const u16* __restrict__ x, const u16* __restrict__ wqkv, const u16* __restrict__ wo,
                                             const u16* __restrict__ wg, const u16* __restrict__ wu, const u16* __restrict__ wd, const u16* __restrict__ outp, char* __restrict__ ws) {
  extern __shared__ __attribute__((aligned(16))) char smem[];
  LAS char* lds = (LAS char*)smem;
  const int tid = threadIdx.x, lane = tid & 63, w = __builtin_amdgcn_readfirstlane(tid >> 6);
  const int bx = blockIdx.x, xcd = bx & 7, jj = bx >> 3;
  const int mt = (xcd >> 1) * 4 + (jj & 3), hp = (xcd & 1) * 8 + (jj >> 2);
  STAMP_DECL STAMP(0);
  if (w == 8) {
    touch_code(CODE_ADDR("kA"), CODE_ADDR("kB"), lane, lds + KA_SCR);
    if (lane < 4) *(LAS unsigned*)(lds + KA_ONES + lane * 4) = 0x3f803f80u;
#pragma unroll
    for (int i = 0; i < 3; ++i) {
      int ln = i * 64 + lane; ln = ln < 162 ? ln : 161;
      const int L = ln * 8 + (jj >> 2);
      int row = mt * 128 - 16 + L % 144; row = row < 0 ? 0 : row;
      touch4((const char*)x + (size_t)row * (D_ * 2) + (L / 144) * 128, lds + KA_SCR);
    }
#pragma unroll
    for (int i = 0; i < 5; ++i) {
      int ln = i * 64 + lane; ln = ln < 270 ? ln : 269;
      const int kr = (ln / 15) * 32 + jj, sl = ln % 15, seg = sl / 5, l5 = sl % 5;
      touch4((const char*)wqkv + (size_t)kr * 3456 + seg * 1152 + ((xcd & 1) * 4 + l5) * 128, lds + KA_SCR);
    }
    WAITL0();
    BAR(); BAR(); BAR(); BAR(); BAR(); BAR(); BAR();
    u32x4 wv[3][2];
#pragma unroll
    for (int i = 0; i < 3; ++i) {
      const int row = bx + 256 * i < D_ ? bx + 256 * i : bx;
      const char* rp = (const char*)wo + (size_t)row * (D_ * 2);
      wv[i][0] = *(const u32x4*)(rp + lane * 16);
      wv[i][1] = *(const u32x4*)(rp + 1024 + (lane & 7) * 16);
    }
    asm volatile("" ::: "memory");
    touch_code(CODE_ADDR("kB"), CODE_ADDR("kC"), lane, lds + KA_SCR);
#pragma unroll
    for (int i = 0; i < 3; ++i) {
      int ln = i * 64 + lane; ln = ln < 162 ? ln : 161;
      touch4((const char*)wo + (size_t)(jj * 162 + ln) * 128, lds + KA_SCR);
    }
#pragma unroll
    for (int i = 0; i < 6; ++i) {
      int ln = i * 64 + lane; ln = ln < 324 ? ln : 323;
      const int kr = (ln / 18) * 32 + jj;
      touch4((const char*)((xcd & 1) ? wu : wg) + (size_t)kr * (F_ * 2) + (xcd >> 1) * 2304 + (ln % 18) * 128, lds + KA_SCR);
    }
    BAR(); BAR(); BAR(); BAR();
    asm volatile("" : "+v"(wv[0][0]), "+v"(wv[0][1]), "+v"(wv[1][0]), "+v"(wv[1][1]), "+v"(wv[2][0]), "+v"(wv[2][1]) :: "memory");
#pragma unroll
    for (int i = 0; i < 3; ++i) {
      float t = 0.f, u = 0.f;
#pragma unroll
      for (int e = 0; e < 4; ++e) { t = dot2bf(wv[i][0][e], 0x3f803f80u, t); u = dot2bf(wv[i][1][e], 0x3f803f80u, u); }
      t += lane < 8 ? u : 0.f;
#pragma unroll
      for (int o = 32; o; o >>= 1) t += __shfl_xor(t, o);
      t *= (1.0f / D_);
      const float hi = rbf(t);
      if (lane == 0 && bx + 256 * i < D_) *(u32x2*)(ws + WS_WM + (size_t)(bx + 256 * i) * 8) = (u32x2){pk_bf16(hi, t - hi), 0u};
    }
    asm volatile("" ::: "memory");
    BAR();
    STAMP(9); STAMP_OUT(0);
    return;
  }
  const char* src[5];
#pragma unroll
  for (int i = 0; i < 3; ++i) {
    if (i < 2 || w < 2) {
      const int lr = (i * 8 + w) * 8 + (lane >> 3);
      int row = mt * 128 - 16 + lr; row = row < 0 ? 0 : row;
      src[i] = (const char*)x + (size_t)row * (D_ * 2) + (((lane & 7) ^ ((lr >> 1) & 7)) * 16);
      if (i < 2) KA_G(i, 0);
    }
  }
  {
    const char* wbase = (const char*)wqkv + ((72 * hp) & ~15);
#pragma unroll
    for (int i = 2; i < 5; ++i) {
      if (i > 2 || w >= 2) {
        const int sl = i < 4 ? i * 8 + w : 32;
        const int P = (sl - 18) * 64 + lane, pr = (P * 4370) >> 16, ch = P - pr * 15, seg = (ch * 13) >> 6, c5 = ch - seg * 5;
        const int p16 = pr & 15, k = (pr & ~15) + 4 * (p16 & 3) + (p16 >> 2);
        src[i] = wbase + (unsigned)(k * 3456 + seg * 1152 + c5 * 16);
      }
    }
  }
  const bool five = (w == 7);
  KA_G(2, 0); KA_G(3, 0); if (five) { glds16(src[4], lds + 32 * 1024); src[4] += 64 * 3456; }
#pragma unroll
  for (int st = 1; st < 3; ++st) { KA_G(0, st); KA_G(1, st); KA_G(2, st); KA_G(3, st); if (five) { glds16(src[4], lds + st * KA_STAGE + 32 * 1024); src[4] += 64 * 3456; } }
  STAMP(1);
  if (bx == 0 && tid < 256) *(unsigned*)(ws + WS_CNT + (size_t)tid * 256) = 0u;
  const int cb = w & 3, tg = w >> 2, hh = lane >> 5, r32 = lane & 31;
  int tl[3];
  unsigned xad[3], xmul[3];
#pragma unroll
  for (int i = 0; i < 3; ++i) {
    tl[i] = tg ? 3 + ((i + cb) & 1) : (i + cb) % 3;
    const int lr = tl[i] ? tl[i] * 32 - 16 + r32 : (r32 & 15);
    const bool one = tl[i] == 0 && r32 == 0;
    xad[i] = one ? (unsigned)KA_ONES : (unsigned)(lr * 128 + ((hh ^ ((lr >> 1) & 7)) * 16));
    xmul[i] = one ? 0u : 0xffffffffu;
  }
  unsigned wad, wmul;
  {
    const int pc = 8 * cb + 4 * ((lane >> 4) & 1) + (lane & 3), q = (lane & 15) >> 2;
    const int seg = (pc * 57) >> 9, wi = pc - seg * 9;
    const bool one = pc > 26;
    wad = one ? (unsigned)KA_ONES : (unsigned)(KA_XB + (4 * q + 2 * hh) * KA_WROW + seg * 80 + ((72 * hp) & 15) + wi * 8);
    wmul = one ? 0u : 0xffffffffu;
  }
  unsigned sto[4][2];
#pragma unroll
  for (int G = 0; G < 4; ++G) {
    const int pd = 8 * cb + 2 * G + hh, sg = (pd * 57) >> 9, c0 = (pd - sg * 9) * 4;
#pragma unroll
    for (int e = 0; e < 2; ++e) {
      const int c = c0 + 2 * e, hd = c >= 18 ? 1 : 0, d = c - 18 * hd;
      sto[G][e] = (unsigned)(pd < 27 ? (sg * 2 + hd) * 48 + d * 2 : 288 + 4 * e);
    }
  }
  f32x16 acc[3];
#pragma unroll
  for (int a = 0; a < 3; ++a)
#pragma unroll
    for (int i = 0; i < 16; ++i) acc[a][i] = 0.f;
  float sq = 0.f;
  if (five) WAITVL(10); else WAITVL(8);
  BAR(); asm volatile("" ::: "memory");
  STAMP(2);
  if (tg == 0) kA_loop<3, 4>(lds, src, w, xad, xmul, wad, wmul, acc, sq);
  else if (five) kA_loop<2, 5>(lds, src, w, xad, xmul, wad, wmul, acc, sq);
  else kA_loop<2, 4>(lds, src, w, xad, xmul, wad, wmul, acc, sq);
  STAMP(3);
  {
    sq += __shfl_xor(sq, 32);
    if (hh == 0) *(LAS float*)(lds + KA_SQ + (tl[0] * 32 + r32) * 4) = sq;
#pragma unroll
    for (int i = 0; i < 3; ++i) {
      if (i == 2 && tg) break;
      if (cb == 3 && hh == 0) *(LAS float*)(lds + KA_SUM + (tl[i] * 32 + r32) * 4) = acc[i][8];
      if (tl[i] == 0 && r32 == 0) {
#pragma unroll
        for (int G = 0; G < 4; ++G) *(LAS f32x4*)(lds + KA_CST + (cb * 32 + G * 8 + hh * 4) * 4) = (f32x4){acc[i][4 * G], acc[i][4 * G + 1], acc[i][4 * G + 2], acc[i][4 * G + 3]};
      }
    }
  }
  WAITL0();
  BAR(); asm volatile("" ::: "memory");
  STAMP(4);
  {
    f32x4 cs[4];
#pragma unroll
    for (int G = 0; G < 4; ++G) cs[G] = *(LAS f32x4*)(lds + KA_CST + (cb * 32 + G * 8 + hh * 4) * 4);
#pragma unroll
    for (int i = 0; i < 3; ++i) {
      if (i == 2 && tg) break;
      const float sm = *(LAS float*)(lds + KA_SUM + (tl[i] * 32 + r32) * 4), s2 = *(LAS float*)(lds + KA_SQ + (tl[i] * 32 + r32) * 4);
      const float mean = sm * (1.0f / D_);
      const float var = rbf(fmaxf(s2 * (1.0f / D_) - mean * mean, 0.f));
      const float rs = __builtin_amdgcn_rcpf(rbf(__builtin_amdgcn_sqrtf(rbf(var + 1e-5f))));
      const float mrs = -rbf(mean) * rs;
      if (hp == 0 && cb == 0 && hh == 0 && tl[i]) *(float*)(ws + WS_XM + (size_t)(mt * 128 + tl[i] * 32 - 32 + r32) * 4) = mean;
      const int lr = tl[i] ? tl[i] * 32 - 16 + r32 : (r32 & 15);
      LAS char* rowp = lds + lr * KA_QROW;
#pragma unroll
      for (int G = 0; G < 4; ++G) {
        const f32x4 a = {acc[i][4 * G], acc[i][4 * G + 1], acc[i][4 * G + 2], acc[i][4 * G + 3]};
        const f32x4 v = a * rs + cs[G] * mrs;
        *(LAS unsigned*)(rowp + sto[G][0]) = pk_bf16(v[0], v[1]); *(LAS unsigned*)(rowp + sto[G][1]) = pk_bf16(v[2], v[3]);
      }
    }
  }
  WAITL0();
  BAR(); asm volatile("" ::: "memory");
  STAMP(5);
  {
    const int hf = tid & 1, t = (tid >> 1) & 127, hd = tid >> 8, T = mt * 128 + t;
    LAS char* base = lds + (t + 16) * KA_QROW + hd * 48;
    const u32x4 qa = *(LAS u32x4*)(base), qb = *(LAS u32x4*)(base + 16); const unsigned qc = *(LAS unsigned*)(base + 32);
    float lg[8];
#pragma unroll
    for (int i = 0; i < 4; ++i) {
      const int wv = hf * 4 + i;
      LAS char* kp = base - wv * 2 * KA_QROW + 96;
      const u32x4 a = *(LAS u32x4*)(kp), b = *(LAS u32x4*)(kp + 16); const unsigned c = *(LAS unsigned*)(kp + 32);
      float s = 0.f;
      s = dot2bf(qa[0], a[0], s); s = dot2bf(qa[1], a[1], s); s = dot2bf(qa[2], a[2], s); s = dot2bf(qa[3], a[3], s);
      s = dot2bf(qb[0], b[0], s); s = dot2bf(qb[1], b[1], s); s = dot2bf(qb[2], b[2], s); s = dot2bf(qb[3], b[3], s);
      s = dot2bf(qc, c, s);
      s = rbf(rbf(s) * 0.23570226039551584f);
      s = (T - 2 * wv >= 0) ? s : -1.0e9f;
      const float o = __builtin_bit_cast(float, __builtin_amdgcn_mov_dpp(__builtin_bit_cast(int, s), 0xB1, 0xF, 0xF, true));
      lg[i] = hf ? o : s; lg[4 + i] = hf ? s : o;
    }
    const float mx = fmaxf(fmaxf(fmaxf(lg[0], lg[1]), fmaxf(lg[2], lg[3])), fmaxf(fmaxf(lg[4], lg[5]), fmaxf(lg[6], lg[7])));
    float sm = 0.f;
#pragma unroll
    for (int wv = 0; wv < 8; ++wv) { lg[wv] = __builtin_amdgcn_exp2f((lg[wv] - mx) * 1.4426950408889634f); sm += lg[wv]; }
    const float inv = 1.0f / sm;
    float o[10];
#pragma unroll
    for (int d = 0; d < 10; ++d) o[d] = 0.f;
#pragma unroll
    for (int wp = 0; wp < 4; ++wp) {
      const unsigned pp = pk_bf16(lg[2 * wp] * inv, lg[2 * wp + 1] * inv);
      LAS char* v0 = base - (2 * wp) * 2 * KA_QROW + 192 + hf * 16;
      LAS char* v1 = v0 - 2 * KA_QROW;
      const u32x4 a = *(LAS u32x4*)(v0), b = *(LAS u32x4*)(v1); const unsigned a4 = *(LAS unsigned*)(v0 + 16), b4 = *(LAS unsigned*)(v1 + 16);
      const unsigned av[5] = {a[0], a[1], a[2], a[3], a4}, bv[5] = {b[0], b[1], b[2], b[3], b4};
#pragma unroll
      for (int i = 0; i < 5; ++i) {
        o[2 * i]     = dot2bf(pp, __builtin_amdgcn_perm(bv[i], av[i], 0x05040100u), o[2 * i]);
        o[2 * i + 1] = dot2bf(pp, __builtin_amdgcn_perm(bv[i], av[i], 0x07060302u), o[2 * i + 1]);
      }
    }
    LAS char* op = lds + KA_OOFF + t * 80 + hd * 36 + hf * 16;
#pragma unroll
    for (int i = 0; i < 5; ++i) *(LAS unsigned*)(op + 4 * i) = pk_bf16(o[2 * i], o[2 * i + 1]);
  }
  WAITL0();
  BAR(); asm volatile("" ::: "memory");
  STAMP(8);
#pragma unroll
  for (int i = 0; i < 3; ++i) {
    const int id = tid + i * 512;
    if (id < 1152) {
      const int pc = id >> 7, t = id & 127;
      const u32x2 v = *(LAS u32x2*)(lds + KA_OOFF + t * 80 + pc * 8);
      *(u32x2*)(ws + WS_O + fm32_piece(mt * 128 + t, hp * 36 + pc * 4, 36)) = v;
    }
  }
  STAMP(9); STAMP_OUT(0);
}

constexpr int KB_WM = 576 * 128;
constexpr int KB_RED = KB_WM + 5120;
constexpr int KB_SCR = KB_RED + 25600;
constexpr int KB_LDS = KB_SCR + 256;

extern "C" __global__ __launch_bounds__(768, 1) void kB(const u16* __restrict__ x, const u16* __restrict__ wo, const u16* __restrict__ wg,
                                             const u16* __restrict__ wu, char* __restrict__ ws) {
  extern __shared__ __attribute__((aligned(16))) char smem[];
  LAS char* lds = (LAS char*)smem;
  const int tid = threadIdx.x, lane = tid & 63, w = __builtin_amdgcn_readfirstlane(tid >> 6);
  const int bx = blockIdx.x, xcd = bx & 7, jj = bx >> 3;
  const int unit = xcd * 30 + jj;
  STAMP_DECL STAMP(0);
  if (jj >= 30 || unit >= 234) {
    return;
  }
  const int mt = (unit * 7282) >> 16, nt = unit - mt * 9;
  const int g = lane >> 4, r = lane & 15;
  if (w >= 10) {
    const int lw = w - 10, kr = lane >> 3;
    const char* wp = (const char*)wo + nt * 128 + (unsigned)((lw * 8 + kr) * (D_ * 2) + (((lane & 7) ^ (((kr >> 1) & 3) << 1)) * 16));
    { const int i = lw * 3;
#pragma unroll
      for (int q = 0; q < 3; ++q) if (i + q < 5) { const int o = (i + q) * 1024 + lane * 16; glds16(ws + WS_WM + (o < 4608 ? o : 0), lds + KB_WM + (i + q) * 1024); } }
#pragma unroll
    for (int i = 0; i < 12; ++i) glds16(wp + (size_t)i * (16 * D_ * 2), lds + (i * 2 + lw) * 1024);
    BAR();
#pragma unroll
    for (int i = 12; i < 24; ++i) glds16(wp + (size_t)i * (16 * D_ * 2), lds + (i * 2 + lw) * 1024);
    WAITV(12); BAR();
#pragma unroll
    for (int i = 24; i < 36; ++i) glds16(wp + (size_t)i * (16 * D_ * 2), lds + (i * 2 + lw) * 1024);
    WAITV(12); BAR();
    WAITV(0); BAR();
    if (lw == 0) touch_code(CODE_ADDR("kC"), CODE_ADDR("kD"), lane, lds + KB_SCR);
    {
#pragma unroll
      for (int i = 0; i < 3; ++i) {
        int ln = (jj * 2 + lw) * 173 + i * 64 + lane; const int lim = (jj * 2 + lw + 1) * 173; ln = ln < lim ? ln : lim - 1; ln = ln < 10368 ? ln : 10367;
        const int kr2 = (ln * 3641) >> 16;
        touch4((const char*)((xcd & 1) ? wg : wu) + (size_t)kr2 * (F_ * 2) + (xcd >> 1) * 2304 + (ln - kr2 * 18) * 128, lds + KB_SCR);
      }
    }
    BAR();
    STAMP(7); STAMP_OUT(1);
    return;
  }
  const int rb = w < 5 ? w : w - 5, kp = w < 5 ? 0 : 1;
  int tok = mt * 80 + rb * 16 + r; const bool valid = tok < S_; tok = valid ? tok : S_ - 1;
  const char* op = ws + WS_O + (size_t)((tok >> 5) * 36 + (g >> 1) + 2 * kp) * 1024 + ((g & 1) * 32 + (tok & 31)) * 16;
  u32x4 of[9];
#pragma unroll
  for (int i = 0; i < 3; ++i) of[i] = *(const u32x4*)(op + i * 4096);
  asm volatile("" ::: "memory");
  BAR();
  asm volatile("" ::: "memory");
#pragma unroll
  for (int i = 3; i < 6; ++i) of[i] = *(const u32x4*)(op + i * 4096);
  asm volatile("" ::: "memory");
  u32x2 xres[4]; float xm = 0.f;
  STAMP(1);
  f32x4 acc[5];
#pragma unroll
  for (int b = 0; b < 5; ++b) acc[b] = (f32x4){0.f, 0.f, 0.f, 0.f};
  const int krow = 32 * kp + 16 * (g >> 1) + 4 * (g & 1) + (r >> 2);
  unsigned adr[4];
#pragma unroll
  for (int b = 0; b < 4; ++b) adr[b] = (unsigned)(krow * 128 + ((b ^ ((krow >> 1) & 3)) * 32) + (r & 3) * 8);
  const unsigned adrm = (unsigned)(KB_WM + krow * 8);
#pragma unroll
  for (int c = 0; c < 3; ++c) {
    BAR();
    asm volatile("" ::: "memory");
    STAMP(2 + c);
    if (c == 0) {
#pragma unroll
      for (int i = 6; i < 9; ++i) of[i] = *(const u32x4*)(op + i * 4096);
      const char* xp = (const char*)x + ((size_t)tok * D_ + nt * 64 + 4 * g) * 2;
#pragma unroll
      for (int b = 0; b < 4; ++b) xres[b] = *(const u32x2*)(xp + b * 32);
      xm = *(const float*)(ws + WS_XM + (size_t)tok * 4);
      asm volatile("" ::: "memory");
    }
    u32x4 wf[3][5];
#pragma unroll
    for (int s3 = 0; s3 < 3; ++s3) {
#pragma unroll
      for (int b = 0; b < 4; ++b) wf[s3][b] = tr_frag(lds + adr[b] + c * (192 * 128) + s3 * (64 * 128), 8 * 128);
      wf[s3][4] = tr_frag(lds + adrm + c * (192 * 8) + s3 * (64 * 8), 64);
    }
#pragma unroll
    for (int s3 = 0; s3 < 3; ++s3)
#pragma unroll
      for (int b = 0; b < 5; ++b) acc[b] = MFMA16(BF8(wf[s3][b]), BF8(of[c * 3 + s3]), acc[b]);
  }
  STAMP(5);
  if (kp == 1) {
#pragma unroll
    for (int b = 0; b < 5; ++b) *(LAS f32x4*)(lds + KB_RED + (rb * 5 + b) * 1024 + lane * 16) = acc[b];
  }
  WAITL0();
  BAR();
  asm volatile("" ::: "memory");
  if (kp == 0) {
    const f32x4 mv = acc[4] + *(LAS f32x4*)(lds + KB_RED + (rb * 5 + 4) * 1024 + lane * 16);
    const float mean = xm + (mv[0] + mv[1]), mb = rbf(mean);
    f32x4 qv = {0.f, 0.f, 0.f, 0.f};
    u32x2 hp2[4], cp2[4];
#pragma unroll
    for (int b = 0; b < 4; ++b) {
      const f32x4 v = acc[b] + *(LAS f32x4*)(lds + KB_RED + (rb * 5 + b) * 1024 + lane * 16);
      const unsigned a0 = pk_bf16(v[0], v[1]), a1 = pk_bf16(v[2], v[3]);
      const unsigned h0 = pk_bf16(bf_lo(xres[b][0]) + bf_lo(a0), bf_hi(xres[b][0]) + bf_hi(a0));
      const unsigned h1 = pk_bf16(bf_lo(xres[b][1]) + bf_lo(a1), bf_hi(xres[b][1]) + bf_hi(a1));
      const f32x4 hv = {bf_lo(h0), bf_hi(h0), bf_lo(h1), bf_hi(h1)};
      const f32x4 d = hv - mean, dc = hv - mb;
      qv += d * d;
      hp2[b] = (u32x2){h0, h1}; cp2[b] = (u32x2){pk_bf16(dc[0], dc[1]), pk_bf16(dc[2], dc[3])};
    }
    const unsigned ho = (unsigned)(((tok >> 5) * 36 + nt * 4 + (g >> 1)) * 1024 + ((g & 1) * 32 + (tok & 31)) * 16);
#pragma unroll
    for (int b = 0; b < 4; b += 2) {
      u32x4 hq, cq;
#pragma unroll
      for (int e = 0; e < 2; ++e) {
        const auto sh = __builtin_amdgcn_permlane32_swap(hp2[b][e], hp2[b + 1][e], false, false);
        const auto sc = __builtin_amdgcn_permlane32_swap(cp2[b][e], cp2[b + 1][e], false, false);
        hq[e] = sh[0]; hq[2 + e] = sh[1]; cq[e] = sc[0]; cq[2 + e] = sc[1];
      }
      if (valid) { *(u32x4*)(ws + WS_H + ho + b * 1024) = hq; *(u32x4*)(ws + WS_HC + ho + b * 1024) = cq; }
    }
    float q = (qv[0] + qv[1]) + (qv[2] + qv[3]);
    q += __shfl_xor(q, 16); q += __shfl_xor(q, 32);
    if (g == 0 && valid) *(float*)(ws + WS_ST + ((size_t)tok * 12 + nt) * 4) = q;
  }
  STAMP(7); STAMP_OUT(1);
}

constexpr int KC_UPOFF = 16384 + 9216 + 128;
constexpr int KC_STAGE = 35072;
constexpr int KC_STOFF = 4 * KC_STAGE;
constexpr int KC_SCR = KC_STOFF + 12288;
constexpr int KC_LDS = KC_SCR + 256;

DI f32x2 geglu2(f32x2 a, f32x2 b, f32x2 k0, f32x2 k1, f32x2 ik2) {
  const f32x2 tt = a * __builtin_elementwise_fma(a * a, k1, k0);
  const f32x2 e = {__builtin_amdgcn_exp2f(tt[0]), __builtin_amdgcn_exp2f(tt[1])};
  const f32x2 d = __builtin_elementwise_fma(e, ik2, ik2);
  const f32x2 rr = {__builtin_amdgcn_rcpf(d[0]), __builtin_amdgcn_rcpf(d[1])};
  return (a * b) * rr;
}
DI void ln_consts(const LAS char* sp_, f32x2& k0, f32x2& k1, f32x2& ik2) {
  const LAS f32x4* sp = (const LAS f32x4*)sp_;
  const f32x4 s0 = sp[0], s1 = sp[1]; const float s8 = *(const LAS float*)(sp + 2);
  const float var = rbf((((s0[0] + s0[1]) + (s0[2] + s0[3])) + ((s1[0] + s1[1]) + (s1[2] + s1[3])) + s8) * (1.0f / D_));
  const float sd = rbf(__builtin_amdgcn_sqrtf(rbf(var + 1e-5f)));
  const float rs = __builtin_amdgcn_rcpf(sd);
  const float k0s = -2.3022081981f * rs;
  const float k1s = -0.1029432397f * rs * rs * rs;
  const float ik2s = sd * sd;
  k0 = (f32x2){k0s, k0s}; k1 = (f32x2){k1s, k1s}; ik2 = (f32x2){ik2s, ik2s};
}

extern "C" __global__ __launch_bounds__(768, 1) void kC(const u16* __restrict__ wg, const u16* __restrict__ wu, const u16* __restrict__ wd, char* __restrict__ ws) {
  extern __shared__ __attribute__((aligned(16))) char smem[];
  LAS char* lds = (LAS char*)smem;
  const int tid = threadIdx.x, lane = tid & 63, w = __builtin_amdgcn_readfirstlane(tid >> 6);
  const int bx = blockIdx.x, xcd = bx & 7, jj = bx >> 3;
  const int pm = (xcd & 1) * 4 + (jj & 3), pn = (xcd >> 1) * 8 + (jj >> 2);
  const int wm = w & 3, wn = w >> 2;

  STAMP_DECL STAMP(0);
  const char* src[3]; unsigned dst[3]; unsigned step[3];
  src[0] = ws + WS_HC + (size_t)((pm * 8 + (w >> 1)) * 36 + (w & 1)) * 1024 + lane * 16; dst[0] = w * 1024; step[0] = 2048;
#define KC_G(i, st) do { glds16(src[i], lds + ((st) & 3) * KC_STAGE + dst[i]); src[i] += step[i]; } while (0)
  KC_G(0, 0);
#pragma unroll
  for (int i = 1; i < 3; ++i) {
    int sl = i * 12 + w; sl = sl > 33 ? 33 : sl;
    if (sl < 16) {
      src[i] = ws + WS_HC + (size_t)((pm * 8 + (sl >> 1)) * 36 + (sl & 1)) * 1024 + lane * 16;
      dst[i] = sl * 1024; step[i] = 2048;
    } else {
      const int c = sl - 16, m = c >= 9 ? 1 : 0, cc = c - 9 * m, q = cc * 64 + lane, rr = (q * 3641) >> 16, ch = q - rr * 18;
      src[i] = (const char*)(m ? wu : wg) + pn * 288 + (unsigned)(rr * (F_ * 2) + ch * 16);
      dst[i] = (m ? KC_UPOFF : 16384) + cc * 1024; step[i] = 32 * F_ * 2;
    }
    KC_G(i, 0);
  }
#define KC_ISSUE(st) do { KC_G(0, st); KC_G(1, st); KC_G(2, st); } while (0)
  KC_ISSUE(1); KC_ISSUE(2);
  glds16(ws + WS_ST + (size_t)pm * 12288 + w * 1024 + lane * 16, lds + KC_STOFF + w * 1024);
  const auto ars = __builtin_amdgcn_make_buffer_rsrc((void*)(ws + WS_ACT), 0, S_ * F_ * 2, 0x00020000);
#define KC_TOUCH_WD() do { if (w < 6) {     \
    int ln = w * 64 + lane; ln = ln < 324 ? ln : 323; \
    touch4((const char*)wd + (size_t)(xcd >> 1) * (1152 * D_ * 2) + (size_t)(jj * 324 + ln) * 128, lds + KC_SCR); } \
    else if (w == 6) touch_code(CODE_ADDR("kD"), CODE_ADDR("kD_end_lbl"), lane, lds + KC_SCR); } while (0)

  const int g = lane >> 4, r = lane & 15;
  f32x4 acc[6][4];
#pragma unroll
  for (int a = 0; a < 6; ++a)
#pragma unroll
    for (int b = 0; b < 4; ++b) acc[a][b] = (f32x4){0.f, 0.f, 0.f, 0.f};
  const unsigned xadr = (unsigned)(wm * 4096 + (g >> 1) * 1024 + ((g & 1) * 32 + r) * 16);
  const unsigned wadr = (unsigned)(16384 + (16 * (g >> 1) + 4 * (g & 1) + (r >> 2)) * 288 + wn * 96 + (r & 3) * 8);
  u32x4 xf[4], wA[3], wB[3];
#define KC_WOFF(j) ((((j) & 1) ? (KC_UPOFF - 16384) : 0) + ((j) >> 1) * 32)
#define KC_RW(WF, i, j, base) WF[i] = tr_frag((base) + wadr + KC_WOFF(j), 2304)
#define KC_RX(c, base) xf[c] = *(LAS u32x4*)((base) + xadr + ((c) >> 1) * 2048 + ((c) & 1) * 256)
#define KC_M3(WF, j0, c) do { acc[j0][c] = MFMA16(BF8(WF[0]), BF8(xf[c]), acc[j0][c]); acc[j0 + 1][c] = MFMA16(BF8(WF[1]), BF8(xf[c]), acc[j0 + 1][c]); \
    acc[j0 + 2][c] = MFMA16(BF8(WF[2]), BF8(xf[c]), acc[j0 + 2][c]); } while (0)
#define KC_HALFA(base, G0, G1) do { \
    KC_M3(wA, 0, 0); SCHED0(); KC_RW(wB, 0, 3, base); SCHED0(); \
    KC_M3(wA, 0, 1); SCHED0(); KC_RW(wB, 1, 4, base); G0; SCHED0(); \
    KC_M3(wA, 0, 2); SCHED0(); KC_RW(wB, 2, 5, base); SCHED0(); \
    KC_M3(wA, 0, 3); SCHED0(); G1; SCHED0(); } while (0)
#define KC_HALFB(nb, G0) do { \
    KC_M3(wB, 3, 0); SCHED0(); KC_RX(0, nb); KC_RW(wA, 0, 0, nb); SCHED0(); \
    KC_M3(wB, 3, 1); SCHED0(); KC_RX(1, nb); KC_RW(wA, 1, 1, nb); SCHED0(); \
    KC_M3(wB, 3, 2); SCHED0(); KC_RX(2, nb); KC_RW(wA, 2, 2, nb); G0; SCHED0(); \
    KC_M3(wB, 3, 3); SCHED0(); KC_RX(3, nb); SCHED0(); } while (0)
  STAMP(1);
  WAITV(7); BAR(); asm volatile("" ::: "memory");
  STAMP(2);
  KC_RX(0, lds); KC_RW(wA, 0, 0, lds); KC_RW(wA, 1, 1, lds); KC_RW(wA, 2, 2, lds); KC_RX(1, lds); KC_RX(2, lds); KC_RX(3, lds);
  KC_ISSUE(3);
  KC_HALFA(lds, (void)0, (void)0);
  STAMP(3);
#pragma unroll 1
  for (int t = 1; t < 15; ++t) {
    WAITVL(6);
    BAR(); asm volatile("" ::: "memory");
    LAS char* sb = lds + (t & 3) * KC_STAGE;
    KC_HALFB(sb, KC_G(0, t + 3));
    KC_HALFA(sb, KC_G(1, t + 3), KC_G(2, t + 3));
  }
  WAITVL(0);
  BAR(); asm volatile("" ::: "memory");
  KC_TOUCH_WD();
#pragma unroll 1
  for (int t = 15; t < 18; ++t) {
    LAS char* sb = lds + (t & 3) * KC_STAGE;
    KC_HALFB(sb, (void)0);
    KC_HALFA(sb, (void)0, (void)0);
  }
  KC_M3(wB, 3, 0); KC_M3(wB, 3, 1); KC_M3(wB, 3, 2); KC_M3(wB, 3, 3);
  STAMP(4);
#pragma unroll
  for (int cp = 0; cp < 2; ++cp) {
    f32x2 k0[2], k1[2], ik2[2];
#pragma unroll
    for (int e = 0; e < 2; ++e) ln_consts(lds + KC_STOFF + (wm * 64 + cp * 32 + e * 16 + r) * 48, k0[e], k1[e], ik2[e]);
#pragma unroll
    for (int f = 0; f < 3; ++f) {
      u32x2 pc[2];
#pragma unroll
      for (int e = 0; e < 2; ++e) {
        const f32x4 a = acc[2 * f][2 * cp + e], b = acc[2 * f + 1][2 * cp + e];
        const f32x2 o0 = geglu2((f32x2){a[0], a[1]}, (f32x2){b[0], b[1]}, k0[e], k1[e], ik2[e]);
        const f32x2 o1 = geglu2((f32x2){a[2], a[3]}, (f32x2){b[2], b[3]}, k0[e], k1[e], ik2[e]);
        pc[e] = (u32x2){pk_bf16(o0[0], o0[1]), pk_bf16(o1[0], o1[1])};
      }
      const auto s0 = __builtin_amdgcn_permlane32_swap(pc[0][0], pc[1][0], false, false);
      const auto s1 = __builtin_amdgcn_permlane32_swap(pc[0][1], pc[1][1], false, false);
      const u32x4 v = {s0[0], s1[0], s0[1], s1[1]};
      __builtin_amdgcn_raw_buffer_store_b128(v, ars, ((pm * 8 + wm * 2 + cp) * 288 + pn * 9 + wn * 3 + f) * 1024 + ((g & 1) * 32 + (g >> 1) * 16 + r) * 16, 0, 16);
    }
  }
  STAMP(5); STAMP_OUT(2);
}

constexpr int KD_STAGE = 16384 + 18432;
constexpr int KD_SCR = 4 * KD_STAGE;
constexpr int KD_LDS = KD_SCR + 256;

template <int NB, int NG>
DI void kD_loop(LAS char* lds, const char* srcA, const char* const (&srcW)[3], int w, int lane, f32x16 (&acc)[3]) {
  const int tt = w & 3, hh = lane >> 5;
#define KD_GA(i, st) glds16(srcA + (size_t)(i) * (2 * 288 * 1024) + (st) * 4096, lds + ((st) & 3) * KD_STAGE + (i) * 8192 + w * 1024)
#define KD_GW(i, st) glds16(srcW[i] + (size_t)(st) * (64 * D_ * 2), lds + ((st) & 3) * KD_STAGE + 16384 + (i) * 8192 + w * 1024)
#define KD_GW2(st) do { if (NG == 5) glds16(srcW[2] + (size_t)(st) * (64 * D_ * 2), lds + ((st) & 3) * KD_STAGE + 32768 + (w & 1) * 1024); } while (0)
#define KD_WAIT2() do { if (NG == 5) WAITVL(10); else WAITVL(8); } while (0)
  const unsigned xadr = (unsigned)(tt * 4096 + lane * 16);
  const unsigned wrow = (unsigned)(16384 + (4 * hh + ((lane & 15) >> 2)) * 288 + (lane & 3) * 8);
  const unsigned wadr0 = wrow + ((lane >> 4) & 1) * 128 + (NB == 3 ? 0 : 96);
  const unsigned wadr1 = NB == 3 ? wadr0 + 32 : wrow + 256;
  u32x4 xP[2], wP[2][3], xQ[2], wQ[2][3];
#define KD_RW(WF, f, ad, base, kk) WF[f] = tr_frag((base) + (ad) + (kk) * 4608, 2304)
#define KD_RX(XF, base, kk) XF = *(LAS u32x4*)((base) + xadr + (kk) * 1024)
#define KD_STEP(XC, WC, XN, WN, base, kk, G0) do { \
    acc[0] = MFMA32(BF8(WC[0]), BF8(XC), acc[0]); SCHED0(); KD_RW(WN, 0, wadr0, base, kk); KD_RX(XN, base, kk); SCHED0(); \
    acc[1] = MFMA32(BF8(WC[1]), BF8(XC), acc[1]); SCHED0(); KD_RW(WN, 1, wadr1, base, kk); G0; SCHED0(); \
    if (NB == 3) { acc[2] = MFMA32(BF8(WC[2]), BF8(XC), acc[2]); SCHED0(); KD_RW(WN, 2, wadr0 + 64, base, kk); SCHED0(); } } while (0)
#define KD_HALF(XC, WC, XN, WN, base, hk, G0, G1) do { KD_STEP(XC[0], WC[0], XN[0], WN[0], base, 2 * (hk), G0); KD_STEP(XC[1], WC[1], XN[1], WN[1], base, 2 * (hk) + 1, G1); } while (0)
#pragma unroll
  for (int kk = 0; kk < 2; ++kk) { KD_RW(wP[kk], 0, wadr0, lds, kk); KD_RX(xP[kk], lds, kk); KD_RW(wP[kk], 1, wadr1, lds, kk); if (NB == 3) KD_RW(wP[kk], 2, wadr0 + 64, lds, kk); }
  KD_GA(0, 3); KD_GA(1, 3); KD_GW(0, 3); KD_GW(1, 3); KD_GW2(3);
  KD_HALF(xP, wP, xQ, wQ, lds, 1, (void)0, (void)0);
#pragma unroll 1
  for (int t = 1; t < 15; ++t) {
    KD_WAIT2();
    BAR(); asm volatile("" ::: "memory");
    LAS char* sb = lds + (t & 3) * KD_STAGE;
    KD_HALF(xQ, wQ, xP, wP, sb, 0, do { KD_GA(0, t + 3); KD_GW2(t + 3); } while (0), KD_GA(1, t + 3));
    KD_HALF(xP, wP, xQ, wQ, sb, 1, KD_GW(0, t + 3), KD_GW(1, t + 3));
  }
#pragma unroll 1
  for (int t = 15; t < 18; ++t) {
    if (t == 15) KD_WAIT2(); else if (t == 16) { if (NG == 5) WAITVL(5); else WAITVL(4); } else WAITVL(0);
    BAR(); asm volatile("" ::: "memory");
    LAS char* sb = lds + (t & 3) * KD_STAGE;
    KD_HALF(xQ, wQ, xP, wP, sb, 0, (void)0, (void)0);
    KD_HALF(xP, wP, xQ, wQ, sb, 1, (void)0, (void)0);
  }
#pragma unroll
  for (int kk = 0; kk < 2; ++kk) {
    acc[0] = MFMA32(BF8(wQ[kk][0]), BF8(xQ[kk]), acc[0]); acc[1] = MFMA32(BF8(wQ[kk][1]), BF8(xQ[kk]), acc[1]);
    if (NB == 3) acc[2] = MFMA32(BF8(wQ[kk][2]), BF8(xQ[kk]), acc[2]);
  }
}

extern "C" __global__ __launch_bounds__(512, 1) void kD(const u16* __restrict__ wd, char* __restrict__ ws, u16* __restrict__ out) {
  extern __shared__ __attribute__((aligned(16))) char smem[];
  LAS char* lds = (LAS char*)smem;
  const int tid = threadIdx.x, lane = tid & 63, w = __builtin_amdgcn_readfirstlane(tid >> 6);
  const int bx = blockIdx.x, xcd = bx & 7, jj = bx >> 3;
  const int ks = xcd >> 1, mt = (xcd & 1) * 8 + (jj & 7), nt = jj >> 3;
  const int tt = w & 3, cg = w >> 2;
  const int hh = lane >> 5;

  STAMP_DECL STAMP(0);
  const char* srcA = ws + WS_ACT + (size_t)((mt * 4 + (w >> 2)) * 288 + ks * 72 + (w & 3)) * 1024 + lane * 16;
  KD_GA(0, 0); KD_GA(1, 0);
  const char* srcW[3];
#pragma unroll
  for (int i = 0; i < 3; ++i) {
    const int cc = i < 2 ? i * 8 + w : 16 + (w & 1);
    const int q = cc * 64 + lane, rr = (q * 3641) >> 16, ch = q - rr * 18;
    srcW[i] = (const char*)wd + (size_t)(ks * 1152 * D_ + nt * 144) * 2 + (unsigned)(rr * (D_ * 2) + ch * 16);
  }
  const bool extra = (w == 4 || w == 5);
#define KD_GX(st) do { if (extra) glds16(srcW[2] + (size_t)(st) * (64 * D_ * 2), lds + ((st) & 3) * KD_STAGE + 32768 + (w & 1) * 1024); } while (0)
  KD_GW(0, 0); KD_GW(1, 0); KD_GX(0);
  KD_GA(0, 1); KD_GA(1, 1); KD_GW(0, 1); KD_GW(1, 1); KD_GX(1);
  KD_GA(0, 2); KD_GA(1, 2); KD_GW(0, 2); KD_GW(1, 2); KD_GX(2);
  const u32x4 hres0 = *(const u32x4*)(ws + WS_H + (size_t)((mt * 4 + ks) * 36 + nt * 9 + w) * 1024 + lane * 16);
  const u32x4 hres1 = *(const u32x4*)(ws + WS_H + (size_t)((mt * 4 + ks) * 36 + nt * 9 + 8) * 1024 + lane * 16);
  asm volatile("" ::: "memory");
  f32x16 acc[3];
#pragma unroll
  for (int a = 0; a < 3; ++a)
#pragma unroll
    for (int i = 0; i < 16; ++i) acc[a][i] = 0.f;
  STAMP(1);
  if (extra) WAITVL(10); else WAITVL(8);
  BAR(); asm volatile("" ::: "memory");
  STAMP(2);
  if (cg == 0) kD_loop<3, 4>(lds, srcA, srcW, w, lane, acc);
  else if (extra) kD_loop<2, 5>(lds, srcA, srcW, w, lane, acc);
  else kD_loop<2, 4>(lds, srcA, srcW, w, lane, acc);
  STAMP(3);
  const int tile = mt * 4 + nt;
  const auto prs = __builtin_amdgcn_make_buffer_rsrc((void*)(ws + WS_PART), 0, (int)(64 * 4 * 3 * PART_SLAB), 0x00020000);
  {
    const int sidx = ks - (ks > tt ? 1 : 0);
    const int po = (int)(((tile * 4 + tt) * 3 + sidx) * PART_SLAB) + lane * 16;
    LAS char* lo = lds + 2 * KD_STAGE + lane * 16;
#pragma unroll
    for (int a = 0; a < 3; ++a)
#pragma unroll
      for (int G = 0; G < 4; ++G) {
        const f32x4 v = {acc[a][4 * G], acc[a][4 * G + 1], acc[a][4 * G + 2], acc[a][4 * G + 3]};
        const int u0 = (a + 4 * (G >> 1)) * 2048 + (G & 1) * 1024;
        const int u1 = (a == 0 ? (3 + 4 * (G >> 1)) : 8) * 2048 + (G & 1) * 1024;
        const bool ok = cg == 0 || a == 0 || (a == 1 && G < 2);
        const int u = cg ? u1 : u0;
        if (ok) {
          if (tt != ks) __builtin_amdgcn_raw_buffer_store_b128(__builtin_bit_cast(u32x4, v), prs, po + u, 0, 16);
          else *(LAS f32x4*)(lo + u) = v;
        }
      }
  }
  STAMP(5);
  WAITVL(0);
  BAR(); asm volatile("" ::: "memory");
  STAMP(6);
  GAS unsigned* cnt = (GAS unsigned*)(ws + WS_CNT);
  if (tid < 4 && tid != ks) __hip_atomic_fetch_add(cnt + (tile * 4 + tid) * 64, 1u, __ATOMIC_RELAXED, __HIP_MEMORY_SCOPE_AGENT);
  if (w == 0) {
    unsigned spins = 0;
    while (__builtin_amdgcn_readfirstlane(__hip_atomic_load(cnt + (tile * 4 + ks) * 64, __ATOMIC_RELAXED, __HIP_MEMORY_SCOPE_AGENT)) < 3u) {
      if (++spins > 2000000u) break;
      __builtin_amdgcn_s_sleep(1);
    }
  }
  asm volatile("" ::: "memory");
  BAR(); asm volatile("" ::: "memory");
  STAMP(7);
#pragma unroll
  for (int i = 0; i < 2; ++i) {
    if (i == 1 && w != 1) break;
    const int grp = i ? 8 : w;
    const int po = (int)((tile * 4 + ks) * 3 * PART_SLAB) + grp * 2048 + lane * 16;
    u32x4 pp[3][2];
#pragma unroll
    for (int q = 0; q < 3; ++q) {
      pp[q][0] = __builtin_amdgcn_raw_buffer_load_b128(prs, po + q * (int)PART_SLAB, 0, 16);
      pp[q][1] = __builtin_amdgcn_raw_buffer_load_b128(prs, po + q * (int)PART_SLAB + 1024, 0, 16);
    }
    const u32x4 hr = i ? hres1 : hres0;
    u16* op = out + (size_t)(mt * 128 + ks * 32 + (lane & 31)) * D_ + nt * 144 + grp * 16 + 4 * hh;
#pragma unroll
    for (int hf = 0; hf < 2; ++hf) {
      f32x4 v = *(LAS f32x4*)(lds + 2 * KD_STAGE + grp * 2048 + hf * 1024 + lane * 16);
      v += __builtin_bit_cast(f32x4, pp[0][hf]); v += __builtin_bit_cast(f32x4, pp[1][hf]); v += __builtin_bit_cast(f32x4, pp[2][hf]);
      const unsigned a0 = pk_bf16(v[0], v[1]), a1 = pk_bf16(v[2], v[3]);
      const unsigned o0 = pk_bf16(bf_lo(hr[2 * hf]) + bf_lo(a0), bf_hi(hr[2 * hf]) + bf_hi(a0));
      const unsigned o1 = pk_bf16(bf_lo(hr[2 * hf + 1]) + bf_lo(a1), bf_hi(hr[2 * hf + 1]) + bf_hi(a1));
      *(u32x2*)(op + 8 * hf) = (u32x2){o0, o1};
    }
  }
  STAMP(8); STAMP_OUT(3);
  asm volatile("kD_end_lbl:");
}

extern "C" void kernel_launch(void* const* d_in, const int* in_sizes, int n_in, void* d_out, int out_size, void* d_ws, size_t ws_size,
                              hipStream_t stream) {
  const u16* x = (const u16*)d_in[0];
  const u16* wqkv = (const u16*)d_in[1];
  const u16* wo = (const u16*)d_in[2];
  const u16* wg = (const u16*)d_in[4];
  const u16* wu = (const u16*)d_in[5];
  const u16* wd = (const u16*)d_in[6];
  char* ws = (char*)d_ws;
  (void)hipFuncSetAttribute((const void*)kA, hipFuncAttributeMaxDynamicSharedMemorySize, KA_LDS);
  (void)hipFuncSetAttribute((const void*)kB, hipFuncAttributeMaxDynamicSharedMemorySize, KB_LDS);
  (void)hipFuncSetAttribute((const void*)kC, hipFuncAttributeMaxDynamicSharedMemorySize, KC_LDS);
  (void)hipFuncSetAttribute((const void*)kD, hipFuncAttributeMaxDynamicSharedMemorySize, KD_LDS);
  kA<<<256, 576, KA_LDS, stream>>>(x, wqkv, wo, wg, wu, wd, (const u16*)d_out, ws);
  kB<<<256, 768, KB_LDS, stream>>>(x, wo, wg, wu, ws);
  kC<<<256, 768, KC_LDS, stream>>>(wg, wu, wd, ws);
  kD<<<256, 512, KD_LDS, stream>>>(wd, ws, (u16*)d_out);
}
```

```cpp
#include <hip/hip_runtime.h>
#include <hip/hip_bf16.h>
#include <stdint.h>

#define NEWLAY 1
#define LAS __attribute__((address_space(3)))
#define GAS __attribute__((address_space(1)))
#define DI __device__ __forceinline__
typedef unsigned short u16;
typedef short s16x4 __attribute__((ext_vector_type(4)));
typedef short s16x8 __attribute__((ext_vector_type(8)));
typedef __bf16 bf16x2 __attribute__((ext_vector_type(2)));
typedef __bf16 bf16x8 __attribute__((ext_vector_type(8)));
typedef float f32x2 __attribute__((ext_vector_type(2)));
typedef float f32x4 __attribute__((ext_vector_type(4)));
typedef float f32x16 __attribute__((ext_vector_type(16)));
typedef unsigned u32x2 __attribute__((ext_vector_type(2)));
typedef unsigned u32x4 __attribute__((ext_vector_type(4)));

#define PFMODE 0
constexpr int S_ = 2048, D_ = 576, F_ = 4608;

constexpr size_t WS_O    = 0;
constexpr size_t WS_H    = WS_O + (size_t)S_ * 640 * 2;
constexpr size_t WS_HC   = WS_H + (size_t)S_ * D_ * 2;
constexpr size_t WS_XN   = WS_HC + (size_t)S_ * D_ * 2;
constexpr size_t WS_WQ   = WS_XN + (size_t)S_ * D_ * 2;
constexpr size_t WS_ST   = WS_WQ + (size_t)16 * 18 * 7 * 1024;
constexpr size_t WS_XM   = WS_ST + (size_t)S_ * 16 * 4;
constexpr size_t WS_WM   = WS_XM + (size_t)S_ * 4;
constexpr size_t WS_CNT  = WS_WM + 8192;
constexpr size_t WS_ACT  = WS_CNT + 512 * 256;
constexpr size_t WS_PART = WS_ACT + (size_t)S_ * F_ * 2;
constexpr size_t PART_SLAB = 9 * 1024;

DI float bf_lo(unsigned v) { return __uint_as_float(v << 16); }
DI float bf_hi(unsigned v) { return __uint_as_float(v & 0xffff0000u); }
DI unsigned pk_bf16(float a, float b) { f32x2 v = {a, b}; return __builtin_bit_cast(unsigned, __builtin_convertvector(v, bf16x2)); }
DI float rbf(float a) { return bf_lo(pk_bf16(a, 0.f)); }
DI float dot2bf(unsigned a, unsigned b, float c) { return __builtin_amdgcn_fdot2_f32_bf16(__builtin_bit_cast(bf16x2, a), __builtin_bit_cast(bf16x2, b), c, false); }
DI s16x4 tr_read(LAS char* p) { return __builtin_amdgcn_ds_read_tr16_b64_v4i16((LAS s16x4*)p); }
DI bf16x8 cat8(s16x4 a, s16x4 b) { return __builtin_bit_cast(bf16x8, __builtin_shufflevector(a, b, 0, 1, 2, 3, 4, 5, 6, 7)); }
DI void glds16(const void* g, LAS char* l) {
  unsigned keep; const unsigned la = __builtin_amdgcn_readfirstlane((unsigned)(size_t)l);
  asm volatile("s_mov_b32 %0, m0\n\ts_mov_b32 m0, %2\n\ts_nop 0\n\tglobal_load_lds_dwordx4 %1, off\n\ts_mov_b32 m0, %0" : "=&s"(keep) : "v"(g), "s"(la) : "memory");
}
DI u32x4 tr_frag(LAS char* p, int second) {
  const u32x2 a = __builtin_bit_cast(u32x2, tr_read(p)), b = __builtin_bit_cast(u32x2, tr_read(p + second));
  return (u32x4){a[0], a[1], b[0], b[1]};
}
DI u32x4 tr_frag2(LAS char* p0, LAS char* p1) {
  const u32x2 a = __builtin_bit_cast(u32x2, tr_read(p0)), b = __builtin_bit_cast(u32x2, tr_read(p1));
  return (u32x4){a[0], a[1], b[0], b[1]};
}
#define BF8(v) __builtin_bit_cast(bf16x8, (v))
DI void touch4(const void* g, LAS char* l) {
  unsigned keep; const unsigned la = __builtin_amdgcn_readfirstlane((unsigned)(size_t)l);
  asm volatile("s_mov_b32 %0, m0\n\ts_mov_b32 m0, %2\n\ts_nop 0\n\tglobal_load_lds_dword %1, off\n\ts_mov_b32 m0, %0" : "=&s"(keep) : "v"(g), "s"(la) : "memory");
}
DI float wave_sum(float v) {
#define DPP_ADD(ctrl) v += __builtin_bit_cast(float, __builtin_amdgcn_mov_dpp(__builtin_bit_cast(int, v), ctrl, 0xF, 0xF, true))
  DPP_ADD(0xB1); DPP_ADD(0x4E); DPP_ADD(0x141); DPP_ADD(0x140);
#undef DPP_ADD
  const int iv = __builtin_bit_cast(int, v);
  return (__builtin_bit_cast(float, __builtin_amdgcn_readlane(iv, 0)) + __builtin_bit_cast(float, __builtin_amdgcn_readlane(iv, 16))) +
         (__builtin_bit_cast(float, __builtin_amdgcn_readlane(iv, 32)) + __builtin_bit_cast(float, __builtin_amdgcn_readlane(iv, 48)));
}
#define CODE_ADDR(sym) ({ unsigned long long _pc; int _off; asm volatile("s_getpc_b64 %0\n\ts_mov_b32 %1, " sym "@rel32@lo+4" : "=s"(_pc), "=s"(_off)); (const char*)(_pc + (long long)_off); })
DI void touch_code(const char* lo, const char* hi, int lane, LAS char* scr) {
  const long long n = (hi - lo) >> 7;
  if (n > 0 && n <= 512) {
#pragma unroll 1
    for (int i = 0; i < (int)n; i += 64) { const int l = i + lane < (int)n ? i + lane : (int)n - 1; touch4(lo + (size_t)l * 128, scr); }
  }
}
#define MFMA16(a, b, c) __builtin_amdgcn_mfma_f32_16x16x32_bf16((a), (b), (c), 0, 0, 0)
#define MFMA32(a, b, c) __builtin_amdgcn_mfma_f32_32x32x16_bf16((a), (b), (c), 0, 0, 0)
#define BAR() __builtin_amdgcn_s_barrier()
#define SCHED0() __builtin_amdgcn_sched_barrier(0)
#define WAITV(n) asm volatile("s_waitcnt vmcnt(" #n ")" ::: "memory")
#define WAITVL(n) asm volatile("s_waitcnt vmcnt(" #n ") lgkmcnt(0)" ::: "memory")
#define WAITL0() asm volatile("s_waitcnt lgkmcnt(0)" ::: "memory")

#define STAMP_DECL
#define STAMP(i) do {} while (0)
#define STAMP_OUT(kid) do {} while (0)
DI unsigned fm32_piece(int tok, int c4, int KB16) {
  const int pi = (c4 & 15) >> 2;
  return (unsigned)(((tok >> 5) * KB16 + (c4 >> 4)) * 1024 + (((pi & 1) * 32 + (tok & 31)) * 16) + (pi >> 1) * 8);
}

#define WS_RSRC(ws) __builtin_amdgcn_make_buffer_rsrc((void*)(ws), 0, (int)WS_ACT, 0x00020000)
#define ST16(rs, off, v) __builtin_amdgcn_raw_buffer_store_b128((v), (rs), (int)(off), 0, 16)
#define ST8(rs, off, v) __builtin_amdgcn_raw_buffer_store_b64((v), (rs), (int)(off), 0, 16)
#define ST4(rs, off, v) __builtin_amdgcn_raw_buffer_store_b32((v), (rs), (int)(off), 0, 16)

constexpr int K0_SCR = 9216;
constexpr int K0_LDS = K0_SCR + 256;

extern "C" __global__ __launch_bounds__(512, 2) void k0(const u16* __restrict__ x, const u16* __restrict__ wqkv, const u16* __restrict__ wo,
                                                        const u16* __restrict__ wg, const u16* __restrict__ wu, char* __restrict__ ws) {
  extern __shared__ __attribute__((aligned(16))) char smem[];
  LAS char* lds = (LAS char*)smem;
  const int tid = threadIdx.x, lane = tid & 63, w = __builtin_amdgcn_readfirstlane(tid >> 6);
  const int bx = blockIdx.x;
  const auto rs = WS_RSRC(ws);
  STAMP_DECL STAMP(0);
  if (bx < 256) {
    const int row = bx * 8 + w, l36 = lane < 36 ? lane : 35;
    const char* xp = (const char*)x + (size_t)row * (D_ * 2) + l36 * 32;
    const u32x4 va = *(const u32x4*)xp, vb = *(const u32x4*)(xp + 16);
    f32x2 v[8];
#pragma unroll
    for (int e = 0; e < 4; ++e) { v[e] = (f32x2){bf_lo(va[e]), bf_hi(va[e])}; v[4 + e] = (f32x2){bf_lo(vb[e]), bf_hi(vb[e])}; }
    const f32x2 s2 = ((v[0] + v[1]) + (v[2] + v[3])) + ((v[4] + v[5]) + (v[6] + v[7]));
    const float mean = wave_sum(lane < 36 ? s2[0] + s2[1] : 0.f) * (1.0f / D_), mb = rbf(mean);
    f32x2 q2 = {0.f, 0.f};
    unsigned pk[8];
#pragma unroll
    for (int e = 0; e < 8; ++e) { const f32x2 d = v[e] - mb; q2 = __builtin_elementwise_fma(d, d, q2); pk[e] = pk_bf16(d[0], d[1]); }
    const float dm = mean - mb;
    const float var = rbf(fmaxf(wave_sum(lane < 36 ? q2[0] + q2[1] : 0.f) * (1.0f / D_) - dm * dm, 0.f));
    const float rsd = __builtin_amdgcn_rcpf(rbf(__builtin_amdgcn_sqrtf(rbf(var + 1e-5f))));
#pragma unroll
    for (int e = 0; e < 8; ++e) pk[e] = pk_bf16(bf_lo(pk[e]) * rsd, bf_hi(pk[e]) * rsd);
    if (lane < 36) {
      *(LAS u32x4*)(lds + lane * 256 + w * 16) = (u32x4){pk[0], pk[1], pk[4], pk[5]};
      *(LAS u32x4*)(lds + lane * 256 + 128 + w * 16) = (u32x4){pk[2], pk[3], pk[6], pk[7]};
    }
    if (lane == 0) ST4(rs, WS_XM + row * 4, __float_as_uint(mean));
    WAITL0();
    STAMP(1);
    BAR(); asm volatile("" ::: "memory");
    STAMP(2);
#pragma unroll
    for (int i = 0; i < 2; ++i) {
      const int t = tid + i * 512;
      if (t < 576) {
        const u32x4 c = *(LAS u32x4*)(lds + t * 16);
        ST16(rs, WS_XN + ((bx >> 2) * 36 + (t >> 4)) * 1024 + (((t >> 3) & 1) * 32 + (bx & 3) * 8 + (t & 7)) * 16, c);
      }
    }
    STAMP(3); STAMP_OUT(4);
  } else if (bx < 472) {
    const int u = bx - 256, ks = (u * 5462) >> 16, rem = u - ks * 12, seg = rem >> 2, hq = rem & 3;
#pragma unroll
    for (int i = 0; i < 2; ++i) {
      const int cc = i * 8 + w;
      if (cc < 9) {
        const int q = cc * 64 + lane, rr = (q * 3641) >> 16, ch = q - rr * 18;
        glds16((const char*)wqkv + (size_t)(ks * 32) * 3456 + seg * 1152 + hq * 288 + (unsigned)(rr * 3456 + ch * 16), lds + cc * 1024);
      }
    }
    const int g = lane >> 4, r = lane & 15;
    const unsigned adr = (unsigned)((16 * (g >> 1) + 4 * (g & 1) + (r >> 2)) * 288 + (r & 3) * 8);
    int dsto[2];
#pragma unroll
    for (int i = 0; i < 2; ++i) {
      const int c = (i * 8 + w) * 16 + r, hl = (c * 1821) >> 16, pcol = seg * 36 + c - hl * 36;
      dsto[i] = (int)(WS_WQ + (((hq * 4 + hl) * 18 + ks) * 7 + (pcol >> 4)) * 1024 + (g * 16 + (pcol & 15)) * 16);
    }
    WAITV(0);
    BAR(); asm volatile("" ::: "memory");
    ST16(rs, dsto[0], tr_frag(lds + adr + w * 32, 2304));
    if (w == 0) ST16(rs, dsto[1], tr_frag(lds + adr + 256, 2304));
  } else if (bx >= 544) {
    const int b2 = bx - 544, xcd = b2 & 7, jj = b2 >> 3;
    if (w < 6) { int ln = w * 64 + lane; ln = ln < 324 ? ln : 323; const int k18 = (ln * 3641) >> 16;
      touch4((const char*)((xcd & 1) ? wu : wg) + (size_t)(k18 * 32 + jj) * (F_ * 2) + (xcd >> 1) * 2304 + (ln - k18 * 18) * 128, lds + K0_SCR); }
  } else {
    const int wrow = (bx - 472) * 8 + w, l36 = lane < 36 ? lane : 35;
    const char* rp = (const char*)wo + (size_t)wrow * (D_ * 2) + l36 * 32;
    const u32x4 wa = *(const u32x4*)rp, wb = *(const u32x4*)(rp + 16);
    if (bx == 472 && tid < 272) ST4(rs, WS_CNT + tid * 256, 0u);
    if (bx < 480 + 8 && w == 7) touch_code(CODE_ADDR("k0"), CODE_ADDR("kC"), lane, lds + K0_SCR);
    float t = 0.f;
#pragma unroll
    for (int e = 0; e < 4; ++e) { t = dot2bf(wa[e], 0x3f803f80u, t); t = dot2bf(wb[e], 0x3f803f80u, t); }
    t = wave_sum(lane < 36 ? t : 0.f) * (1.0f / D_);
    const float hi = rbf(t);
    if (lane == 0) ST8(rs, WS_WM + wrow * 8, ((u32x2){pk_bf16(hi, t - hi), 0u}));
  }
}

constexpr int KA_STAGE = 16384;
constexpr int KA_QROW = 304;
constexpr int KA_OOFF = 144 * KA_QROW;
constexpr int KA_WO = 54272;
constexpr int KA_WM = KA_WO + 82944;
constexpr int KA_XR = KA_WM + 5120;
constexpr int KA_SCR = KA_XR + 9216;
constexpr int KA_LDS = KA_SCR + 256;
constexpr int KA_RED = 0;

template <int NB>
DI void kA_loop(LAS char* lds, const char* s0, const char* s1, unsigned st0, unsigned st1, int w, int lane, f32x4 (&acc)[2][4], f32x4& acch) {
  const int g = lane >> 4, r = lane & 15, wm = w & 3;
#define KA_G0(sl) do { glds16(s0, lds + (sl) * KA_STAGE + w * 1024); s0 += st0; } while (0)
#define KA_G1(sl) do { glds16(s1, lds + (sl) * KA_STAGE + 8192 + w * 1024); s1 += st1; } while (0)
  const unsigned xadr = (unsigned)(wm * 2048 + (g >> 1) * 1024 + ((g & 1) * 32 + r) * 16);
  const unsigned hadr = (unsigned)(8192 + lane * 16);
  const unsigned wadr = (unsigned)(9216 + (NB == 4 ? 0 : 4096) + lane * 16);
  const unsigned whadr = wadr + (unsigned)((NB == 3 && wm == 3 ? 2 : wm) * 1024);
  u32x4 xP[3], wP[5], xQ[3], wQ[5];
#define KA_RD(dst, base, off) dst = *(LAS u32x4*)((base) + (off))
#define KA_M(XC, WC, b) do { acc[0][b] = MFMA16(BF8(WC[b]), BF8(XC[0]), acc[0][b]); acc[1][b] = MFMA16(BF8(WC[b]), BF8(XC[1]), acc[1][b]); } while (0)
#define KA_STEP(XC, WC, XN, WN, nb, G0, G1) do { \
    KA_M(XC, WC, 0); SCHED0(); KA_RD(WN[0], nb, wadr); KA_RD(XN[0], nb, xadr); KA_RD(XN[1], nb, xadr + 256); G0; SCHED0(); \
    KA_M(XC, WC, 1); SCHED0(); KA_RD(WN[1], nb, wadr + 1024); KA_RD(WN[2], nb, wadr + 2048); SCHED0(); \
    KA_M(XC, WC, 2); SCHED0(); if (NB == 4) KA_RD(WN[3], nb, wadr + 3072); KA_RD(XN[2], nb, hadr); G1; SCHED0(); \
    if (NB == 4) KA_M(XC, WC, 3); \
    acch = MFMA16(BF8(WC[4]), BF8(XC[2]), acch); SCHED0(); KA_RD(WN[4], nb, whadr); SCHED0(); } while (0)
  KA_RD(wP[0], lds, wadr); KA_RD(xP[0], lds, xadr); KA_RD(xP[1], lds, xadr + 256); KA_RD(wP[1], lds, wadr + 1024); KA_RD(wP[2], lds, wadr + 2048);
  if (NB == 4) KA_RD(wP[3], lds, wadr + 3072);
  KA_RD(xP[2], lds, hadr); KA_RD(wP[4], lds, whadr);
  KA_STEP(xP, wP, xQ, wQ, lds + KA_STAGE, KA_G0(5), KA_G1(5));
  int cur = 2, fre = 0;
#pragma unroll 1
  for (int t = 2; t < 14; t += 2) {
    WAITVL(4);
    BAR(); asm volatile("" ::: "memory");
    LAS char* sb = lds + cur * KA_STAGE;
    KA_STEP(xQ, wQ, xP, wP, sb, KA_G0(fre), KA_G1(fre));
    KA_STEP(xP, wP, xQ, wQ, sb + KA_STAGE, KA_G0(fre + 1), KA_G1(fre + 1));
    fre = cur; cur = cur == 4 ? 0 : cur + 2;
  }
#pragma unroll 1
  for (int t = 14; t < 18; t += 2) {
    if (t == 14) WAITVL(4); else WAITVL(0);
    BAR(); asm volatile("" ::: "memory");
    LAS char* sb = lds + cur * KA_STAGE;
    KA_STEP(xQ, wQ, xP, wP, sb, (void)0, (void)0);
    KA_STEP(xP, wP, xQ, wQ, sb + KA_STAGE, (void)0, (void)0);
    cur = cur == 4 ? 0 : cur + 2;
  }
  KA_M(xQ, wQ, 0); KA_M(xQ, wQ, 1); KA_M(xQ, wQ, 2);
  if (NB == 4) KA_M(xQ, wQ, 3);
  acch = MFMA16(BF8(wQ[4]), BF8(xQ[2]), acch);
}

extern "C" __global__ __launch_bounds__(576, 1) void kA(const u16* __restrict__ x, const u16* __restrict__ wo, const u16* __restrict__ wg,
                                                        const u16* __restrict__ wu, char* __restrict__ ws) {
  extern __shared__ __attribute__((aligned(16))) char smem[];
  LAS char* lds = (LAS char*)smem;
  const int tid = threadIdx.x, lane = tid & 63, w = __builtin_amdgcn_readfirstlane(tid >> 6);
  const int bx = blockIdx.x, xcd = bx & 7, jj = bx >> 3;
  const int mt = (xcd >> 1) * 4 + (jj & 3), hp = (xcd & 1) * 8 + (jj >> 2);
  const int g = lane >> 4, r = lane & 15, wm = w & 3, wn = w >> 2;
  const auto rs = WS_RSRC(ws);
  STAMP_DECL STAMP(0);
  if (w == 8) {
    const int th = hp & 1, n8 = hp >> 1;
    unsigned po[9];
#pragma unroll
    for (int j = 0; j < 9; ++j) {
      const int q = j * 64 + lane, k = (q * 7282) >> 16;
      po[j] = (unsigned)(k * (D_ * 2) + (q - k * 9) * 16 + n8 * 144);
    }
#define KA_WOG(gi) do { _Pragma("unroll") for (int _j = 0; _j < 9; ++_j) glds16((const char*)wo + (size_t)(gi) * (64 * D_ * 2) + po[_j], lds + KA_WO + ((gi) * 9 + _j) * 1024); } while (0)
#pragma unroll 1
    for (int i = 0; i < 9; ++i) {
      BAR();
      if (i >= 1 && i < 5) KA_WOG(i + 4);
      else if (i == 5) {
#pragma unroll
        for (int j = 0; j < 9; ++j) glds16((const char*)x + (size_t)(mt * 128 + th * 64) * (D_ * 2) + po[j], lds + KA_XR + j * 1024);
      } else if (i == 6) {
#pragma unroll
        for (int j = 0; j < 5; ++j) { const int o = j * 1024 + lane * 16; glds16(ws + WS_WM + (o < 4608 ? o : 0), lds + KA_WM + j * 1024); }
      }
    }
    BAR();
    KA_WOG(0); KA_WOG(1);
    BAR();
    KA_WOG(2); KA_WOG(3); KA_WOG(4);
    touch_code(CODE_ADDR("kC"), CODE_ADDR("kD"), lane, lds + KA_SCR);
#define KA_PF(m, i0, i1) do { _Pragma("unroll") for (int _i = i0; _i < i1; ++_i) { int ln = _i * 64 + lane; ln = ln < 324 ? ln : 323; const int k18 = (ln * 3641) >> 16; \
      touch4((const char*)(((xcd ^ (m)) & 1) ? wu : wg) + (size_t)(k18 * 32 + jj) * (F_ * 2) + (xcd >> 1) * 2304 + (ln - k18 * 18) * 128, lds + KA_SCR); } } while (0)
#pragma unroll
    for (int i = 0; i < 2; ++i) {
      const int l54 = lane < 54 ? lane : 53, k3 = (l54 * 3641) >> 16;
      touch4((const char*)(i ? wu : wg) + (size_t)(k3 * 32 + jj) * (F_ * 2) + (xcd >> 1) * 2304 + (l54 - k3 * 18) * 128, lds + KA_SCR);
    }
    BAR();
    WAITV(0);
    BAR(); BAR(); BAR();
    STAMP(9); STAMP_OUT(0);
    return;
  }
  const char* s0 = ws + WS_XN + (size_t)((mt * 4 + (w >> 1)) * 36 + (w & 1)) * 1024 + lane * 16;
  const char* s1 = w ? ws + WS_WQ + (size_t)(hp * 126 + (w - 1)) * 1024 + lane * 16
                     : ws + WS_XN + (size_t)((mt ? mt * 4 - 1 : 0) * 36 + (g >> 1)) * 1024 + ((g & 1) * 32 + 16 + r) * 16;
  const unsigned st0 = 2048, st1 = w ? 7168u : 2048u;
#pragma unroll
  for (int st = 0; st < 5; ++st) { KA_G0(st); KA_G1(st); }
  STAMP(1);
  unsigned sto[5][2];
#pragma unroll
  for (int b = 0; b < 5; ++b) {
    const int pd = 4 * (4 * wn + (b < 4 ? b : wm)) + g, sg = (pd * 57) >> 9, c0 = (pd - sg * 9) * 4;
#pragma unroll
    for (int e = 0; e < 2; ++e) {
      const int c = c0 + 2 * e, hd = c >= 18 ? 1 : 0, d = c - 18 * hd;
      sto[b][e] = (unsigned)(r * KA_QROW + (pd < 27 ? (sg * 2 + hd) * 48 + d * 2 : 288 + 4 * e));
    }
  }
  f32x4 acc[2][4], acch = {0.f, 0.f, 0.f, 0.f};
#pragma unroll
  for (int a = 0; a < 2; ++a)
#pragma unroll
    for (int b = 0; b < 4; ++b) acc[a][b] = (f32x4){0.f, 0.f, 0.f, 0.f};
  WAITV(6);
  BAR(); asm volatile("" ::: "memory");
  STAMP(2);
  if (wn == 0) kA_loop<4>(lds, s0, s1, st0, st1, w, lane, acc, acch);
  else kA_loop<3>(lds, s0, s1, st0, st1, w, lane, acc, acch);
  STAMP(3);
  WAITL0();
  BAR(); asm volatile("" ::: "memory");
  const int tok = mt * 128 + (hp & 1) * 64 + wm * 16 + r;
  const float xm = *(const float*)(ws + WS_XM + (size_t)tok * 4);
  asm volatile("" ::: "memory");
  unsigned pa[5], pm4[5];
  const unsigned p4s = (r & 3) < 2 ? 288u * 144u : 288u * 8u, p4t = (r & 3) < 2 ? 576u : 32u;
#pragma unroll
  for (int i = 0; i < 5; ++i) {
    const int c = 8 * i + 4 * wn + g, h5 = (c * 205) >> 10, krow = 36 * h5 + 8 * (c - 5 * h5) + (r >> 2), p = r & 3;
    pa[i] = (unsigned)(KA_WO + krow * 144 + p * 8);
    pm4[i] = p < 2 ? (unsigned)(KA_WO + krow * 144 + 128 + p * 8) : (unsigned)(KA_WM + krow * 8);
  }
  {
    LAS char* row0 = lds + (2 * wm + 1) * 16 * KA_QROW;
#pragma unroll
    for (int b = 0; b < 4; ++b)
#pragma unroll
      for (int a = 0; a < 2; ++a) {
        *(LAS unsigned*)(row0 + a * 16 * KA_QROW + sto[b][0]) = pk_bf16(acc[a][b][0], acc[a][b][1]);
        *(LAS unsigned*)(row0 + a * 16 * KA_QROW + sto[b][1]) = pk_bf16(acc[a][b][2], acc[a][b][3]);
      }
    *(LAS unsigned*)(lds + sto[4][0]) = pk_bf16(acch[0], acch[1]); *(LAS unsigned*)(lds + sto[4][1]) = pk_bf16(acch[2], acch[3]);
  }
  WAITL0();
  BAR(); asm volatile("" ::: "memory");
  STAMP(4);
  {
    const int hf = tid & 1, t = (tid >> 1) & 127, hd = tid >> 8, T = mt * 128 + t;
    LAS char* base = lds + (t + 16) * KA_QROW + hd * 48;
    const u32x4 qa = *(LAS u32x4*)(base), qb = *(LAS u32x4*)(base + 16); const unsigned qc = *(LAS unsigned*)(base + 32);
    float lg[8];
#pragma unroll
    for (int i = 0; i < 4; ++i) {
      const int wv = hf * 4 + i;
      LAS char* kp = base - wv * 2 * KA_QROW + 96;
      const u32x4 a = *(LAS u32x4*)(kp), b = *(LAS u32x4*)(kp + 16); const unsigned c = *(LAS unsigned*)(kp + 32);
      float s = 0.f;
      s = dot2bf(qa[0], a[0], s); s = dot2bf(qa[1], a[1], s); s = dot2bf(qa[2], a[2], s); s = dot2bf(qa[3], a[3], s);
      s = dot2bf(qb[0], b[0], s); s = dot2bf(qb[1], b[1], s); s = dot2bf(qb[2], b[2], s); s = dot2bf(qb[3], b[3], s);
      s = dot2bf(qc, c, s);
      s = rbf(rbf(s) * 0.23570226039551584f);
      s = (T - 2 * wv >= 0) ? s : -1.0e9f;
      const float o = __builtin_bit_cast(float, __builtin_amdgcn_mov_dpp(__builtin_bit_cast(int, s), 0xB1, 0xF, 0xF, true));
      lg[i] = hf ? o : s; lg[4 + i] = hf ? s : o;
    }
    const float mx = fmaxf(fmaxf(fmaxf(lg[0], lg[1]), fmaxf(lg[2], lg[3])), fmaxf(fmaxf(lg[4], lg[5]), fmaxf(lg[6], lg[7])));
    float sm = 0.f;
#pragma unroll
    for (int wv = 0; wv < 8; ++wv) { lg[wv] = __builtin_amdgcn_exp2f((lg[wv] - mx) * 1.4426950408889634f); sm += lg[wv]; }
    const float inv = 1.0f / sm;
    float o[10];
#pragma unroll
    for (int d = 0; d < 10; ++d) o[d] = 0.f;
#pragma unroll
    for (int wp = 0; wp < 4; ++wp) {
      const unsigned pp = pk_bf16(lg[2 * wp] * inv, lg[2 * wp + 1] * inv);
      LAS char* v0 = base - (2 * wp) * 2 * KA_QROW + 192 + hf * 16;
      LAS char* v1 = v0 - 2 * KA_QROW;
      const u32x4 a = *(LAS u32x4*)(v0), b = *(LAS u32x4*)(v1); const unsigned a4 = *(LAS unsigned*)(v0 + 16), b4 = *(LAS unsigned*)(v1 + 16);
      const unsigned av[5] = {a[0], a[1], a[2], a[3], a4}, bv[5] = {b[0], b[1], b[2], b[3], b4};
#pragma unroll
      for (int i = 0; i < 5; ++i) {
        o[2 * i]     = dot2bf(pp, __builtin_amdgcn_perm(bv[i], av[i], 0x05040100u), o[2 * i]);
        o[2 * i + 1] = dot2bf(pp, __builtin_amdgcn_perm(bv[i], av[i], 0x07060302u), o[2 * i + 1]);
      }
    }
    LAS char* op = lds + KA_OOFF + t * 80 + hd * 36 + hf * 16;
#pragma unroll
    for (int i = 0; i < 5; ++i) *(LAS unsigned*)(op + 4 * i) = pk_bf16(o[2 * i], o[2 * i + 1]);
    if (hd == 0) *(LAS unsigned*)(lds + KA_OOFF + t * 80 + 72 + hf * 4) = 0u;
  }
  WAITL0();
  BAR(); asm volatile("" ::: "memory");
  STAMP(5);
#pragma unroll
  for (int i = 0; i < 2; ++i) {
    const int id = tid + i * 512;
    if (id < 640) {
      const int mb = (id * 820) >> 16, rem = id - mb * 80;
      const u32x4 v = *(LAS u32x4*)(lds + KA_OOFF + (mb * 16 + (rem & 15)) * 80 + (rem >> 4) * 16);
      ST16(rs, WS_O + ((mt * 8 + mb) * 80 + hp * 5) * 256 + rem * 16, v);
    }
  }
  WAITV(0);
  BAR(); asm volatile("" ::: "memory");
  STAMP(6);
  GAS unsigned* cnt = (GAS unsigned*)(ws + WS_CNT) + (256 + mt) * 64;
  if (tid == 0) __hip_atomic_fetch_add(cnt, 1u, __ATOMIC_RELAXED, __HIP_MEMORY_SCOPE_AGENT);
  u32x4 wf[4][5];
#define KA_P2R(set, i) do { _Pragma("unroll") for (int _b = 0; _b < 4; ++_b) wf[set][_b] = tr_frag(lds + pa[(i) % 5] + ((i) / 5) * (288 * 144) + _b * 32, 576); \
      wf[set][4] = tr_frag2(lds + pm4[(i) % 5] + ((i) / 5) * p4s, lds + pm4[(i) % 5] + ((i) / 5) * p4s + p4t); } while (0)
  KA_P2R(0, 0); KA_P2R(1, 1); KA_P2R(2, 2); KA_P2R(3, 3);
  SCHED0();
  if (w == 0) {
    unsigned spins = 0;
    while (__builtin_amdgcn_readfirstlane(__hip_atomic_load(cnt, __ATOMIC_RELAXED, __HIP_MEMORY_SCOPE_AGENT)) < 16u) {
      if (++spins > 2000000u) break;
      __builtin_amdgcn_s_sleep(1);
    }
  }
  asm volatile("" ::: "memory");
  BAR(); asm volatile("" ::: "memory");
  STAMP(7);
  {
    const int n8 = hp >> 1;
    const int oo = (int)(WS_O + ((tok >> 4) * 80 + 4 * wn) * 256 + lane * 16);
    u32x4 of[10];
#pragma unroll
    for (int i = 0; i < 10; ++i) of[i] = __builtin_amdgcn_raw_buffer_load_b128(rs, oo + i * 2048, 0, 16);
    f32x4 c[5];
#pragma unroll
    for (int b = 0; b < 5; ++b) c[b] = (f32x4){0.f, 0.f, 0.f, 0.f};
#define KA_P2M(set, i) do { _Pragma("unroll") for (int _b = 0; _b < 5; ++_b) c[_b] = MFMA16(BF8(wf[set][_b]), BF8(of[i]), c[_b]); } while (0)
    SCHED0();
#pragma unroll
    for (int i = 0; i < 10; ++i) {
      KA_P2M(i & 3, i); SCHED0();
      if (i + 4 < 10) KA_P2R(i & 3, i + 4);
      SCHED0();
    }
    STAMP(8);
    if (wn == 1) {
#pragma unroll
      for (int b = 0; b < 5; ++b) *(LAS f32x4*)(lds + KA_RED + (wm * 5 + b) * 1024 + lane * 16) = c[b];
    }
    WAITL0();
    BAR(); asm volatile("" ::: "memory");
    if (wn == 0) {
#pragma unroll
      for (int b = 0; b < 5; ++b) c[b] += *(LAS f32x4*)(lds + KA_RED + (wm * 5 + b) * 1024 + lane * 16);
      const float mean = xm + __shfl(c[4][0] + c[4][1], 32 + r), mb = rbf(mean);
      LAS char* xq = lds + KA_XR + (wm * 16 + r) * 144 + g * 8;
      unsigned hw_[10], cw_[10];
      f32x2 qv = {0.f, 0.f};
#pragma unroll
      for (int b = 0; b < 5; ++b) {
        const u32x2 xv = *(LAS u32x2*)(xq + ((b < 4 || g < 2) ? b * 32 : 0));
#pragma unroll
        for (int e = 0; e < 2; ++e) {
          const unsigned a = pk_bf16(c[b][2 * e], c[b][2 * e + 1]);
          const unsigned hh = pk_bf16(bf_lo(xv[e]) + bf_lo(a), bf_hi(xv[e]) + bf_hi(a));
          const f32x2 hv = {bf_lo(hh), bf_hi(hh)};
          const f32x2 d = hv - mean, dc = hv - mb;
          hw_[2 * b + e] = hh; cw_[2 * b + e] = pk_bf16(dc[0], dc[1]);
          if (b < 4) qv += d * d; else qv += g < 2 ? d * d : (f32x2){0.f, 0.f};
        }
      }
      const int ho = ((tok >> 4) * 72 + n8 * 9 + 2 * (g & 1) + (g >> 1)) * 256 + r * 16;
#pragma unroll
      for (int b = 0; b < 4; b += 2) {
        const auto c0 = __builtin_amdgcn_permlane16_swap(cw_[2 * b], cw_[2 * b + 2], false, false), c1 = __builtin_amdgcn_permlane16_swap(cw_[2 * b + 1], cw_[2 * b + 3], false, false);
        const auto h0 = __builtin_amdgcn_permlane16_swap(hw_[2 * b], hw_[2 * b + 2], false, false), h1 = __builtin_amdgcn_permlane16_swap(hw_[2 * b + 1], hw_[2 * b + 3], false, false);
        ST16(rs, WS_HC + ho + b * 512, ((u32x4){c0[0], c1[0], c0[1], c1[1]}));
        ST16(rs, WS_H + ho + b * 512, ((u32x4){h0[0], h1[0], h0[1], h1[1]}));
      }
      const auto s0 = __builtin_amdgcn_permlane32_swap(cw_[8], hw_[8], false, false), s1 = __builtin_amdgcn_permlane32_swap(cw_[9], hw_[9], false, false);
      ST8(rs, (g < 2 ? WS_HC : WS_H) + ((tok >> 4) * 72 + n8 * 9 + 8) * 256 + r * 16 + (g & 1) * 8, ((u32x2){s0[0], s1[0]}));
      float q = qv[0] + qv[1];
      q += __shfl_xor(q, 16); q += __shfl_xor(q, 32);
      if (g == 0) ST4(rs, WS_ST + (tok * 8 + n8) * 4, __float_as_uint(q));
    }
  }
  STAMP(9); STAMP_OUT(0);
}

constexpr int KC_UPOFF = 16384 + 9216 + 128;
constexpr int KC_STAGE = 35072;
constexpr int KC_STOFF = 4 * KC_STAGE;
constexpr int KC_SCR = KC_STOFF + 8192;
constexpr int KC_LDS = KC_SCR + 256;

DI f32x2 geglu2(f32x2 a, f32x2 b, f32x2 k0, f32x2 k1, f32x2 ik2) {
  const f32x2 tt = a * __builtin_elementwise_fma(a * a, k1, k0);
  const f32x2 e = {__builtin_amdgcn_exp2f(tt[0]), __builtin_amdgcn_exp2f(tt[1])};
  const f32x2 d = __builtin_elementwise_fma(e, ik2, ik2);
  const f32x2 rr = {__builtin_amdgcn_rcpf(d[0]), __builtin_amdgcn_rcpf(d[1])};
  return (a * b) * rr;
}
DI void ln_consts(const LAS char* sp_, f32x2& k0, f32x2& k1, f32x2& ik2) {
  const LAS f32x4* sp = (const LAS f32x4*)sp_;
  const f32x4 s4 = sp[0] + sp[1];
  const float var = rbf(((s4[0] + s4[1]) + (s4[2] + s4[3])) * (1.0f / D_));
  const float sd = rbf(__builtin_amdgcn_sqrtf(rbf(var + 1e-5f)));
  const float rs = __builtin_amdgcn_rcpf(sd);
  const float k0s = -2.3022081981f * rs;
  const float k1s = -0.1029432397f * rs * rs * rs;
  const float ik2s = sd * sd;
  k0 = (f32x2){k0s, k0s}; k1 = (f32x2){k1s, k1s}; ik2 = (f32x2){ik2s, ik2s};
}

extern "C" __global__ __launch_bounds__(768, 1) void kC(const u16* __restrict__ wg, const u16* __restrict__ wu, const u16* __restrict__ wd, char* __restrict__ ws) {
  extern __shared__ __attribute__((aligned(16))) char smem[];
  LAS char* lds = (LAS char*)smem;
  const int tid = threadIdx.x, lane = tid & 63, w = __builtin_amdgcn_readfirstlane(tid >> 6);
  const int bx = blockIdx.x, xcd = bx & 7, jj = bx >> 3;
  const int pm = (xcd & 1) * 4 + (jj & 3), pn = (xcd >> 1) * 8 + (jj >> 2);
  const int wm = w & 3, wn = w >> 2;

  STAMP_DECL STAMP(0);
  const char* src[3]; unsigned dst[3]; unsigned step[3];
  src[0] = ws + WS_HC + (size_t)(pm * 16 + w) * (72 * 256) + lane * 16; dst[0] = w * 1024; step[0] = 1024;
#define KC_G(i, st) do { glds16(src[i], lds + ((st) & 3) * KC_STAGE + dst[i]); src[i] += step[i]; } while (0)
  KC_G(0, 0);
#pragma unroll
  for (int i = 1; i < 3; ++i) {
    int sl = i * 12 + w; sl = sl > 33 ? 33 : sl;
    if (sl < 16) {
      src[i] = ws + WS_HC + (size_t)(pm * 16 + sl) * (72 * 256) + lane * 16;
      dst[i] = sl * 1024; step[i] = 1024;
    } else {
      const int c = sl - 16, m = c >= 9 ? 1 : 0, cc = c - 9 * m, q = cc * 64 + lane, rr = (q * 3641) >> 16, ch = q - rr * 18;
      const int kr = (rr & 19) | ((rr & 4) << 1) | ((rr & 8) >> 1);
      src[i] = (const char*)(m ? wu : wg) + pn * 288 + (unsigned)(kr * (F_ * 2) + ch * 16);
      dst[i] = (m ? KC_UPOFF : 16384) + cc * 1024; step[i] = 32 * F_ * 2;
    }
    KC_G(i, 0);
  }
#define KC_ISSUE(st) do { KC_G(0, st); KC_G(1, st); KC_G(2, st); } while (0)
  KC_ISSUE(1); KC_ISSUE(2);
  glds16(ws + WS_ST + (size_t)pm * 8192 + (w & 7) * 1024 + lane * 16, lds + KC_STOFF + (w & 7) * 1024);
  const auto ars = __builtin_amdgcn_make_buffer_rsrc((void*)(ws + WS_ACT), 0, S_ * F_ * 2, 0x00020000);
#define KC_TOUCH_WD() do { if (w < 6) {     \
    int ln = w * 64 + lane; ln = ln < 324 ? ln : 323; \
    touch4((const char*)wd + (size_t)(xcd >> 1) * (1152 * D_ * 2) + (size_t)(jj * 324 + ln) * 128, lds + KC_SCR); } \
    else if (w == 6) touch_code(CODE_ADDR("kD"), CODE_ADDR("kD_end_lbl"), lane, lds + KC_SCR); } while (0)

  const int g = lane >> 4, r = lane & 15;
  f32x4 acc[6][4];
#pragma unroll
  for (int a = 0; a < 6; ++a)
#pragma unroll
    for (int b = 0; b < 4; ++b) acc[a][b] = (f32x4){0.f, 0.f, 0.f, 0.f};
  const unsigned xadr = (unsigned)(wm * 4096 + lane * 16);
  const unsigned wadr = (unsigned)(16384 + (16 * (g >> 1) + 4 * (g & 1) + (r >> 2)) * 288 + wn * 96 + (r & 3) * 8);
  u32x4 xf[4], wA[3], wB[3];
#define KC_WOFF(j) ((((j) & 1) ? (KC_UPOFF - 16384) : 0) + ((j) >> 1) * 32)
#define KC_RW(WF, i, j, base) WF[i] = tr_frag((base) + wadr + KC_WOFF(j), 2304)
#define KC_RX(c, base) xf[c] = *(LAS u32x4*)((base) + xadr + (c) * 1024)
#define KC_M3(WF, j0, c) do { acc[j0][c] = MFMA16(BF8(WF[0]), BF8(xf[c]), acc[j0][c]); acc[j0 + 1][c] = MFMA16(BF8(WF[1]), BF8(xf[c]), acc[j0 + 1][c]); \
    acc[j0 + 2][c] = MFMA16(BF8(WF[2]), BF8(xf[c]), acc[j0 + 2][c]); } while (0)
#define KC_HALFA(base, G0, G1) do { \
    KC_M3(wA, 0, 0); SCHED0(); KC_RW(wB, 0, 3, base); SCHED0(); \
    KC_M3(wA, 0, 1); SCHED0(); KC_RW(wB, 1, 4, base); G0; SCHED0(); \
    KC_M3(wA, 0, 2); SCHED0(); KC_RW(wB, 2, 5, base); SCHED0(); \
    KC_M3(wA, 0, 3); SCHED0(); G1; SCHED0(); } while (0)
#define KC_HALFB(nb, G0) do { \
    KC_M3(wB, 3, 0); SCHED0(); KC_RX(0, nb); KC_RW(wA, 0, 0, nb); SCHED0(); \
    KC_M3(wB, 3, 1); SCHED0(); KC_RX(1, nb); KC_RW(wA, 1, 1, nb); SCHED0(); \
    KC_M3(wB, 3, 2); SCHED0(); KC_RX(2, nb); KC_RW(wA, 2, 2, nb); G0; SCHED0(); \
    KC_M3(wB, 3, 3); SCHED0(); KC_RX(3, nb); SCHED0(); } while (0)
  STAMP(1);
  WAITV(7); BAR(); asm volatile("" ::: "memory");
  STAMP(2);
  KC_RX(0, lds); KC_RW(wA, 0, 0, lds); KC_RW(wA, 1, 1, lds); KC_RW(wA, 2, 2, lds); KC_RX(1, lds); KC_RX(2, lds); KC_RX(3, lds);
  KC_ISSUE(3);
  KC_HALFA(lds, (void)0, (void)0);
  STAMP(3);
#pragma unroll 1
  for (int t = 1; t < 15; ++t) {
    WAITVL(6);
    BAR(); asm volatile("" ::: "memory");
    LAS char* sb = lds + (t & 3) * KC_STAGE;
    KC_HALFB(sb, KC_G(0, t + 3));
    KC_HALFA(sb, KC_G(1, t + 3), KC_G(2, t + 3));
  }
  WAITVL(0);
  BAR(); asm volatile("" ::: "memory");
  KC_TOUCH_WD();
#pragma unroll 1
  for (int t = 15; t < 18; ++t) {
    LAS char* sb = lds + (t & 3) * KC_STAGE;
    KC_HALFB(sb, (void)0);
    KC_HALFA(sb, (void)0, (void)0);
  }
  KC_M3(wB, 3, 0); KC_M3(wB, 3, 1); KC_M3(wB, 3, 2); KC_M3(wB, 3, 3);
  STAMP(4);
#pragma unroll
  for (int cp = 0; cp < 2; ++cp) {
    f32x2 k0[2], k1[2], ik2[2];
#pragma unroll
    for (int e = 0; e < 2; ++e) ln_consts(lds + KC_STOFF + (wm * 64 + cp * 32 + e * 16 + r) * 32, k0[e], k1[e], ik2[e]);
#pragma unroll
    for (int f = 0; f < 3; ++f) {
      u32x2 pc[2];
#pragma unroll
      for (int e = 0; e < 2; ++e) {
        const f32x4 a = acc[2 * f][2 * cp + e], b = acc[2 * f + 1][2 * cp + e];
        const f32x2 o0 = geglu2((f32x2){a[0], a[1]}, (f32x2){b[0], b[1]}, k0[e], k1[e], ik2[e]);
        const f32x2 o1 = geglu2((f32x2){a[2], a[3]}, (f32x2){b[2], b[3]}, k0[e], k1[e], ik2[e]);
        pc[e] = (u32x2){pk_bf16(o0[0], o0[1]), pk_bf16(o1[0], o1[1])};
      }
      const auto s0 = __builtin_amdgcn_permlane32_swap(pc[0][0], pc[1][0], false, false);
      const auto s1 = __builtin_amdgcn_permlane32_swap(pc[0][1], pc[1][1], false, false);
      const u32x4 v = {s0[0], s1[0], s0[1], s1[1]};
      __builtin_amdgcn_raw_buffer_store_b128(v, ars, ((pm * 8 + wm * 2 + cp) * 288 + pn * 9 + wn * 3 + f) * 1024 + ((g & 1) * 32 + (g >> 1) * 16 + r) * 16, 0, 16);
    }
  }
  STAMP(5); STAMP_OUT(2);
}

constexpr int KD_STAGE = 16384 + 18432;
constexpr int KD_LDS = 2 * KD_STAGE + 72 * 1024;

template <int NB, int NG>
DI void kD_loop(LAS char* lds, const char* srcA, const char* const (&srcW)[3], int w, int lane, f32x4 (&acc)[4][5]) {
  const int gk = w >> 2, wm = w & 1, g = lane >> 4, r = lane & 15;
#define KD_GA(i, st) glds16(srcA + (size_t)(i) * (2 * 288 * 1024) + (st) * 4096, lds + ((st) & 3) * KD_STAGE + (i) * 8192 + w * 1024)
#define KD_GW(i, st) glds16(srcW[i] + (size_t)(st) * (64 * D_ * 2), lds + ((st) & 3) * KD_STAGE + 16384 + (i) * 8192 + w * 1024)
#define KD_GW2(st) do { if (NG == 5) glds16(srcW[2] + (size_t)(st) * (64 * D_ * 2), lds + ((st) & 3) * KD_STAGE + 32768 + (w & 1) * 1024); } while (0)
#define KD_WAIT2() do { if (NG == 5) WAITVL(10); else WAITVL(8); } while (0)
  const unsigned xadr = (unsigned)(wm * 8192 + (gk * 2 + (g >> 1)) * 1024 + ((g & 1) * 32 + r) * 16);
  const unsigned wadr = (unsigned)(16384 + (gk * 32 + 16 * (g >> 1) + 4 * (g & 1) + (r >> 2)) * 288 + (NB == 5 ? 0 : 160) + (r & 3) * 8);
  u32x4 xP[4], wP[5], xQ[4], wQ[5];
#define KD_RW(WF, b, base) WF[b] = tr_frag((base) + wadr + (b) * 32, 2304)
#define KD_RX(XF, a, base) XF[a] = *(LAS u32x4*)((base) + xadr + ((a) >> 1) * 4096 + ((a) & 1) * 256)
#define KD_M(XC, WC, b) do { _Pragma("unroll") for (int _a = 0; _a < 4; ++_a) acc[_a][b] = MFMA16(BF8(WC[b]), BF8(XC[_a]), acc[_a][b]); } while (0)
#define KD_STEP(XC, WC, XN, WN, nb, G0, G1, G2, G3) do { \
    KD_M(XC, WC, 0); SCHED0(); KD_RW(WN, 0, nb); KD_RX(XN, 0, nb); KD_RX(XN, 1, nb); G0; SCHED0(); \
    KD_M(XC, WC, 1); SCHED0(); KD_RX(XN, 2, nb); KD_RX(XN, 3, nb); KD_RW(WN, 1, nb); G1; SCHED0(); \
    KD_M(XC, WC, 2); SCHED0(); KD_RW(WN, 2, nb); KD_RW(WN, 3, nb); G2; SCHED0(); \
    KD_M(XC, WC, 3); SCHED0(); if (NB == 5) KD_RW(WN, 4, nb); G3; SCHED0(); \
    if (NB == 5) { KD_M(XC, WC, 4); SCHED0(); } } while (0)
  KD_RW(wP, 0, lds); KD_RX(xP, 0, lds); KD_RX(xP, 1, lds); KD_RX(xP, 2, lds); KD_RX(xP, 3, lds); KD_RW(wP, 1, lds); KD_RW(wP, 2, lds); KD_RW(wP, 3, lds);
  if (NB == 5) KD_RW(wP, 4, lds);
  KD_GA(0, 3); KD_GA(1, 3); KD_GW(0, 3); KD_GW(1, 3); KD_GW2(3);
#pragma unroll 1
  for (int t = 1; t < 15; t += 2) {
    KD_WAIT2();
    BAR(); asm volatile("" ::: "memory");
    KD_STEP(xP, wP, xQ, wQ, lds + (t & 3) * KD_STAGE, do { KD_GA(0, t + 3); KD_GW2(t + 3); } while (0), KD_GA(1, t + 3), KD_GW(0, t + 3), KD_GW(1, t + 3));
    KD_WAIT2();
    BAR(); asm volatile("" ::: "memory");
    KD_STEP(xQ, wQ, xP, wP, lds + ((t + 1) & 3) * KD_STAGE, do { KD_GA(0, t + 4); KD_GW2(t + 4); } while (0), KD_GA(1, t + 4), KD_GW(0, t + 4), KD_GW(1, t + 4));
  }
  KD_WAIT2(); BAR(); asm volatile("" ::: "memory");
  KD_STEP(xP, wP, xQ, wQ, lds + 3 * KD_STAGE, (void)0, (void)0, (void)0, (void)0);
  if (NG == 5) WAITVL(5); else WAITVL(4);
  BAR(); asm volatile("" ::: "memory");
  KD_STEP(xQ, wQ, xP, wP, lds, (void)0, (void)0, (void)0, (void)0);
  WAITVL(0); BAR(); asm volatile("" ::: "memory");
  KD_STEP(xP, wP, xQ, wQ, lds + KD_STAGE, (void)0, (void)0, (void)0, (void)0);
  KD_M(xQ, wQ, 0); KD_M(xQ, wQ, 1); KD_M(xQ, wQ, 2); KD_M(xQ, wQ, 3);
  if (NB == 5) KD_M(xQ, wQ, 4);
}

extern "C" __global__ __launch_bounds__(512, 1) void kD(const u16* __restrict__ wd, char* __restrict__ ws, u16* __restrict__ out) {
  extern __shared__ __attribute__((aligned(16))) char smem[];
  LAS char* lds = (LAS char*)smem;
  const int tid = threadIdx.x, lane = tid & 63, w = __builtin_amdgcn_readfirstlane(tid >> 6);
  const int bx = blockIdx.x, xcd = bx & 7, jj = bx >> 3;
  const int ks = xcd >> 1, mt = (xcd & 1) * 8 + (jj & 7), nt = jj >> 3;
  const int gk = w >> 2, wm = w & 1, wn = ((w >> 1) & 1) ^ gk;
  const int g = lane >> 4, r = lane & 15;

  STAMP_DECL STAMP(0);
  const char* srcA = ws + WS_ACT + (size_t)((mt * 4 + (w >> 2)) * 288 + ks * 72 + (w & 3)) * 1024 + lane * 16;
  KD_GA(0, 0); KD_GA(1, 0);
  const char* srcW[3];
#pragma unroll
  for (int i = 0; i < 3; ++i) {
    const int cc = i < 2 ? i * 8 + w : 16 + (w & 1);
    const int q = cc * 64 + lane, rr = (q * 3641) >> 16, ch = q - rr * 18;
    srcW[i] = (const char*)wd + (size_t)(ks * 1152 * D_ + nt * 144) * 2 + (unsigned)(rr * (D_ * 2) + ch * 16);
  }
  const bool extra = (w == 2 || w == 3);
#define KD_GX(st) do { if (extra) glds16(srcW[2] + (size_t)(st) * (64 * D_ * 2), lds + ((st) & 3) * KD_STAGE + 32768 + (w & 1) * 1024); } while (0)
  KD_GW(0, 0); KD_GW(1, 0); KD_GX(0);
  KD_GA(0, 1); KD_GA(1, 1); KD_GW(0, 1); KD_GW(1, 1); KD_GX(1);
  KD_GA(0, 2); KD_GA(1, 2); KD_GW(0, 2); KD_GW(1, 2); KD_GX(2);
  const char* hb = ws + WS_H + (size_t)((mt * 8 + ks * 2 + (g >> 1)) * 72 + nt * 18) * 256 + r * 16 + (g & 1) * 8;
  const u32x2 ha0 = *(const u32x2*)(hb + w * 512), ha1 = *(const u32x2*)(hb + w * 512 + 256), hb0 = *(const u32x2*)(hb + 4096), hb1 = *(const u32x2*)(hb + 4096 + 256);
  const u32x4 hres0 = {ha0[0], ha0[1], ha1[0], ha1[1]}, hres1 = {hb0[0], hb0[1], hb1[0], hb1[1]};
  asm volatile("" ::: "memory");
  f32x4 acc[4][5];
#pragma unroll
  for (int a = 0; a < 4; ++a)
#pragma unroll
    for (int b = 0; b < 5; ++b) acc[a][b] = (f32x4){0.f, 0.f, 0.f, 0.f};
  STAMP(1);
  if (extra) WAITVL(10); else WAITVL(8);
  BAR(); asm volatile("" ::: "memory");
  STAMP(2);
  if (wn == 0) kD_loop<5, 4>(lds, srcA, srcW, w, lane, acc);
  else if (extra) kD_loop<4, 5>(lds, srcA, srcW, w, lane, acc);
  else kD_loop<4, 4>(lds, srcA, srcW, w, lane, acc);
  STAMP(3);
  const int qk = 2 * wm + gk;
  {
    LAS char* xw = lds + 2 * KD_STAGE + ((qk ^ 1) * 18 + wn * 5) * 1024 + lane * 16;
#pragma unroll
    for (int sb = 0; sb < 2; ++sb)
#pragma unroll
      for (int b = 0; b < 5; ++b)
        if (b < 4 || wn == 0) *(LAS f32x4*)(xw + (sb * 9 + b) * 1024) = gk ? acc[sb][b] : acc[2 + sb][b];
  }
  WAITL0();
  BAR(); asm volatile("" ::: "memory");
  STAMP(4);
  const int tile = mt * 4 + nt;
  const auto prs = __builtin_amdgcn_make_buffer_rsrc((void*)(ws + WS_PART), 0, (int)(64 * 4 * 3 * PART_SLAB), 0x00020000);
  {
    LAS char* xr = lds + 2 * KD_STAGE + (qk * 18 + wn * 5) * 1024 + lane * 16;
    const int sidx = ks - (ks > qk ? 1 : 0);
    const int po = (int)(((tile * 4 + qk) * 3 + sidx) * PART_SLAB) + wn * 5120 + lane * 16;
#pragma unroll
    for (int b = 0; b < 5; ++b)
      if (b < 4 || wn == 0) {
        const f32x4 v0 = (gk ? acc[2][b] : acc[0][b]) + *(LAS f32x4*)(xr + b * 1024);
        const f32x4 v1 = (gk ? acc[3][b] : acc[1][b]) + *(LAS f32x4*)(xr + (9 + b) * 1024);
        if (qk != ks) {
          const auto s0 = __builtin_amdgcn_permlane32_swap(pk_bf16(v0[0], v0[1]), pk_bf16(v1[0], v1[1]), false, false);
          const auto s1 = __builtin_amdgcn_permlane32_swap(pk_bf16(v0[2], v0[3]), pk_bf16(v1[2], v1[3]), false, false);
          __builtin_amdgcn_raw_buffer_store_b128((u32x4){s0[0], s1[0], s0[1], s1[1]}, prs, po + b * 1024, 0, 16);
        } else {
          *(LAS f32x4*)(lds + (wn * 5 + b) * 2048 + lane * 16) = v0;
          *(LAS f32x4*)(lds + (wn * 5 + b) * 2048 + 1024 + lane * 16) = v1;
        }
      }
  }
  STAMP(5);
  WAITVL(0);
  BAR(); asm volatile("" ::: "memory");
  STAMP(6);
  GAS unsigned* cnt = (GAS unsigned*)(ws + WS_CNT);
  if (tid < 4 && tid != ks) __hip_atomic_fetch_add(cnt + (tile * 4 + tid) * 64, 1u, __ATOMIC_RELAXED, __HIP_MEMORY_SCOPE_AGENT);
  if (w == 0) {
    unsigned spins = 0;
    while (__builtin_amdgcn_readfirstlane(__hip_atomic_load(cnt + (tile * 4 + ks) * 64, __ATOMIC_RELAXED, __HIP_MEMORY_SCOPE_AGENT)) < 3u) {
      if (++spins > 2000000u) break;
      __builtin_amdgcn_s_sleep(1);
    }
  }
  asm volatile("" ::: "memory");
  BAR(); asm volatile("" ::: "memory");
  STAMP(7);
  u32x4 pp[2][3];
#pragma unroll
  for (int i = 0; i < 2; ++i)
#pragma unroll
    for (int q = 0; q < 3; ++q)
      if (i == 0 || w == 1) pp[i][q] = __builtin_amdgcn_raw_buffer_load_b128(prs, (int)(((tile * 4 + ks) * 3 + q) * PART_SLAB) + (i ? 8 : w) * 1024 + lane * 16, 0, 16);
#pragma unroll
  for (int i = 0; i < 2; ++i) {
    if (i == 1 && w != 1) break;
    const int grp = i ? 8 : w;
    const u32x4 hr = i ? hres1 : hres0;
    u16* op = out + (size_t)(mt * 128 + ks * 32 + (g >> 1) * 16 + r) * D_ + nt * 144 + grp * 16 + 4 * (g & 1);
#pragma unroll
    for (int hf = 0; hf < 2; ++hf) {
      f32x4 v = *(LAS f32x4*)(lds + grp * 2048 + (g >> 1) * 1024 + (((g & 1) + 2 * hf) * 16 + r) * 16);
#pragma unroll
      for (int q = 0; q < 3; ++q) v += (f32x4){bf_lo(pp[i][q][2 * hf]), bf_hi(pp[i][q][2 * hf]), bf_lo(pp[i][q][2 * hf + 1]), bf_hi(pp[i][q][2 * hf + 1])};
      const unsigned a0 = pk_bf16(v[0], v[1]), a1 = pk_bf16(v[2], v[3]);
      const unsigned o0 = pk_bf16(bf_lo(hr[2 * hf]) + bf_lo(a0), bf_hi(hr[2 * hf]) + bf_hi(a0));
      const unsigned o1 = pk_bf16(bf_lo(hr[2 * hf + 1]) + bf_lo(a1), bf_hi(hr[2 * hf + 1]) + bf_hi(a1));
      *(u32x2*)(op + 8 * hf) = (u32x2){o0, o1};
    }
  }
  STAMP(8); STAMP_OUT(3);
  asm volatile("kD_end_lbl:");
}

extern "C" void kernel_launch(void* const* d_in, const int* in_sizes, int n_in, void* d_out, int out_size, void* d_ws, size_t ws_size,
                              hipStream_t stream) {
  const u16* x = (const u16*)d_in[0];
  const u16* wqkv = (const u16*)d_in[1];
  const u16* wo = (const u16*)d_in[2];
  const u16* wg = (const u16*)d_in[4];
  const u16* wu = (const u16*)d_in[5];
  const u16* wd = (const u16*)d_in[6];
  char* ws = (char*)d_ws;
  (void)hipFuncSetAttribute((const void*)kA, hipFuncAttributeMaxDynamicSharedMemorySize, KA_LDS);
  (void)hipFuncSetAttribute((const void*)kC, hipFuncAttributeMaxDynamicSharedMemorySize, KC_LDS);
  (void)hipFuncSetAttribute((const void*)kD, hipFuncAttributeMaxDynamicSharedMemorySize, KD_LDS);
  k0<<<PFMODE == 3 ? 800 : 544, 512, K0_LDS, stream>>>(x, wqkv, wo, wg, wu, ws);
  kA<<<256, 576, KA_LDS, stream>>>(x, wo, wg, wu, ws);
  kC<<<256, 768, KC_LDS, stream>>>(wg, wu, wd, ws);
  kD<<<256, 512, KD_LDS, stream>>>(wd, ws, (u16*)d_out);
}
```

```cpp
#include <hip/hip_runtime.h>
#include <hip/hip_bf16.h>
#include <stdint.h>

#define NEWLAY 1
#define LAS __attribute__((address_space(3)))
#define GAS __attribute__((address_space(1)))
#define DI __device__ __forceinline__
typedef unsigned short u16;
typedef short s16x4 __attribute__((ext_vector_type(4)));
typedef short s16x8 __attribute__((ext_vector_type(8)));
typedef __bf16 bf16x2 __attribute__((ext_vector_type(2)));
typedef __bf16 bf16x8 __attribute__((ext_vector_type(8)));
typedef float f32x2 __attribute__((ext_vector_type(2)));
typedef float f32x4 __attribute__((ext_vector_type(4)));
typedef float f32x16 __attribute__((ext_vector_type(16)));
typedef unsigned u32x2 __attribute__((ext_vector_type(2)));
typedef unsigned u32x4 __attribute__((ext_vector_type(4)));

#define KC_PF 0
#define PFMODE 0
constexpr int S_ = 2048, D_ = 576, F_ = 4608;

constexpr size_t WS_O    = 0;
constexpr size_t WS_H    = WS_O + (size_t)S_ * 640 * 2;
constexpr size_t WS_HC   = WS_H + (size_t)S_ * D_ * 2;
constexpr size_t WS_XN   = WS_HC + (size_t)S_ * D_ * 2;
constexpr size_t WS_WQ   = WS_XN + (size_t)S_ * D_ * 2;
constexpr size_t WS_ST   = WS_WQ + (size_t)16 * 18 * 7 * 1024;
constexpr size_t WS_XM   = WS_ST + (size_t)S_ * 16 * 4;
constexpr size_t WS_WM   = WS_XM + (size_t)S_ * 4;
constexpr size_t WS_CNT  = WS_WM + 8192;
constexpr size_t WS_ACT  = WS_CNT + 512 * 256;
constexpr size_t WS_PART = WS_ACT + (size_t)S_ * F_ * 2;
constexpr size_t PART_SLAB = 9 * 1024;

DI float bf_lo(unsigned v) { return __uint_as_float(v << 16); }
DI float bf_hi(unsigned v) { return __uint_as_float(v & 0xffff0000u); }
DI unsigned pk_bf16(float a, float b) { f32x2 v = {a, b}; return __builtin_bit_cast(unsigned, __builtin_convertvector(v, bf16x2)); }
DI float rbf(float a) { return bf_lo(pk_bf16(a, 0.f)); }
DI float dot2bf(unsigned a, unsigned b, float c) { return __builtin_amdgcn_fdot2_f32_bf16(__builtin_bit_cast(bf16x2, a), __builtin_bit_cast(bf16x2, b), c, false); }
DI s16x4 tr_read(LAS char* p) { return __builtin_amdgcn_ds_read_tr16_b64_v4i16((LAS s16x4*)p); }
DI bf16x8 cat8(s16x4 a, s16x4 b) { return __builtin_bit_cast(bf16x8, __builtin_shufflevector(a, b, 0, 1, 2, 3, 4, 5, 6, 7)); }
DI void glds16(const void* g, LAS char* l) {
  unsigned keep; const unsigned la = __builtin_amdgcn_readfirstlane((unsigned)(size_t)l);
  asm volatile("s_mov_b32 %0, m0\n\ts_mov_b32 m0, %2\n\ts_nop 0\n\tglobal_load_lds_dwordx4 %1, off\n\ts_mov_b32 m0, %0" : "=&s"(keep) : "v"(g), "s"(la) : "memory");
}
DI u32x4 tr_frag(LAS char* p, int second) {
  const u32x2 a = __builtin_bit_cast(u32x2, tr_read(p)), b = __builtin_bit_cast(u32x2, tr_read(p + second));
  return (u32x4){a[0], a[1], b[0], b[1]};
}
DI u32x4 tr_frag2(LAS char* p0, LAS char* p1) {
  const u32x2 a = __builtin_bit_cast(u32x2, tr_read(p0)), b = __builtin_bit_cast(u32x2, tr_read(p1));
  return (u32x4){a[0], a[1], b[0], b[1]};
}
#define BF8(v) __builtin_bit_cast(bf16x8, (v))
DI void touch4(const void* g, LAS char* l) {
  unsigned keep; const unsigned la = __builtin_amdgcn_readfirstlane((unsigned)(size_t)l);
  asm volatile("s_mov_b32 %0, m0\n\ts_mov_b32 m0, %2\n\ts_nop 0\n\tglobal_load_lds_dword %1, off\n\ts_mov_b32 m0, %0" : "=&s"(keep) : "v"(g), "s"(la) : "memory");
}
DI float wave_sum(float v) {
#define DPP_ADD(ctrl) v += __builtin_bit_cast(float, __builtin_amdgcn_mov_dpp(__builtin_bit_cast(int, v), ctrl, 0xF, 0xF, true))
  DPP_ADD(0xB1); DPP_ADD(0x4E); DPP_ADD(0x141); DPP_ADD(0x140);
#undef DPP_ADD
  const int iv = __builtin_bit_cast(int, v);
  return (__builtin_bit_cast(float, __builtin_amdgcn_readlane(iv, 0)) + __builtin_bit_cast(float, __builtin_amdgcn_readlane(iv, 16))) +
         (__builtin_bit_cast(float, __builtin_amdgcn_readlane(iv, 32)) + __builtin_bit_cast(float, __builtin_amdgcn_readlane(iv, 48)));
}
#define CODE_ADDR(sym) ({ unsigned long long _pc; int _off; asm volatile("s_getpc_b64 %0\n\ts_mov_b32 %1, " sym "@rel32@lo+4" : "=s"(_pc), "=s"(_off)); (const char*)(_pc + (long long)_off); })
DI void touch_code(const char* lo, const char* hi, int lane, LAS char* scr) {
  const long long n = (hi - lo) >> 7;
  if (n > 0 && n <= 512) {
#pragma unroll 1
    for (int i = 0; i < (int)n; i += 64) { const int l = i + lane < (int)n ? i + lane : (int)n - 1; touch4(lo + (size_t)l * 128, scr); }
  }
}
#define MFMA16(a, b, c) __builtin_amdgcn_mfma_f32_16x16x32_bf16((a), (b), (c), 0, 0, 0)
#define MFMA32(a, b, c) __builtin_amdgcn_mfma_f32_32x32x16_bf16((a), (b), (c), 0, 0, 0)
#define BAR() __builtin_amdgcn_s_barrier()
#define SCHED0() __builtin_amdgcn_sched_barrier(0)
#define WAITV(n) asm volatile("s_waitcnt vmcnt(" #n ")" ::: "memory")
#define WAITVL(n) asm volatile("s_waitcnt vmcnt(" #n ") lgkmcnt(0)" ::: "memory")
#define WAITL0() asm volatile("s_waitcnt lgkmcnt(0)" ::: "memory")

#define STAMP_DECL
#define STAMP(i) do {} while (0)
#define STAMP_OUT(kid) do {} while (0)
DI unsigned fm32_piece(int tok, int c4, int KB16) {
  const int pi = (c4 & 15) >> 2;
  return (unsigned)(((tok >> 5) * KB16 + (c4 >> 4)) * 1024 + (((pi & 1) * 32 + (tok & 31)) * 16) + (pi >> 1) * 8);
}

#define WS_RSRC(ws) __builtin_amdgcn_make_buffer_rsrc((void*)(ws), 0, (int)WS_ACT, 0x00020000)
#define ST16(rs, off, v) __builtin_amdgcn_raw_buffer_store_b128((v), (rs), (int)(off), 0, 16)
#define ST8(rs, off, v) __builtin_amdgcn_raw_buffer_store_b64((v), (rs), (int)(off), 0, 16)
#define ST4(rs, off, v) __builtin_amdgcn_raw_buffer_store_b32((v), (rs), (int)(off), 0, 16)

constexpr int K0_SCR = 9216;
constexpr int K0_LDS = K0_SCR + 256;

extern "C" __global__ __launch_bounds__(512, 2) void k0(const u16* __restrict__ x, const u16* __restrict__ wqkv, const u16* __restrict__ wo,
                                                        const u16* __restrict__ wg, const u16* __restrict__ wu, char* __restrict__ ws) {
  extern __shared__ __attribute__((aligned(16))) char smem[];
  LAS char* lds = (LAS char*)smem;
  const int tid = threadIdx.x, lane = tid & 63, w = __builtin_amdgcn_readfirstlane(tid >> 6);
  const int bx = blockIdx.x;
  const auto rs = WS_RSRC(ws);
  STAMP_DECL STAMP(0);
  if (bx < 256) {
    const int row = bx * 8 + w, l36 = lane < 36 ? lane : 35;
    const char* xp = (const char*)x + (size_t)row * (D_ * 2) + l36 * 32;
    const u32x4 va = *(const u32x4*)xp, vb = *(const u32x4*)(xp + 16);
    f32x2 v[8];
#pragma unroll
    for (int e = 0; e < 4; ++e) { v[e] = (f32x2){bf_lo(va[e]), bf_hi(va[e])}; v[4 + e] = (f32x2){bf_lo(vb[e]), bf_hi(vb[e])}; }
    const f32x2 s2 = ((v[0] + v[1]) + (v[2] + v[3])) + ((v[4] + v[5]) + (v[6] + v[7]));
    const float mean = wave_sum(lane < 36 ? s2[0] + s2[1] : 0.f) * (1.0f / D_), mb = rbf(mean);
    f32x2 q2 = {0.f, 0.f};
    unsigned pk[8];
#pragma unroll
    for (int e = 0; e < 8; ++e) { const f32x2 d = v[e] - mb; q2 = __builtin_elementwise_fma(d, d, q2); pk[e] = pk_bf16(d[0], d[1]); }
    const float dm = mean - mb;
    const float var = rbf(fmaxf(wave_sum(lane < 36 ? q2[0] + q2[1] : 0.f) * (1.0f / D_) - dm * dm, 0.f));
    const float rsd = __builtin_amdgcn_rcpf(rbf(__builtin_amdgcn_sqrtf(rbf(var + 1e-5f))));
#pragma unroll
    for (int e = 0; e < 8; ++e) pk[e] = pk_bf16(bf_lo(pk[e]) * rsd, bf_hi(pk[e]) * rsd);
    if (lane < 36) {
      *(LAS u32x4*)(lds + lane * 256 + w * 16) = (u32x4){pk[0], pk[1], pk[4], pk[5]};
      *(LAS u32x4*)(lds + lane * 256 + 128 + w * 16) = (u32x4){pk[2], pk[3], pk[6], pk[7]};
    }
    if (lane == 0) ST4(rs, WS_XM + row * 4, __float_as_uint(mean));
    WAITL0();
    STAMP(1);
    BAR(); asm volatile("" ::: "memory");
    STAMP(2);
#pragma unroll
    for (int i = 0; i < 2; ++i) {
      const int t = tid + i * 512;
      if (t < 576) {
        const u32x4 c = *(LAS u32x4*)(lds + t * 16);
        ST16(rs, WS_XN + ((bx >> 2) * 36 + (t >> 4)) * 1024 + (((t >> 3) & 1) * 32 + (bx & 3) * 8 + (t & 7)) * 16, c);
      }
    }
    STAMP(3); STAMP_OUT(4);
  } else if (bx < 472) {
    const int u = bx - 256, ks = (u * 5462) >> 16, rem = u - ks * 12, seg = rem >> 2, hq = rem & 3;
#pragma unroll
    for (int i = 0; i < 2; ++i) {
      const int cc = i * 8 + w;
      if (cc < 9) {
        const int q = cc * 64 + lane, rr = (q * 3641) >> 16, ch = q - rr * 18;
        glds16((const char*)wqkv + (size_t)(ks * 32) * 3456 + seg * 1152 + hq * 288 + (unsigned)(rr * 3456 + ch * 16), lds + cc * 1024);
      }
    }
    const int g = lane >> 4, r = lane & 15;
    const unsigned adr = (unsigned)((16 * (g >> 1) + 4 * (g & 1) + (r >> 2)) * 288 + (r & 3) * 8);
    int dsto[2];
#pragma unroll
    for (int i = 0; i < 2; ++i) {
      const int c = (i * 8 + w) * 16 + r, hl = (c * 1821) >> 16, pcol = seg * 36 + c - hl * 36;
      dsto[i] = (int)(WS_WQ + (((hq * 4 + hl) * 18 + ks) * 7 + (pcol >> 4)) * 1024 + (g * 16 + (pcol & 15)) * 16);
    }
    WAITV(0);
    BAR(); asm volatile("" ::: "memory");
    ST16(rs, dsto[0], tr_frag(lds + adr + w * 32, 2304));
    if (w == 0) ST16(rs, dsto[1], tr_frag(lds + adr + 256, 2304));
  } else if (bx >= 544) {
    const int b2 = bx - 544, xcd = b2 & 7, jj = b2 >> 3;
    if (w < 6) { int ln = w * 64 + lane; ln = ln < 324 ? ln : 323; const int k18 = (ln * 3641) >> 16;
      touch4((const char*)((xcd & 1) ? wu : wg) + (size_t)(k18 * 32 + jj) * (F_ * 2) + (xcd >> 1) * 2304 + (ln - k18 * 18) * 128, lds + K0_SCR); }
  } else {
    const int wrow = (bx - 472) * 8 + w, l36 = lane < 36 ? lane : 35;
    const char* rp = (const char*)wo + (size_t)wrow * (D_ * 2) + l36 * 32;
    const u32x4 wa = *(const u32x4*)rp, wb = *(const u32x4*)(rp + 16);
    if (bx == 472 && tid < 272) ST4(rs, WS_CNT + tid * 256, 0u);
    if (bx < 480 + 8 && w == 7) touch_code(CODE_ADDR("k0"), CODE_ADDR("kC"), lane, lds + K0_SCR);
    float t = 0.f;
#pragma unroll
    for (int e = 0; e < 4; ++e) { t = dot2bf(wa[e], 0x3f803f80u, t); t = dot2bf(wb[e], 0x3f803f80u, t); }
    t = wave_sum(lane < 36 ? t : 0.f) * (1.0f / D_);
    const float hi = rbf(t);
    if (lane == 0) ST8(rs, WS_WM + wrow * 8, ((u32x2){pk_bf16(hi, t - hi), 0u}));
  }
}

constexpr int KA_STAGE = 16384;
constexpr int KA_QROW = 304;
constexpr int KA_OOFF = 144 * KA_QROW;
constexpr int KA_WO = 54272;
constexpr int KA_WM = KA_WO + 82944;
constexpr int KA_XR = KA_WM + 5120;
constexpr int KA_SCR = KA_XR + 9216;
constexpr int KA_LDS = KA_SCR + 256;
constexpr int KA_RED = 0;

template <int NB>
DI void kA_loop(LAS char* lds, const char* s0, const char* s1, unsigned st0, unsigned st1, int w, int lane, f32x4 (&acc)[2][4], f32x4& acch) {
  const int g = lane >> 4, r = lane & 15, wm = w & 3;
#define KA_G0(sl) do { glds16(s0, lds + (sl) * KA_STAGE + w * 1024); s0 += st0; } while (0)
#define KA_G1(sl) do { glds16(s1, lds + (sl) * KA_STAGE + 8192 + w * 1024); s1 += st1; } while (0)
  const unsigned xadr = (unsigned)(wm * 2048 + (g >> 1) * 1024 + ((g & 1) * 32 + r) * 16);
  const unsigned hadr = (unsigned)(8192 + lane * 16);
  const unsigned wadr = (unsigned)(9216 + (NB == 4 ? 0 : 4096) + lane * 16);
  const unsigned whadr = wadr + (unsigned)((NB == 3 && wm == 3 ? 2 : wm) * 1024);
  u32x4 xP[3], wP[5], xQ[3], wQ[5];
#define KA_RD(dst, base, off) dst = *(LAS u32x4*)((base) + (off))
#define KA_M(XC, WC, b) do { acc[0][b] = MFMA16(BF8(WC[b]), BF8(XC[0]), acc[0][b]); acc[1][b] = MFMA16(BF8(WC[b]), BF8(XC[1]), acc[1][b]); } while (0)
#define KA_STEP(XC, WC, XN, WN, nb, G0, G1) do { \
    KA_M(XC, WC, 0); SCHED0(); KA_RD(WN[0], nb, wadr); KA_RD(XN[0], nb, xadr); KA_RD(XN[1], nb, xadr + 256); G0; SCHED0(); \
    KA_M(XC, WC, 1); SCHED0(); KA_RD(WN[1], nb, wadr + 1024); KA_RD(WN[2], nb, wadr + 2048); SCHED0(); \
    KA_M(XC, WC, 2); SCHED0(); if (NB == 4) KA_RD(WN[3], nb, wadr + 3072); KA_RD(XN[2], nb, hadr); G1; SCHED0(); \
    if (NB == 4) KA_M(XC, WC, 3); \
    acch = MFMA16(BF8(WC[4]), BF8(XC[2]), acch); SCHED0(); KA_RD(WN[4], nb, whadr); SCHED0(); } while (0)
  KA_RD(wP[0], lds, wadr); KA_RD(xP[0], lds, xadr); KA_RD(xP[1], lds, xadr + 256); KA_RD(wP[1], lds, wadr + 1024); KA_RD(wP[2], lds, wadr + 2048);
  if (NB == 4) KA_RD(wP[3], lds, wadr + 3072);
  KA_RD(xP[2], lds, hadr); KA_RD(wP[4], lds, whadr);
  KA_STEP(xP, wP, xQ, wQ, lds + KA_STAGE, KA_G0(5), KA_G1(5));
  int cur = 2, fre = 0;
#pragma unroll 1
  for (int t = 2; t < 14; t += 2) {
    WAITVL(4);
    BAR(); asm volatile("" ::: "memory");
    LAS char* sb = lds + cur * KA_STAGE;
    KA_STEP(xQ, wQ, xP, wP, sb, KA_G0(fre), KA_G1(fre));
    KA_STEP(xP, wP, xQ, wQ, sb + KA_STAGE, KA_G0(fre + 1), KA_G1(fre + 1));
    fre = cur; cur = cur == 4 ? 0 : cur + 2;
  }
#pragma unroll 1
  for (int t = 14; t < 18; t += 2) {
    if (t == 14) WAITVL(4); else WAITVL(0);
    BAR(); asm volatile("" ::: "memory");
    LAS char* sb = lds + cur * KA_STAGE;
    KA_STEP(xQ, wQ, xP, wP, sb, (void)0, (void)0);
    KA_STEP(xP, wP, xQ, wQ, sb + KA_STAGE, (void)0, (void)0);
    cur = cur == 4 ? 0 : cur + 2;
  }
  KA_M(xQ, wQ, 0); KA_M(xQ, wQ, 1); KA_M(xQ, wQ, 2);
  if (NB == 4) KA_M(xQ, wQ, 3);
  acch = MFMA16(BF8(wQ[4]), BF8(xQ[2]), acch);
}

extern "C" __global__ __launch_bounds__(576, 1) void kA(const u16* __restrict__ x, const u16* __restrict__ wo, const u16* __restrict__ wg,
                                                        const u16* __restrict__ wu, char* __restrict__ ws) {
  extern __shared__ __attribute__((aligned(16))) char smem[];
  LAS char* lds = (LAS char*)smem;
  const int tid = threadIdx.x, lane = tid & 63, w = __builtin_amdgcn_readfirstlane(tid >> 6);
  const int bx = blockIdx.x, xcd = bx & 7, jj = bx >> 3;
  const int mt = (xcd >> 1) * 4 + (jj & 3), hp = (xcd & 1) * 8 + (jj >> 2);
  const int g = lane >> 4, r = lane & 15, wm = w & 3, wn = w >> 2;
  const auto rs = WS_RSRC(ws);
  STAMP_DECL STAMP(0);
  if (w == 8) {
    const int th = hp & 1, n8 = hp >> 1;
    unsigned po[9];
#pragma unroll
    for (int j = 0; j < 9; ++j) {
      const int q = j * 64 + lane, k = (q * 7282) >> 16;
      po[j] = (unsigned)(k * (D_ * 2) + (q - k * 9) * 16 + n8 * 144);
    }
#define KA_WOG(gi) do { _Pragma("unroll") for (int _j = 0; _j < 9; ++_j) glds16((const char*)wo + (size_t)(gi) * (64 * D_ * 2) + po[_j], lds + KA_WO + ((gi) * 9 + _j) * 1024); } while (0)
#pragma unroll 1
    for (int i = 0; i < 9; ++i) {
      BAR();
      if (i >= 1 && i < 5) KA_WOG(i + 4);
      else if (i == 5) {
#pragma unroll
        for (int j = 0; j < 9; ++j) glds16((const char*)x + (size_t)(mt * 128 + th * 64) * (D_ * 2) + po[j], lds + KA_XR + j * 1024);
      } else if (i == 6) {
#pragma unroll
        for (int j = 0; j < 5; ++j) { const int o = j * 1024 + lane * 16; glds16(ws + WS_WM + (o < 4608 ? o : 0), lds + KA_WM + j * 1024); }
      }
    }
    BAR();
    KA_WOG(0); KA_WOG(1);
    BAR();
    KA_WOG(2); KA_WOG(3); KA_WOG(4);
    touch_code(CODE_ADDR("kC"), CODE_ADDR("kD"), lane, lds + KA_SCR);
#define KA_PF(m, i0, i1) do { _Pragma("unroll") for (int _i = i0; _i < i1; ++_i) { int ln = _i * 64 + lane; ln = ln < 324 ? ln : 323; const int k18 = (ln * 3641) >> 16; \
      touch4((const char*)(((xcd ^ (m)) & 1) ? wu : wg) + (size_t)(k18 * 32 + jj) * (F_ * 2) + (xcd >> 1) * 2304 + (ln - k18 * 18) * 128, lds + KA_SCR); } } while (0)
#pragma unroll
    for (int i = 0; i < 2; ++i) {
      const int l54 = lane < 54 ? lane : 53, k3 = (l54 * 3641) >> 16;
      touch4((const char*)(i ? wu : wg) + (size_t)(k3 * 32 + jj) * (F_ * 2) + (xcd >> 1) * 2304 + (l54 - k3 * 18) * 128, lds + KA_SCR);
    }
    BAR();
    WAITV(0);
    BAR(); BAR(); BAR();
    STAMP(9); STAMP_OUT(0);
    return;
  }
  const char* s0 = ws + WS_XN + (size_t)((mt * 4 + (w >> 1)) * 36 + (w & 1)) * 1024 + lane * 16;
  const char* s1 = w ? ws + WS_WQ + (size_t)(hp * 126 + (w - 1)) * 1024 + lane * 16
                     : ws + WS_XN + (size_t)((mt ? mt * 4 - 1 : 0) * 36 + (g >> 1)) * 1024 + ((g & 1) * 32 + 16 + r) * 16;
  const unsigned st0 = 2048, st1 = w ? 7168u : 2048u;
#pragma unroll
  for (int st = 0; st < 5; ++st) { KA_G0(st); KA_G1(st); }
  STAMP(1);
  unsigned sto[5][2];
#pragma unroll
  for (int b = 0; b < 5; ++b) {
    const int pd = 4 * (4 * wn + (b < 4 ? b : wm)) + g, sg = (pd * 57) >> 9, c0 = (pd - sg * 9) * 4;
#pragma unroll
    for (int e = 0; e < 2; ++e) {
      const int c = c0 + 2 * e, hd = c >= 18 ? 1 : 0, d = c - 18 * hd;
      sto[b][e] = (unsigned)(r * KA_QROW + (pd < 27 ? (sg * 2 + hd) * 48 + d * 2 : 288 + 4 * e));
    }
  }
  f32x4 acc[2][4], acch = {0.f, 0.f, 0.f, 0.f};
#pragma unroll
  for (int a = 0; a < 2; ++a)
#pragma unroll
    for (int b = 0; b < 4; ++b) acc[a][b] = (f32x4){0.f, 0.f, 0.f, 0.f};
  WAITV(6);
  BAR(); asm volatile("" ::: "memory");
  STAMP(2);
  if (wn == 0) kA_loop<4>(lds, s0, s1, st0, st1, w, lane, acc, acch);
  else kA_loop<3>(lds, s0, s1, st0, st1, w, lane, acc, acch);
  STAMP(3);
  WAITL0();
  BAR(); asm volatile("" ::: "memory");
  const int tok = mt * 128 + (hp & 1) * 64 + wm * 16 + r;
  const float xm = *(const float*)(ws + WS_XM + (size_t)tok * 4);
  asm volatile("" ::: "memory");
  unsigned pa[5], pm4[5];
  const unsigned p4s = (r & 3) < 2 ? 288u * 144u : 288u * 8u, p4t = (r & 3) < 2 ? 576u : 32u;
#pragma unroll
  for (int i = 0; i < 5; ++i) {
    const int c = 8 * i + 4 * wn + g, h5 = (c * 205) >> 10, krow = 36 * h5 + 8 * (c - 5 * h5) + (r >> 2), p = r & 3;
    pa[i] = (unsigned)(KA_WO + krow * 144 + p * 8);
    pm4[i] = p < 2 ? (unsigned)(KA_WO + krow * 144 + 128 + p * 8) : (unsigned)(KA_WM + krow * 8);
  }
  {
    LAS char* row0 = lds + (2 * wm + 1) * 16 * KA_QROW;
#pragma unroll
    for (int b = 0; b < 4; ++b)
#pragma unroll
      for (int a = 0; a < 2; ++a) {
        *(LAS unsigned*)(row0 + a * 16 * KA_QROW + sto[b][0]) = pk_bf16(acc[a][b][0], acc[a][b][1]);
        *(LAS unsigned*)(row0 + a * 16 * KA_QROW + sto[b][1]) = pk_bf16(acc[a][b][2], acc[a][b][3]);
      }
    *(LAS unsigned*)(lds + sto[4][0]) = pk_bf16(acch[0], acch[1]); *(LAS unsigned*)(lds + sto[4][1]) = pk_bf16(acch[2], acch[3]);
  }
  WAITL0();
  BAR(); asm volatile("" ::: "memory");
  STAMP(4);
  {
    const int hf = tid & 1, t = (tid >> 1) & 127, hd = tid >> 8, T = mt * 128 + t;
    LAS char* base = lds + (t + 16) * KA_QROW + hd * 48;
    const u32x4 qa = *(LAS u32x4*)(base), qb = *(LAS u32x4*)(base + 16); const unsigned qc = *(LAS unsigned*)(base + 32);
    float lg[8];
#pragma unroll
    for (int i = 0; i < 4; ++i) {
      const int wv = hf * 4 + i;
      LAS char* kp = base - wv * 2 * KA_QROW + 96;
      const u32x4 a = *(LAS u32x4*)(kp), b = *(LAS u32x4*)(kp + 16); const unsigned c = *(LAS unsigned*)(kp + 32);
      float s = 0.f;
      s = dot2bf(qa[0], a[0], s); s = dot2bf(qa[1], a[1], s); s = dot2bf(qa[2], a[2], s); s = dot2bf(qa[3], a[3], s);
      s = dot2bf(qb[0], b[0], s); s = dot2bf(qb[1], b[1], s); s = dot2bf(qb[2], b[2], s); s = dot2bf(qb[3], b[3], s);
      s = dot2bf(qc, c, s);
      s = rbf(rbf(s) * 0.23570226039551584f);
      s = (T - 2 * wv >= 0) ? s : -1.0e9f;
      const float o = __builtin_bit_cast(float, __builtin_amdgcn_mov_dpp(__builtin_bit_cast(int, s), 0xB1, 0xF, 0xF, true));
      lg[i] = hf ? o : s; lg[4 + i] = hf ? s : o;
    }
    const float mx = fmaxf(fmaxf(fmaxf(lg[0], lg[1]), fmaxf(lg[2], lg[3])), fmaxf(fmaxf(lg[4], lg[5]), fmaxf(lg[6], lg[7])));
    float sm = 0.f;
#pragma unroll
    for (int wv = 0; wv < 8; ++wv) { lg[wv] = __builtin_amdgcn_exp2f((lg[wv] - mx) * 1.4426950408889634f); sm += lg[wv]; }
    const float inv = 1.0f / sm;
    float o[10];
#pragma unroll
    for (int d = 0; d < 10; ++d) o[d] = 0.f;
#pragma unroll
    for (int wp = 0; wp < 4; ++wp) {
      const unsigned pp = pk_bf16(lg[2 * wp] * inv, lg[2 * wp + 1] * inv);
      LAS char* v0 = base - (2 * wp) * 2 * KA_QROW + 192 + hf * 16;
      LAS char* v1 = v0 - 2 * KA_QROW;
      const u32x4 a = *(LAS u32x4*)(v0), b = *(LAS u32x4*)(v1); const unsigned a4 = *(LAS unsigned*)(v0 + 16), b4 = *(LAS unsigned*)(v1 + 16);
      const unsigned av[5] = {a[0], a[1], a[2], a[3], a4}, bv[5] = {b[0], b[1], b[2], b[3], b4};
#pragma unroll
      for (int i = 0; i < 5; ++i) {
        o[2 * i]     = dot2bf(pp, __builtin_amdgcn_perm(bv[i], av[i], 0x05040100u), o[2 * i]);
        o[2 * i + 1] = dot2bf(pp, __builtin_amdgcn_perm(bv[i], av[i], 0x07060302u), o[2 * i + 1]);
      }
    }
    LAS char* op = lds + KA_OOFF + t * 80 + hd * 36 + hf * 16;
#pragma unroll
    for (int i = 0; i < 5; ++i) *(LAS unsigned*)(op + 4 * i) = pk_bf16(o[2 * i], o[2 * i + 1]);
    if (hd == 0) *(LAS unsigned*)(lds + KA_OOFF + t * 80 + 72 + hf * 4) = 0u;
  }
  WAITL0();
  BAR(); asm volatile("" ::: "memory");
  STAMP(5);
#pragma unroll
  for (int i = 0; i < 2; ++i) {
    const int id = tid + i * 512;
    if (id < 640) {
      const int mb = (id * 820) >> 16, rem = id - mb * 80;
      const u32x4 v = *(LAS u32x4*)(lds + KA_OOFF + (mb * 16 + (rem & 15)) * 80 + (rem >> 4) * 16);
      ST16(rs, WS_O + ((mt * 8 + mb) * 80 + hp * 5) * 256 + rem * 16, v);
    }
  }
  WAITV(0);
  BAR(); asm volatile("" ::: "memory");
  STAMP(6);
  GAS unsigned* cnt = (GAS unsigned*)(ws + WS_CNT) + (256 + mt) * 64;
  if (tid == 0) __hip_atomic_fetch_add(cnt, 1u, __ATOMIC_RELAXED, __HIP_MEMORY_SCOPE_AGENT);
  u32x4 wf[4][5];
#define KA_P2R(set, i) do { _Pragma("unroll") for (int _b = 0; _b < 4; ++_b) wf[set][_b] = tr_frag(lds + pa[(i) % 5] + ((i) / 5) * (288 * 144) + _b * 32, 576); \
      wf[set][4] = tr_frag2(lds + pm4[(i) % 5] + ((i) / 5) * p4s, lds + pm4[(i) % 5] + ((i) / 5) * p4s + p4t); } while (0)
  KA_P2R(0, 0); KA_P2R(1, 1); KA_P2R(2, 2); KA_P2R(3, 3);
  SCHED0();
  if (w == 0) {
    unsigned spins = 0;
    while (__builtin_amdgcn_readfirstlane(__hip_atomic_load(cnt, __ATOMIC_RELAXED, __HIP_MEMORY_SCOPE_AGENT)) < 16u) {
      if (++spins > 2000000u) break;
      __builtin_amdgcn_s_sleep(1);
    }
  }
  asm volatile("" ::: "memory");
  BAR(); asm volatile("" ::: "memory");
  STAMP(7);
  {
    const int n8 = hp >> 1;
    const int oo = (int)(WS_O + ((tok >> 4) * 80 + 4 * wn) * 256 + lane * 16);
    u32x4 of[10];
#pragma unroll
    for (int i = 0; i < 10; ++i) of[i] = __builtin_amdgcn_raw_buffer_load_b128(rs, oo + i * 2048, 0, 16);
    f32x4 c[5];
#pragma unroll
    for (int b = 0; b < 5; ++b) c[b] = (f32x4){0.f, 0.f, 0.f, 0.f};
#define KA_P2M(set, i) do { _Pragma("unroll") for (int _b = 0; _b < 5; ++_b) c[_b] = MFMA16(BF8(wf[set][_b]), BF8(of[i]), c[_b]); } while (0)
    SCHED0();
#pragma unroll
    for (int i = 0; i < 10; ++i) {
      KA_P2M(i & 3, i); SCHED0();
      if (i + 4 < 10) KA_P2R(i & 3, i + 4);
      SCHED0();
    }
    STAMP(8);
    if (wn == 1) {
#pragma unroll
      for (int b = 0; b < 5; ++b) *(LAS f32x4*)(lds + KA_RED + (wm * 5 + b) * 1024 + lane * 16) = c[b];
    }
    WAITL0();
    BAR(); asm volatile("" ::: "memory");
    if (wn == 0) {
#pragma unroll
      for (int b = 0; b < 5; ++b) c[b] += *(LAS f32x4*)(lds + KA_RED + (wm * 5 + b) * 1024 + lane * 16);
      const float mean = xm + __shfl(c[4][0] + c[4][1], 32 + r), mb = rbf(mean);
      LAS char* xq = lds + KA_XR + (wm * 16 + r) * 144 + g * 8;
      unsigned hw_[10], cw_[10];
      f32x2 qv = {0.f, 0.f};
#pragma unroll
      for (int b = 0; b < 5; ++b) {
        const u32x2 xv = *(LAS u32x2*)(xq + ((b < 4 || g < 2) ? b * 32 : 0));
#pragma unroll
        for (int e = 0; e < 2; ++e) {
          const unsigned a = pk_bf16(c[b][2 * e], c[b][2 * e + 1]);
          const unsigned hh = pk_bf16(bf_lo(xv[e]) + bf_lo(a), bf_hi(xv[e]) + bf_hi(a));
          const f32x2 hv = {bf_lo(hh), bf_hi(hh)};
          const f32x2 d = hv - mean, dc = hv - mb;
          hw_[2 * b + e] = hh; cw_[2 * b + e] = pk_bf16(dc[0], dc[1]);
          if (b < 4) qv += d * d; else qv += g < 2 ? d * d : (f32x2){0.f, 0.f};
        }
      }
      const int ho = ((tok >> 4) * 72 + n8 * 9 + 2 * (g & 1) + (g >> 1)) * 256 + r * 16;
#pragma unroll
      for (int b = 0; b < 4; b += 2) {
        const auto c0 = __builtin_amdgcn_permlane16_swap(cw_[2 * b], cw_[2 * b + 2], false, false), c1 = __builtin_amdgcn_permlane16_swap(cw_[2 * b + 1], cw_[2 * b + 3], false, false);
        const auto h0 = __builtin_amdgcn_permlane16_swap(hw_[2 * b], hw_[2 * b + 2], false, false), h1 = __builtin_amdgcn_permlane16_swap(hw_[2 * b + 1], hw_[2 * b + 3], false, false);
        ST16(rs, WS_HC + ho + b * 512, ((u32x4){c0[0], c1[0], c0[1], c1[1]}));
        ST16(rs, WS_H + ho + b * 512, ((u32x4){h0[0], h1[0], h0[1], h1[1]}));
      }
      const auto s0 = __builtin_amdgcn_permlane32_swap(cw_[8], hw_[8], false, false), s1 = __builtin_amdgcn_permlane32_swap(cw_[9], hw_[9], false, false);
      ST8(rs, (g < 2 ? WS_HC : WS_H) + ((tok >> 4) * 72 + n8 * 9 + 8) * 256 + r * 16 + (g & 1) * 8, ((u32x2){s0[0], s1[0]}));
      float q = qv[0] + qv[1];
      q += __shfl_xor(q, 16); q += __shfl_xor(q, 32);
      if (g == 0) ST4(rs, WS_ST + (tok * 8 + n8) * 4, __float_as_uint(q));
    }
  }
  STAMP(9); STAMP_OUT(0);
}

constexpr int KC_UPOFF = 16384 + 9216 + 128;
constexpr int KC_STAGE = 35072;
constexpr int KC_STOFF = 4 * KC_STAGE;
constexpr int KC_SCR = KC_STOFF + 8192;
constexpr int KC_LDS = KC_SCR + 256;

DI f32x2 geglu2(f32x2 a, f32x2 b, f32x2 k0, f32x2 k1, f32x2 ik2) {
  const f32x2 tt = a * __builtin_elementwise_fma(a * a, k1, k0);
  const f32x2 e = {__builtin_amdgcn_exp2f(tt[0]), __builtin_amdgcn_exp2f(tt[1])};
  const f32x2 d = __builtin_elementwise_fma(e, ik2, ik2);
  const f32x2 rr = {__builtin_amdgcn_rcpf(d[0]), __builtin_amdgcn_rcpf(d[1])};
  return (a * b) * rr;
}
DI void ln_consts(const LAS char* sp_, f32x2& k0, f32x2& k1, f32x2& ik2) {
  const LAS f32x4* sp = (const LAS f32x4*)sp_;
  const f32x4 s4 = sp[0] + sp[1];
  const float var = rbf(((s4[0] + s4[1]) + (s4[2] + s4[3])) * (1.0f / D_));
  const float sd = rbf(__builtin_amdgcn_sqrtf(rbf(var + 1e-5f)));
  const float rs = __builtin_amdgcn_rcpf(sd);
  const float k0s = -2.3022081981f * rs;
  const float k1s = -0.1029432397f * rs * rs * rs;
  const float ik2s = sd * sd;
  k0 = (f32x2){k0s, k0s}; k1 = (f32x2){k1s, k1s}; ik2 = (f32x2){ik2s, ik2s};
}

extern "C" __global__ __launch_bounds__(768, 1) void kC(const u16* __restrict__ wg, const u16* __restrict__ wu, const u16* __restrict__ wd, char* __restrict__ ws) {
  extern __shared__ __attribute__((aligned(16))) char smem[];
  LAS char* lds = (LAS char*)smem;
  const int tid = threadIdx.x, lane = tid & 63, w = __builtin_amdgcn_readfirstlane(tid >> 6);
  const int bx = blockIdx.x, xcd = bx & 7, jj = bx >> 3;
  const int pm = (xcd & 1) * 4 + (jj & 3), pn = (xcd >> 1) * 8 + (jj >> 2);
  const int wm = w & 3, wn = w >> 2;

  STAMP_DECL STAMP(0);
  const char* src[3]; unsigned dst[3]; unsigned step[3];
  src[0] = ws + WS_HC + (size_t)(pm * 16 + w) * (72 * 256) + lane * 16; dst[0] = w * 1024; step[0] = 1024;
#define KC_G(i, st) do { glds16(src[i], lds + ((st) & 3) * KC_STAGE + dst[i]); src[i] += step[i]; } while (0)
  KC_G(0, 0);
#pragma unroll
  for (int i = 1; i < 3; ++i) {
    int sl = i * 12 + w; sl = sl > 33 ? 33 : sl;
    if (sl < 16) {
      src[i] = ws + WS_HC + (size_t)(pm * 16 + sl) * (72 * 256) + lane * 16;
      dst[i] = sl * 1024; step[i] = 1024;
    } else {
      const int c = sl - 16, m = c >= 9 ? 1 : 0, cc = c - 9 * m, q = cc * 64 + lane, rr = (q * 3641) >> 16, ch = q - rr * 18;
      const int kr = (rr & 19) | ((rr & 4) << 1) | ((rr & 8) >> 1);
      src[i] = (const char*)(m ? wu : wg) + pn * 288 + (unsigned)(kr * (F_ * 2) + ch * 16);
      dst[i] = (m ? KC_UPOFF : 16384) + cc * 1024; step[i] = 32 * F_ * 2;
    }
    KC_G(i, 0);
  }
#define KC_ISSUE(st) do { KC_G(0, st); KC_G(1, st); KC_G(2, st); } while (0)
  KC_ISSUE(1); KC_ISSUE(2);
  glds16(ws + WS_ST + (size_t)pm * 8192 + (w & 7) * 1024 + lane * 16, lds + KC_STOFF + (w & 7) * 1024);
  const auto ars = __builtin_amdgcn_make_buffer_rsrc((void*)(ws + WS_ACT), 0, S_ * F_ * 2, 0x00020000);
#define WDW 6
#define KC_TOUCH_WD() do { if (w < WDW) {     \
    int ln = w * 64 + lane; ln = ln < 324 ? ln : 323; \
    touch4((const char*)wd + (size_t)(xcd >> 1) * (1152 * D_ * 2) + (size_t)(jj * 324 + ln) * 128, lds + KC_SCR); } \
    else if (w == 6) touch_code(CODE_ADDR("kD"), CODE_ADDR("kD_end_lbl"), lane, lds + KC_SCR); } while (0)

  const int g = lane >> 4, r = lane & 15;
  f32x4 acc[6][4];
#pragma unroll
  for (int a = 0; a < 6; ++a)
#pragma unroll
    for (int b = 0; b < 4; ++b) acc[a][b] = (f32x4){0.f, 0.f, 0.f, 0.f};
  const unsigned xadr = (unsigned)(wm * 4096 + lane * 16);
  const unsigned wadr = (unsigned)(16384 + (16 * (g >> 1) + 4 * (g & 1) + (r >> 2)) * 288 + wn * 96 + (r & 3) * 8);
  u32x4 xf[4], wA[3], wB[3];
#define KC_WOFF(j) ((((j) & 1) ? (KC_UPOFF - 16384) : 0) + ((j) >> 1) * 32)
#define KC_RW(WF, i, j, base) WF[i] = tr_frag((base) + wadr + KC_WOFF(j), 2304)
#define KC_RX(c, base) xf[c] = *(LAS u32x4*)((base) + xadr + (c) * 1024)
#define KC_M3(WF, j0, c) do { acc[j0][c] = MFMA16(BF8(WF[0]), BF8(xf[c]), acc[j0][c]); acc[j0 + 1][c] = MFMA16(BF8(WF[1]), BF8(xf[c]), acc[j0 + 1][c]); \
    acc[j0 + 2][c] = MFMA16(BF8(WF[2]), BF8(xf[c]), acc[j0 + 2][c]); } while (0)
#define KC_HALFA(base, G0, G1) do { \
    KC_M3(wA, 0, 0); SCHED0(); KC_RW(wB, 0, 3, base); SCHED0(); \
    KC_M3(wA, 0, 1); SCHED0(); KC_RW(wB, 1, 4, base); G0; SCHED0(); \
    KC_M3(wA, 0, 2); SCHED0(); KC_RW(wB, 2, 5, base); SCHED0(); \
    KC_M3(wA, 0, 3); SCHED0(); G1; SCHED0(); } while (0)
#define KC_HALFB(nb, G0) do { \
    KC_M3(wB, 3, 0); SCHED0(); KC_RX(0, nb); KC_RW(wA, 0, 0, nb); SCHED0(); \
    KC_M3(wB, 3, 1); SCHED0(); KC_RX(1, nb); KC_RW(wA, 1, 1, nb); SCHED0(); \
    KC_M3(wB, 3, 2); SCHED0(); KC_RX(2, nb); KC_RW(wA, 2, 2, nb); G0; SCHED0(); \
    KC_M3(wB, 3, 3); SCHED0(); KC_RX(3, nb); SCHED0(); } while (0)
  STAMP(1);
  WAITV(7); BAR(); asm volatile("" ::: "memory");
  STAMP(2);
  KC_RX(0, lds); KC_RW(wA, 0, 0, lds); KC_RW(wA, 1, 1, lds); KC_RW(wA, 2, 2, lds); KC_RX(1, lds); KC_RX(2, lds); KC_RX(3, lds);
  KC_ISSUE(3);
  KC_HALFA(lds, (void)0, (void)0);
  STAMP(3);
#pragma unroll 1
  for (int t = 1; t < 15; ++t) {
    WAITVL(6);
    BAR(); asm volatile("" ::: "memory");
    LAS char* sb = lds + (t & 3) * KC_STAGE;
    KC_HALFB(sb, KC_G(0, t + 3));
    KC_HALFA(sb, KC_G(1, t + 3), KC_G(2, t + 3));
  }
  WAITVL(0);
  BAR(); asm volatile("" ::: "memory");
  KC_TOUCH_WD();
#define KC_T0 15
#pragma unroll 1
  for (int t = KC_T0; t < 18; ++t) {
    LAS char* sb = lds + (t & 3) * KC_STAGE;
    KC_HALFB(sb, (void)0);
    KC_HALFA(sb, (void)0, (void)0);
  }
  KC_M3(wB, 3, 0); KC_M3(wB, 3, 1); KC_M3(wB, 3, 2); KC_M3(wB, 3, 3);
  STAMP(4);
#pragma unroll
  for (int cp = 0; cp < 2; ++cp) {
    f32x2 k0[2], k1[2], ik2[2];
#pragma unroll
    for (int e = 0; e < 2; ++e) ln_consts(lds + KC_STOFF + (wm * 64 + cp * 32 + e * 16 + r) * 32, k0[e], k1[e], ik2[e]);
#pragma unroll
    for (int f = 0; f < 3; ++f) {
      u32x2 pc[2];
#pragma unroll
      for (int e = 0; e < 2; ++e) {
        const f32x4 a = acc[2 * f][2 * cp + e], b = acc[2 * f + 1][2 * cp + e];
        const f32x2 o0 = geglu2((f32x2){a[0], a[1]}, (f32x2){b[0], b[1]}, k0[e], k1[e], ik2[e]);
        const f32x2 o1 = geglu2((f32x2){a[2], a[3]}, (f32x2){b[2], b[3]}, k0[e], k1[e], ik2[e]);
        pc[e] = (u32x2){pk_bf16(o0[0], o0[1]), pk_bf16(o1[0], o1[1])};
      }
      const auto s0 = __builtin_amdgcn_permlane32_swap(pc[0][0], pc[1][0], false, false);
      const auto s1 = __builtin_amdgcn_permlane32_swap(pc[0][1], pc[1][1], false, false);
      const u32x4 v = {s0[0], s1[0], s0[1], s1[1]};
      __builtin_amdgcn_raw_buffer_store_b128(v, ars, ((pm * 8 + wm * 2 + cp) * 288 + pn * 9 + wn * 3 + f) * 1024 + ((g & 1) * 32 + (g >> 1) * 16 + r) * 16, 0, 16);
    }
  }
  STAMP(5); STAMP_OUT(2);
}

constexpr int KD_STAGE = 16384 + 18432;
constexpr int KD_LDS = 2 * KD_STAGE + 72 * 1024;

template <int NB, int NG>
DI void kD_loop(LAS char* lds, const char* srcA, const char* const (&srcW)[3], int w, int lane, f32x4 (&acc)[4][5]) {
  const int gk = w >> 2, wm = w & 1, g = lane >> 4, r = lane & 15;
#define KD_GA(i, st) glds16(srcA + (size_t)(i) * (2 * 288 * 1024) + (st) * 4096, lds + ((st) & 3) * KD_STAGE + (i) * 8192 + w * 1024)
#define KD_GW(i, st) glds16(srcW[i] + (size_t)(st) * (64 * D_ * 2), lds + ((st) & 3) * KD_STAGE + 16384 + (i) * 8192 + w * 1024)
#define KD_GW2(st) do { if (NG == 5) glds16(srcW[2] + (size_t)(st) * (64 * D_ * 2), lds + ((st) & 3) * KD_STAGE + 32768 + (w & 1) * 1024); } while (0)
#define KD_WAIT2() do { if (NG == 5) WAITVL(10); else WAITVL(8); } while (0)
  const unsigned xadr = (unsigned)(wm * 8192 + (gk * 2 + (g >> 1)) * 1024 + ((g & 1) * 32 + r) * 16);
  const unsigned wadr = (unsigned)(16384 + (gk * 32 + 16 * (g >> 1) + 4 * (g & 1) + (r >> 2)) * 288 + (NB == 5 ? 0 : 160) + (r & 3) * 8);
  u32x4 xP[4], wP[5], xQ[4], wQ[5];
#define KD_RW(WF, b, base) WF[b] = tr_frag((base) + wadr + (b) * 32, 2304)
#define KD_RX(XF, a, base) XF[a] = *(LAS u32x4*)((base) + xadr + ((a) >> 1) * 4096 + ((a) & 1) * 256)
#define KD_M(XC, WC, b) do { _Pragma("unroll") for (int _a = 0; _a < 4; ++_a) acc[_a][b] = MFMA16(BF8(WC[b]), BF8(XC[_a]), acc[_a][b]); } while (0)
#define KD_STEP(XC, WC, XN, WN, nb, G0, G1, G2, G3) do { \
    KD_M(XC, WC, 0); SCHED0(); KD_RW(WN, 0, nb); KD_RX(XN, 0, nb); KD_RX(XN, 1, nb); G0; SCHED0(); \
    KD_M(XC, WC, 1); SCHED0(); KD_RX(XN, 2, nb); KD_RX(XN, 3, nb); KD_RW(WN, 1, nb); G1; SCHED0(); \
    KD_M(XC, WC, 2); SCHED0(); KD_RW(WN, 2, nb); KD_RW(WN, 3, nb); G2; SCHED0(); \
    KD_M(XC, WC, 3); SCHED0(); if (NB == 5) KD_RW(WN, 4, nb); G3; SCHED0(); \
    if (NB == 5) { KD_M(XC, WC, 4); SCHED0(); } } while (0)
  KD_RW(wP, 0, lds); KD_RX(xP, 0, lds); KD_RX(xP, 1, lds); KD_RX(xP, 2, lds); KD_RX(xP, 3, lds); KD_RW(wP, 1, lds); KD_RW(wP, 2, lds); KD_RW(wP, 3, lds);
  if (NB == 5) KD_RW(wP, 4, lds);
  KD_GA(0, 3); KD_GA(1, 3); KD_GW(0, 3); KD_GW(1, 3); KD_GW2(3);
#pragma unroll 1
  for (int t = 1; t < 15; t += 2) {
    KD_WAIT2();
    BAR(); asm volatile("" ::: "memory");
    KD_STEP(xP, wP, xQ, wQ, lds + (t & 3) * KD_STAGE, do { KD_GA(0, t + 3); KD_GW2(t + 3); } while (0), KD_GA(1, t + 3), KD_GW(0, t + 3), KD_GW(1, t + 3));
    KD_WAIT2();
    BAR(); asm volatile("" ::: "memory");
    KD_STEP(xQ, wQ, xP, wP, lds + ((t + 1) & 3) * KD_STAGE, do { KD_GA(0, t + 4); KD_GW2(t + 4); } while (0), KD_GA(1, t + 4), KD_GW(0, t + 4), KD_GW(1, t + 4));
  }
  KD_WAIT2(); BAR(); asm volatile("" ::: "memory");
  KD_STEP(xP, wP, xQ, wQ, lds + 3 * KD_STAGE, (void)0, (void)0, (void)0, (void)0);
  if (NG == 5) WAITVL(5); else WAITVL(4);
  BAR(); asm volatile("" ::: "memory");
  KD_STEP(xQ, wQ, xP, wP, lds, (void)0, (void)0, (void)0, (void)0);
  WAITVL(0); BAR(); asm volatile("" ::: "memory");
  KD_STEP(xP, wP, xQ, wQ, lds + KD_STAGE, (void)0, (void)0, (void)0, (void)0);
  KD_M(xQ, wQ, 0); KD_M(xQ, wQ, 1); KD_M(xQ, wQ, 2); KD_M(xQ, wQ, 3);
  if (NB == 5) KD_M(xQ, wQ, 4);
}

extern "C" __global__ __launch_bounds__(512, 1) void kD(const u16* __restrict__ wd, char* __restrict__ ws, u16* __restrict__ out) {
  extern __shared__ __attribute__((aligned(16))) char smem[];
  LAS char* lds = (LAS char*)smem;
  const int tid = threadIdx.x, lane = tid & 63, w = __builtin_amdgcn_readfirstlane(tid >> 6);
  const int bx = blockIdx.x, xcd = bx & 7, jj = bx >> 3;
  const int ks = xcd >> 1, mt = (xcd & 1) * 8 + (jj & 7), nt = jj >> 3;
  const int gk = w >> 2, wm = w & 1, wn = ((w >> 1) & 1) ^ gk;
  const int g = lane >> 4, r = lane & 15;

  STAMP_DECL STAMP(0);
  const char* srcA = ws + WS_ACT + (size_t)((mt * 4 + (w >> 2)) * 288 + ks * 72 + (w & 3)) * 1024 + lane * 16;
  KD_GA(0, 0); KD_GA(1, 0);
  const char* srcW[3];
#pragma unroll
  for (int i = 0; i < 3; ++i) {
    const int cc = i < 2 ? i * 8 + w : 16 + (w & 1);
    const int q = cc * 64 + lane, rr = (q * 3641) >> 16, ch = q - rr * 18;
    srcW[i] = (const char*)wd + (size_t)(ks * 1152 * D_ + nt * 144) * 2 + (unsigned)(rr * (D_ * 2) + ch * 16);
  }
  const bool extra = (w == 2 || w == 3);
#define KD_GX(st) do { if (extra) glds16(srcW[2] + (size_t)(st) * (64 * D_ * 2), lds + ((st) & 3) * KD_STAGE + 32768 + (w & 1) * 1024); } while (0)
  KD_GW(0, 0); KD_GW(1, 0); KD_GX(0);
  KD_GA(0, 1); KD_GA(1, 1); KD_GW(0, 1); KD_GW(1, 1); KD_GX(1);
  KD_GA(0, 2); KD_GA(1, 2); KD_GW(0, 2); KD_GW(1, 2); KD_GX(2);
  const char* hb = ws + WS_H + (size_t)((mt * 8 + ks * 2 + (g >> 1)) * 72 + nt * 18) * 256 + r * 16 + (g & 1) * 8;
  const u32x2 ha0 = *(const u32x2*)(hb + w * 512), ha1 = *(const u32x2*)(hb + w * 512 + 256), hb0 = *(const u32x2*)(hb + 4096), hb1 = *(const u32x2*)(hb + 4096 + 256);
  const u32x4 hres0 = {ha0[0], ha0[1], ha1[0], ha1[1]}, hres1 = {hb0[0], hb0[1], hb1[0], hb1[1]};
  asm volatile("" ::: "memory");
  f32x4 acc[4][5];
#pragma unroll
  for (int a = 0; a < 4; ++a)
#pragma unroll
    for (int b = 0; b < 5; ++b) acc[a][b] = (f32x4){0.f, 0.f, 0.f, 0.f};
  STAMP(1);
  if (extra) WAITVL(10); else WAITVL(8);
  BAR(); asm volatile("" ::: "memory");
  STAMP(2);
  if (wn == 0) kD_loop<5, 4>(lds, srcA, srcW, w, lane, acc);
  else if (extra) kD_loop<4, 5>(lds, srcA, srcW, w, lane, acc);
  else kD_loop<4, 4>(lds, srcA, srcW, w, lane, acc);
  STAMP(3);
  const int qk = 2 * wm + gk;
  {
    LAS char* xw = lds + 2 * KD_STAGE + ((qk ^ 1) * 18 + wn * 5) * 1024 + lane * 16;
#pragma unroll
    for (int sb = 0; sb < 2; ++sb)
#pragma unroll
      for (int b = 0; b < 5; ++b)
        if (b < 4 || wn == 0) *(LAS f32x4*)(xw + (sb * 9 + b) * 1024) = gk ? acc[sb][b] : acc[2 + sb][b];
  }
  WAITL0();
  BAR(); asm volatile("" ::: "memory");
  STAMP(4);
  const int tile = mt * 4 + nt;
  const auto prs = __builtin_amdgcn_make_buffer_rsrc((void*)(ws + WS_PART), 0, (int)(64 * 4 * 3 * PART_SLAB), 0x00020000);
  {
    LAS char* xr = lds + 2 * KD_STAGE + (qk * 18 + wn * 5) * 1024 + lane * 16;
    const int sidx = ks - (ks > qk ? 1 : 0);
    const int po = (int)(((tile * 4 + qk) * 3 + sidx) * PART_SLAB) + wn * 5120 + lane * 16;
#pragma unroll
    for (int b = 0; b < 5; ++b)
      if (b < 4 || wn == 0) {
        const f32x4 v0 = (gk ? acc[2][b] : acc[0][b]) + *(LAS f32x4*)(xr + b * 1024);
        const f32x4 v1 = (gk ? acc[3][b] : acc[1][b]) + *(LAS f32x4*)(xr + (9 + b) * 1024);
        if (qk != ks) {
          const auto s0 = __builtin_amdgcn_permlane32_swap(pk_bf16(v0[0], v0[1]), pk_bf16(v1[0], v1[1]), false, false);
          const auto s1 = __builtin_amdgcn_permlane32_swap(pk_bf16(v0[2], v0[3]), pk_bf16(v1[2], v1[3]), false, false);
          __builtin_amdgcn_raw_buffer_store_b128((u32x4){s0[0], s1[0], s0[1], s1[1]}, prs, po + b * 1024, 0, 16);
        } else {
          *(LAS f32x4*)(lds + (wn * 5 + b) * 2048 + lane * 16) = v0;
          *(LAS f32x4*)(lds + (wn * 5 + b) * 2048 + 1024 + lane * 16) = v1;
        }
      }
  }
  STAMP(5);
  WAITVL(0);
  BAR(); asm volatile("" ::: "memory");
  STAMP(6);
  GAS unsigned* cnt = (GAS unsigned*)(ws + WS_CNT);
  if (tid < 4 && tid != ks) __hip_atomic_fetch_add(cnt + (tile * 4 + tid) * 64, 1u, __ATOMIC_RELAXED, __HIP_MEMORY_SCOPE_AGENT);
  if (w == 0) {
    unsigned spins = 0;
    while (__builtin_amdgcn_readfirstlane(__hip_atomic_load(cnt + (tile * 4 + ks) * 64, __ATOMIC_RELAXED, __HIP_MEMORY_SCOPE_AGENT)) < 3u) {
      if (++spins > 2000000u) break;
      __builtin_amdgcn_s_sleep(1);
    }
  }
  asm volatile("" ::: "memory");
  BAR(); asm volatile("" ::: "memory");
  STAMP(7);
  u32x4 pp[2][3];
#pragma unroll
  for (int i = 0; i < 2; ++i)
#pragma unroll
    for (int q = 0; q < 3; ++q)
      if (i == 0 || w == 1) pp[i][q] = __builtin_amdgcn_raw_buffer_load_b128(prs, (int)(((tile * 4 + ks) * 3 + q) * PART_SLAB) + (i ? 8 : w) * 1024 + lane * 16, 0, 16);
#pragma unroll
  for (int i = 0; i < 2; ++i) {
    if (i == 1 && w != 1) break;
    const int grp = i ? 8 : w;
    const u32x4 hr = i ? hres1 : hres0;
    u16* op = out + (size_t)(mt * 128 + ks * 32 + (g >> 1) * 16 + r) * D_ + nt * 144 + grp * 16 + 4 * (g & 1);
#pragma unroll
    for (int hf = 0; hf < 2; ++hf) {
      f32x4 v = *(LAS f32x4*)(lds + grp * 2048 + (g >> 1) * 1024 + (((g & 1) + 2 * hf) * 16 + r) * 16);
#pragma unroll
      for (int q = 0; q < 3; ++q) v += (f32x4){bf_lo(pp[i][q][2 * hf]), bf_hi(pp[i][q][2 * hf]), bf_lo(pp[i][q][2 * hf + 1]), bf_hi(pp[i][q][2 * hf + 1])};
      const unsigned a0 = pk_bf16(v[0], v[1]), a1 = pk_bf16(v[2], v[3]);
      const unsigned o0 = pk_bf16(bf_lo(hr[2 * hf]) + bf_lo(a0), bf_hi(hr[2 * hf]) + bf_hi(a0));
      const unsigned o1 = pk_bf16(bf_lo(hr[2 * hf + 1]) + bf_lo(a1), bf_hi(hr[2 * hf + 1]) + bf_hi(a1));
      *(u32x2*)(op + 8 * hf) = (u32x2){o0, o1};
    }
  }
  STAMP(8); STAMP_OUT(3);
  asm volatile("kD_end_lbl:");
}

extern "C" void kernel_launch(void* const* d_in, const int* in_sizes, int n_in, void* d_out, int out_size, void* d_ws, size_t ws_size,
                              hipStream_t stream) {
  const u16* x = (const u16*)d_in[0];
  const u16* wqkv = (const u16*)d_in[1];
  const u16* wo = (const u16*)d_in[2];
  const u16* wg = (const u16*)d_in[4];
  const u16* wu = (const u16*)d_in[5];
  const u16* wd = (const u16*)d_in[6];
  char* ws = (char*)d_ws;
  (void)hipFuncSetAttribute((const void*)kA, hipFuncAttributeMaxDynamicSharedMemorySize, KA_LDS);
  (void)hipFuncSetAttribute((const void*)kC, hipFuncAttributeMaxDynamicSharedMemorySize, KC_LDS);
  (void)hipFuncSetAttribute((const void*)kD, hipFuncAttributeMaxDynamicSharedMemorySize, KD_LDS);
  k0<<<PFMODE == 3 ? 800 : 544, 512, K0_LDS, stream>>>(x, wqkv, wo, wg, wu, ws);
  kA<<<256, 576, KA_LDS, stream>>>(x, wo, wg, wu, ws);
  kC<<<256, 768, KC_LDS, stream>>>(wg, wu, wd, ws);
  kD<<<256, 512, KD_LDS, stream>>>(wd, ws, (u16*)d_out);
}
```

```cpp
#include <hip/hip_runtime.h>
#include <hip/hip_bf16.h>
#include <stdint.h>

#define NEWLAY 1
#define LAS __attribute__((address_space(3)))
#define GAS __attribute__((address_space(1)))
#define DI __device__ __forceinline__
typedef unsigned short u16;
typedef short s16x4 __attribute__((ext_vector_type(4)));
typedef short s16x8 __attribute__((ext_vector_type(8)));
typedef __bf16 bf16x2 __attribute__((ext_vector_type(2)));
typedef __bf16 bf16x8 __attribute__((ext_vector_type(8)));
typedef float f32x2 __attribute__((ext_vector_type(2)));
typedef float f32x4 __attribute__((ext_vector_type(4)));
typedef float f32x16 __attribute__((ext_vector_type(16)));
typedef unsigned u32x2 __attribute__((ext_vector_type(2)));
typedef unsigned u32x4 __attribute__((ext_vector_type(4)));

#define KC_PF 0
#define PFMODE 0
constexpr int S_ = 2048, D_ = 576, F_ = 4608;

constexpr size_t WS_O    = 0;
constexpr size_t WS_XN   = WS_O + (size_t)S_ * 640 * 2;
constexpr size_t WS_WQ   = WS_XN + (size_t)S_ * D_ * 2;
constexpr size_t WS_HC   = WS_WQ + (size_t)16 * 18 * 7 * 1024;
constexpr size_t WS_H    = WS_HC + (size_t)S_ * D_ * 2;
constexpr size_t WS_ST   = WS_H + (size_t)S_ * D_ * 2;
constexpr size_t WS_XM   = WS_ST + (size_t)S_ * 16 * 4;
constexpr size_t WS_WM   = WS_XM + (size_t)S_ * 4;
constexpr size_t WS_CNT  = WS_WM + 8192;
constexpr size_t WS_ACT  = WS_CNT + 512 * 256;
constexpr size_t WS_PART = WS_ACT + (size_t)S_ * F_ * 2;
constexpr size_t PART_SLAB = 9 * 1024;
static_assert(64 * 4 * 3 * PART_SLAB <= WS_H, "partial slabs must not reach h");

DI float bf_lo(unsigned v) { return __uint_as_float(v << 16); }
DI float bf_hi(unsigned v) { return __uint_as_float(v & 0xffff0000u); }
DI unsigned pk_bf16(float a, float b) { f32x2 v = {a, b}; return __builtin_bit_cast(unsigned, __builtin_convertvector(v, bf16x2)); }
DI float rbf(float a) { return bf_lo(pk_bf16(a, 0.f)); }
DI float dot2bf(unsigned a, unsigned b, float c) { return __builtin_amdgcn_fdot2_f32_bf16(__builtin_bit_cast(bf16x2, a), __builtin_bit_cast(bf16x2, b), c, false); }
DI s16x4 tr_read(LAS char* p) { return __builtin_amdgcn_ds_read_tr16_b64_v4i16((LAS s16x4*)p); }
DI bf16x8 cat8(s16x4 a, s16x4 b) { return __builtin_bit_cast(bf16x8, __builtin_shufflevector(a, b, 0, 1, 2, 3, 4, 5, 6, 7)); }
DI void glds16(const void* g, LAS char* l) {
  unsigned keep; const unsigned la = __builtin_amdgcn_readfirstlane((unsigned)(size_t)l);
  asm volatile("s_mov_b32 %0, m0\n\ts_mov_b32 m0, %2\n\ts_nop 0\n\tglobal_load_lds_dwordx4 %1, off\n\ts_mov_b32 m0, %0" : "=&s"(keep) : "v"(g), "s"(la) : "memory");
}
DI u32x4 tr_frag(LAS char* p, int second) {
  const u32x2 a = __builtin_bit_cast(u32x2, tr_read(p)), b = __builtin_bit_cast(u32x2, tr_read(p + second));
  return (u32x4){a[0], a[1], b[0], b[1]};
}
DI u32x4 tr_frag2(LAS char* p0, LAS char* p1) {
  const u32x2 a = __builtin_bit_cast(u32x2, tr_read(p0)), b = __builtin_bit_cast(u32x2, tr_read(p1));
  return (u32x4){a[0], a[1], b[0], b[1]};
}
#define BF8(v) __builtin_bit_cast(bf16x8, (v))
DI void touch4(const void* g, LAS char* l) {
  unsigned keep; const unsigned la = __builtin_amdgcn_readfirstlane((unsigned)(size_t)l);
  asm volatile("s_mov_b32 %0, m0\n\ts_mov_b32 m0, %2\n\ts_nop 0\n\tglobal_load_lds_dword %1, off\n\ts_mov_b32 m0, %0" : "=&s"(keep) : "v"(g), "s"(la) : "memory");
}
DI float wave_sum(float v) {
#define DPP_ADD(ctrl) v += __builtin_bit_cast(float, __builtin_amdgcn_mov_dpp(__builtin_bit_cast(int, v), ctrl, 0xF, 0xF, true))
  DPP_ADD(0xB1); DPP_ADD(0x4E); DPP_ADD(0x141); DPP_ADD(0x140);
#undef DPP_ADD
  const int iv = __builtin_bit_cast(int, v);
  return (__builtin_bit_cast(float, __builtin_amdgcn_readlane(iv, 0)) + __builtin_bit_cast(float, __builtin_amdgcn_readlane(iv, 16))) +
         (__builtin_bit_cast(float, __builtin_amdgcn_readlane(iv, 32)) + __builtin_bit_cast(float, __builtin_amdgcn_readlane(iv, 48)));
}
#define CODE_ADDR(sym) ({ unsigned long long _pc; int _off; asm volatile("s_getpc_b64 %0\n\ts_mov_b32 %1, " sym "@rel32@lo+4" : "=s"(_pc), "=s"(_off)); (const char*)(_pc + (long long)_off); })
DI void touch_code(const char* lo, const char* hi, int lane, LAS char* scr) {
  const long long n = (hi - lo) >> 7;
  if (n > 0 && n <= 512) {
#pragma unroll 1
    for (int i = 0; i < (int)n; i += 64) { const int l = i + lane < (int)n ? i + lane : (int)n - 1; touch4(lo + (size_t)l * 128, scr); }
  }
}
#define MFMA16(a, b, c) __builtin_amdgcn_mfma_f32_16x16x32_bf16((a), (b), (c), 0, 0, 0)
#define MFMA32(a, b, c) __builtin_amdgcn_mfma_f32_32x32x16_bf16((a), (b), (c), 0, 0, 0)
#define BAR() __builtin_amdgcn_s_barrier()
#define SCHED0() __builtin_amdgcn_sched_barrier(0)
#define WAITV(n) asm volatile("s_waitcnt vmcnt(" #n ")" ::: "memory")
#define WAITVL(n) asm volatile("s_waitcnt vmcnt(" #n ") lgkmcnt(0)" ::: "memory")
#define WAITL0() asm volatile("s_waitcnt lgkmcnt(0)" ::: "memory")

#define STAMP_DECL
#define STAMP(i) do {} while (0)
#define STAMP_OUT(kid) do {} while (0)
DI unsigned fm32_piece(int tok, int c4, int KB16) {
  const int pi = (c4 & 15) >> 2;
  return (unsigned)(((tok >> 5) * KB16 + (c4 >> 4)) * 1024 + (((pi & 1) * 32 + (tok & 31)) * 16) + (pi >> 1) * 8);
}

#define WS_RSRC(ws) __builtin_amdgcn_make_buffer_rsrc((void*)(ws), 0, (int)WS_ACT, 0x00020000)
#define ST16(rs, off, v) __builtin_amdgcn_raw_buffer_store_b128((v), (rs), (int)(off), 0, 16)
#define ST8(rs, off, v) __builtin_amdgcn_raw_buffer_store_b64((v), (rs), (int)(off), 0, 16)
#define ST4(rs, off, v) __builtin_amdgcn_raw_buffer_store_b32((v), (rs), (int)(off), 0, 16)

constexpr int K0_SCR = 9216;
constexpr int K0_LDS = K0_SCR + 256;

extern "C" __global__ __launch_bounds__(512, 2) void k0(const u16* __restrict__ x, const u16* __restrict__ wqkv, const u16* __restrict__ wo,
                                                        const u16* __restrict__ wg, const u16* __restrict__ wu, char* __restrict__ ws) {
  extern __shared__ __attribute__((aligned(16))) char smem[];
  LAS char* lds = (LAS char*)smem;
  const int tid = threadIdx.x, lane = tid & 63, w = __builtin_amdgcn_readfirstlane(tid >> 6);
  const int bx = blockIdx.x;
  const auto rs = WS_RSRC(ws);
  STAMP_DECL STAMP(0);
  if (bx < 256) {
    const int row = bx * 8 + w, l36 = lane < 36 ? lane : 35;
    const char* xp = (const char*)x + (size_t)row * (D_ * 2) + l36 * 32;
    const u32x4 va = *(const u32x4*)xp, vb = *(const u32x4*)(xp + 16);
    f32x2 v[8];
#pragma unroll
    for (int e = 0; e < 4; ++e) { v[e] = (f32x2){bf_lo(va[e]), bf_hi(va[e])}; v[4 + e] = (f32x2){bf_lo(vb[e]), bf_hi(vb[e])}; }
    const f32x2 s2 = ((v[0] + v[1]) + (v[2] + v[3])) + ((v[4] + v[5]) + (v[6] + v[7]));
    const float mean = wave_sum(lane < 36 ? s2[0] + s2[1] : 0.f) * (1.0f / D_), mb = rbf(mean);
    f32x2 q2 = {0.f, 0.f};
    unsigned pk[8];
#pragma unroll
    for (int e = 0; e < 8; ++e) { const f32x2 d = v[e] - mb; q2 = __builtin_elementwise_fma(d, d, q2); pk[e] = pk_bf16(d[0], d[1]); }
    const float dm = mean - mb;
    const float var = rbf(fmaxf(wave_sum(lane < 36 ? q2[0] + q2[1] : 0.f) * (1.0f / D_) - dm * dm, 0.f));
    const float rsd = __builtin_amdgcn_rcpf(rbf(__builtin_amdgcn_sqrtf(rbf(var + 1e-5f))));
#pragma unroll
    for (int e = 0; e < 8; ++e) pk[e] = pk_bf16(bf_lo(pk[e]) * rsd, bf_hi(pk[e]) * rsd);
    if (lane < 36) {
      *(LAS u32x4*)(lds + lane * 256 + w * 16) = (u32x4){pk[0], pk[1], pk[4], pk[5]};
      *(LAS u32x4*)(lds + lane * 256 + 128 + w * 16) = (u32x4){pk[2], pk[3], pk[6], pk[7]};
    }
    if (lane == 0) ST4(rs, WS_XM + row * 4, __float_as_uint(mean));
    WAITL0();
    STAMP(1);
    BAR(); asm volatile("" ::: "memory");
    STAMP(2);
#pragma unroll
    for (int i = 0; i < 2; ++i) {
      const int t = tid + i * 512;
      if (t < 576) {
        const u32x4 c = *(LAS u32x4*)(lds + t * 16);
        ST16(rs, WS_XN + ((bx >> 2) * 36 + (t >> 4)) * 1024 + (((t >> 3) & 1) * 32 + (bx & 3) * 8 + (t & 7)) * 16, c);
      }
    }
    STAMP(3); STAMP_OUT(4);
  } else if (bx < 472) {
    const int u = bx - 256, ks = (u * 5462) >> 16, rem = u - ks * 12, seg = rem >> 2, hq = rem & 3;
#pragma unroll
    for (int i = 0; i < 2; ++i) {
      const int cc = i * 8 + w;
      if (cc < 9) {
        const int q = cc * 64 + lane, rr = (q * 3641) >> 16, ch = q - rr * 18;
        glds16((const char*)wqkv + (size_t)(ks * 32) * 3456 + seg * 1152 + hq * 288 + (unsigned)(rr * 3456 + ch * 16), lds + cc * 1024);
      }
    }
    const int g = lane >> 4, r = lane & 15;
    const unsigned adr = (unsigned)((16 * (g >> 1) + 4 * (g & 1) + (r >> 2)) * 288 + (r & 3) * 8);
    int dsto[2];
#pragma unroll
    for (int i = 0; i < 2; ++i) {
      const int c = (i * 8 + w) * 16 + r, hl = (c * 1821) >> 16, pcol = seg * 36 + c - hl * 36;
      dsto[i] = (int)(WS_WQ + (((hq * 4 + hl) * 18 + ks) * 7 + (pcol >> 4)) * 1024 + (g * 16 + (pcol & 15)) * 16);
    }
    WAITV(0);
    BAR(); asm volatile("" ::: "memory");
    ST16(rs, dsto[0], tr_frag(lds + adr + w * 32, 2304));
    if (w == 0) ST16(rs, dsto[1], tr_frag(lds + adr + 256, 2304));
  } else if (bx >= 544) {
    const int b2 = bx - 544, xcd = b2 & 7, jj = b2 >> 3;
    if (w < 6) { int ln = w * 64 + lane; ln = ln < 324 ? ln : 323; const int k18 = (ln * 3641) >> 16;
      touch4((const char*)((xcd & 1) ? wu : wg) + (size_t)(k18 * 32 + jj) * (F_ * 2) + (xcd >> 1) * 2304 + (ln - k18 * 18) * 128, lds + K0_SCR); }
  } else {
    const int wrow = (bx - 472) * 8 + w, l36 = lane < 36 ? lane : 35;
    const char* rp = (const char*)wo + (size_t)wrow * (D_ * 2) + l36 * 32;
    const u32x4 wa = *(const u32x4*)rp, wb = *(const u32x4*)(rp + 16);
    if (bx == 472 && tid < 272) ST4(rs, WS_CNT + tid * 256, 0u);
    if (bx < 480 + 8 && w == 7) touch_code(CODE_ADDR("k0"), CODE_ADDR("kC"), lane, lds + K0_SCR);
    float t = 0.f;
#pragma unroll
    for (int e = 0; e < 4; ++e) { t = dot2bf(wa[e], 0x3f803f80u, t); t = dot2bf(wb[e], 0x3f803f80u, t); }
    t = wave_sum(lane < 36 ? t : 0.f) * (1.0f / D_);
    const float hi = rbf(t);
    if (lane == 0) ST8(rs, WS_WM + wrow * 8, ((u32x2){pk_bf16(hi, t - hi), 0u}));
  }
}

constexpr int KA_STAGE = 16384;
constexpr int KA_QROW = 304;
constexpr int KA_OOFF = 144 * KA_QROW;
constexpr int KA_WO = 54272;
constexpr int KA_WM = KA_WO + 82944;
constexpr int KA_XR = KA_WM + 5120;
constexpr int KA_SCR = KA_XR + 9216;
constexpr int KA_LDS = KA_SCR + 256;
constexpr int KA_RED = 0;

template <int NB>
DI void kA_loop(LAS char* lds, const char* s0, const char* s1, unsigned st0, unsigned st1, int w, int lane, f32x4 (&acc)[2][4], f32x4& acch) {
  const int g = lane >> 4, r = lane & 15, wm = w & 3;
#define KA_G0(sl) do { glds16(s0, lds + (sl) * KA_STAGE + w * 1024); s0 += st0; } while (0)
#define KA_G1(sl) do { glds16(s1, lds + (sl) * KA_STAGE + 8192 + w * 1024); s1 += st1; } while (0)
  const unsigned xadr = (unsigned)(wm * 2048 + (g >> 1) * 1024 + ((g & 1) * 32 + r) * 16);
  const unsigned hadr = (unsigned)(8192 + lane * 16);
  const unsigned wadr = (unsigned)(9216 + (NB == 4 ? 0 : 4096) + lane * 16);
  const unsigned whadr = wadr + (unsigned)((NB == 3 && wm == 3 ? 2 : wm) * 1024);
  u32x4 xP[3], wP[5], xQ[3], wQ[5];
#define KA_RD(dst, base, off) dst = *(LAS u32x4*)((base) + (off))
#define KA_M(XC, WC, b) do { acc[0][b] = MFMA16(BF8(WC[b]), BF8(XC[0]), acc[0][b]); acc[1][b] = MFMA16(BF8(WC[b]), BF8(XC[1]), acc[1][b]); } while (0)
#define KA_STEP(XC, WC, XN, WN, nb, G0, G1) do { \
    KA_M(XC, WC, 0); SCHED0(); KA_RD(WN[0], nb, wadr); KA_RD(XN[0], nb, xadr); KA_RD(XN[1], nb, xadr + 256); G0; SCHED0(); \
    KA_M(XC, WC, 1); SCHED0(); KA_RD(WN[1], nb, wadr + 1024); KA_RD(WN[2], nb, wadr + 2048); SCHED0(); \
    KA_M(XC, WC, 2); SCHED0(); if (NB == 4) KA_RD(WN[3], nb, wadr + 3072); KA_RD(XN[2], nb, hadr); G1; SCHED0(); \
    if (NB == 4) KA_M(XC, WC, 3); \
    acch = MFMA16(BF8(WC[4]), BF8(XC[2]), acch); SCHED0(); KA_RD(WN[4], nb, whadr); SCHED0(); } while (0)
  KA_RD(wP[0], lds, wadr); KA_RD(xP[0], lds, xadr); KA_RD(xP[1], lds, xadr + 256); KA_RD(wP[1], lds, wadr + 1024); KA_RD(wP[2], lds, wadr + 2048);
  if (NB == 4) KA_RD(wP[3], lds, wadr + 3072);
  KA_RD(xP[2], lds, hadr); KA_RD(wP[4], lds, whadr);
  KA_STEP(xP, wP, xQ, wQ, lds + KA_STAGE, KA_G0(5), KA_G1(5));
  int cur = 2, fre = 0;
#pragma unroll 1
  for (int t = 2; t < 14; t += 2) {
    WAITVL(4);
    BAR(); asm volatile("" ::: "memory");
    LAS char* sb = lds + cur * KA_STAGE;
    KA_STEP(xQ, wQ, xP, wP, sb, KA_G0(fre), KA_G1(fre));
    KA_STEP(xP, wP, xQ, wQ, sb + KA_STAGE, KA_G0(fre + 1), KA_G1(fre + 1));
    fre = cur; cur = cur == 4 ? 0 : cur + 2;
  }
#pragma unroll 1
  for (int t = 14; t < 18; t += 2) {
    if (t == 14) WAITVL(4); else WAITVL(0);
    BAR(); asm volatile("" ::: "memory");
    LAS char* sb = lds + cur * KA_STAGE;
    KA_STEP(xQ, wQ, xP, wP, sb, (void)0, (void)0);
    KA_STEP(xP, wP, xQ, wQ, sb + KA_STAGE, (void)0, (void)0);
    cur = cur == 4 ? 0 : cur + 2;
  }
  KA_M(xQ, wQ, 0); KA_M(xQ, wQ, 1); KA_M(xQ, wQ, 2);
  if (NB == 4) KA_M(xQ, wQ, 3);
  acch = MFMA16(BF8(wQ[4]), BF8(xQ[2]), acch);
}

extern "C" __global__ __launch_bounds__(576, 1) void kA(const u16* __restrict__ x, const u16* __restrict__ wo, const u16* __restrict__ wg,
                                                        const u16* __restrict__ wu, char* __restrict__ ws) {
  extern __shared__ __attribute__((aligned(16))) char smem[];
  LAS char* lds = (LAS char*)smem;
  const int tid = threadIdx.x, lane = tid & 63, w = __builtin_amdgcn_readfirstlane(tid >> 6);
  const int bx = blockIdx.x, xcd = bx & 7, jj = bx >> 3;
  const int mt = (xcd >> 1) * 4 + (jj & 3), hp = (xcd & 1) * 8 + (jj >> 2);
  const int g = lane >> 4, r = lane & 15, wm = w & 3, wn = w >> 2;
  const auto rs = WS_RSRC(ws);
  STAMP_DECL STAMP(0);
  if (w == 8) {
    const int th = hp & 1, n8 = hp >> 1;
    unsigned po[9];
#pragma unroll
    for (int j = 0; j < 9; ++j) {
      const int q = j * 64 + lane, k = (q * 7282) >> 16;
      po[j] = (unsigned)(k * (D_ * 2) + (q - k * 9) * 16 + n8 * 144);
    }
#define KA_WOG(gi) do { _Pragma("unroll") for (int _j = 0; _j < 9; ++_j) glds16((const char*)wo + (size_t)(gi) * (64 * D_ * 2) + po[_j], lds + KA_WO + ((gi) * 9 + _j) * 1024); } while (0)
#pragma unroll 1
    for (int i = 0; i < 9; ++i) {
      BAR();
      if (i >= 1 && i < 5) KA_WOG(i + 4);
      else if (i == 5) {
#pragma unroll
        for (int j = 0; j < 9; ++j) glds16((const char*)x + (size_t)(mt * 128 + th * 64) * (D_ * 2) + po[j], lds + KA_XR + j * 1024);
      } else if (i == 6) {
#pragma unroll
        for (int j = 0; j < 5; ++j) { const int o = j * 1024 + lane * 16; glds16(ws + WS_WM + (o < 4608 ? o : 0), lds + KA_WM + j * 1024); }
      }
    }
    BAR();
    KA_WOG(0); KA_WOG(1);
    BAR();
    KA_WOG(2); KA_WOG(3); KA_WOG(4);
    WAITV(0);
    touch_code(CODE_ADDR("kC"), CODE_ADDR("kD"), lane, lds + KA_SCR);
#define KA_PF(m, i0, i1) do { _Pragma("unroll") for (int _i = i0; _i < i1; ++_i) { int ln = _i * 64 + lane; ln = ln < 324 ? ln : 323; const int k18 = (ln * 3641) >> 16; \
      touch4((const char*)(((xcd ^ (m)) & 1) ? wu : wg) + (size_t)(k18 * 32 + jj) * (F_ * 2) + (xcd >> 1) * 2304 + (ln - k18 * 18) * 128, lds + KA_SCR); } } while (0)
#define PFROWS 1
#pragma unroll
    for (int i = 0; i < 2 * PFROWS; ++i) {
      const int l54 = lane < 54 ? lane : 53, k3 = (l54 * 3641) >> 16;
      touch4((const char*)((i & 1) ? wu : wg) + (size_t)((i >> 1) * 96 + k3 * 32 + jj) * (F_ * 2) + (xcd >> 1) * 2304 + (l54 - k3 * 18) * 128, lds + KA_SCR);
    }
    BAR();
    BAR(); BAR(); BAR();
    STAMP(9); STAMP_OUT(0);
    return;
  }
  const char* s0 = ws + WS_XN + (size_t)((mt * 4 + (w >> 1)) * 36 + (w & 1)) * 1024 + lane * 16;
  const char* s1 = w ? ws + WS_WQ + (size_t)(hp * 126 + (w - 1)) * 1024 + lane * 16
                     : ws + WS_XN + (size_t)((mt ? mt * 4 - 1 : 0) * 36 + (g >> 1)) * 1024 + ((g & 1) * 32 + 16 + r) * 16;
  const unsigned st0 = 2048, st1 = w ? 7168u : 2048u;
#pragma unroll
  for (int st = 0; st < 5; ++st) { KA_G0(st); KA_G1(st); }
  STAMP(1);
  unsigned sto[5][2];
#pragma unroll
  for (int b = 0; b < 5; ++b) {
    const int pd = 4 * (4 * wn + (b < 4 ? b : wm)) + g, sg = (pd * 57) >> 9, c0 = (pd - sg * 9) * 4;
#pragma unroll
    for (int e = 0; e < 2; ++e) {
      const int c = c0 + 2 * e, hd = c >= 18 ? 1 : 0, d = c - 18 * hd;
      sto[b][e] = (unsigned)(r * KA_QROW + (pd < 27 ? (sg * 2 + hd) * 48 + d * 2 : 288 + 4 * e));
    }
  }
  f32x4 acc[2][4], acch = {0.f, 0.f, 0.f, 0.f};
#pragma unroll
  for (int a = 0; a < 2; ++a)
#pragma unroll
    for (int b = 0; b < 4; ++b) acc[a][b] = (f32x4){0.f, 0.f, 0.f, 0.f};
  WAITV(6);
  BAR(); asm volatile("" ::: "memory");
  STAMP(2);
  if (wn == 0) kA_loop<4>(lds, s0, s1, st0, st1, w, lane, acc, acch);
  else kA_loop<3>(lds, s0, s1, st0, st1, w, lane, acc, acch);
  STAMP(3);
  WAITL0();
  BAR(); asm volatile("" ::: "memory");
  const int tok = mt * 128 + (hp & 1) * 64 + wm * 16 + r;
  const float xm = *(const float*)(ws + WS_XM + (size_t)tok * 4);
  asm volatile("" ::: "memory");
  unsigned pa[5], pm4[5];
  const unsigned p4s = (r & 3) < 2 ? 288u * 144u : 288u * 8u, p4t = (r & 3) < 2 ? 576u : 32u;
#pragma unroll
  for (int i = 0; i < 5; ++i) {
    const int c = 8 * i + 4 * wn + g, h5 = (c * 205) >> 10, krow = 36 * h5 + 8 * (c - 5 * h5) + (r >> 2), p = r & 3;
    pa[i] = (unsigned)(KA_WO + krow * 144 + p * 8);
    pm4[i] = p < 2 ? (unsigned)(KA_WO + krow * 144 + 128 + p * 8) : (unsigned)(KA_WM + krow * 8);
  }
  {
    LAS char* row0 = lds + (2 * wm + 1) * 16 * KA_QROW;
#pragma unroll
    for (int b = 0; b < 4; ++b)
#pragma unroll
      for (int a = 0; a < 2; ++a) {
        *(LAS unsigned*)(row0 + a * 16 * KA_QROW + sto[b][0]) = pk_bf16(acc[a][b][0], acc[a][b][1]);
        *(LAS unsigned*)(row0 + a * 16 * KA_QROW + sto[b][1]) = pk_bf16(acc[a][b][2], acc[a][b][3]);
      }
    *(LAS unsigned*)(lds + sto[4][0]) = pk_bf16(acch[0], acch[1]); *(LAS unsigned*)(lds + sto[4][1]) = pk_bf16(acch[2], acch[3]);
  }
  WAITL0();
  BAR(); asm volatile("" ::: "memory");
  STAMP(4);
  {
    const int hf = tid & 1, t = (tid >> 1) & 127, hd = tid >> 8, T = mt * 128 + t;
    LAS char* base = lds + (t + 16) * KA_QROW + hd * 48;
    const u32x4 qa = *(LAS u32x4*)(base), qb = *(LAS u32x4*)(base + 16); const unsigned qc = *(LAS unsigned*)(base + 32);
    float lg[8];
#pragma unroll
    for (int i = 0; i < 4; ++i) {
      const int wv = hf * 4 + i;
      LAS char* kp = base - wv * 2 * KA_QROW + 96;
      const u32x4 a = *(LAS u32x4*)(kp), b = *(LAS u32x4*)(kp + 16); const unsigned c = *(LAS unsigned*)(kp + 32);
      float s = 0.f;
      s = dot2bf(qa[0], a[0], s); s = dot2bf(qa[1], a[1], s); s = dot2bf(qa[2], a[2], s); s = dot2bf(qa[3], a[3], s);
      s = dot2bf(qb[0], b[0], s); s = dot2bf(qb[1], b[1], s); s = dot2bf(qb[2], b[2], s); s = dot2bf(qb[3], b[3], s);
      s = dot2bf(qc, c, s);
      s = rbf(rbf(s) * 0.23570226039551584f);
      s = (T - 2 * wv >= 0) ? s : -1.0e9f;
      const float o = __builtin_bit_cast(float, __builtin_amdgcn_mov_dpp(__builtin_bit_cast(int, s), 0xB1, 0xF, 0xF, true));
      lg[i] = hf ? o : s; lg[4 + i] = hf ? s : o;
    }
    const float mx = fmaxf(fmaxf(fmaxf(lg[0], lg[1]), fmaxf(lg[2], lg[3])), fmaxf(fmaxf(lg[4], lg[5]), fmaxf(lg[6], lg[7])));
    float sm = 0.f;
#pragma unroll
    for (int wv = 0; wv < 8; ++wv) { lg[wv] = __builtin_amdgcn_exp2f((lg[wv] - mx) * 1.4426950408889634f); sm += lg[wv]; }
    const float inv = 1.0f / sm;
    float o[10];
#pragma unroll
    for (int d = 0; d < 10; ++d) o[d] = 0.f;
#pragma unroll
    for (int wp = 0; wp < 4; ++wp) {
      const unsigned pp = pk_bf16(lg[2 * wp] * inv, lg[2 * wp + 1] * inv);
      LAS char* v0 = base - (2 * wp) * 2 * KA_QROW + 192 + hf * 16;
      LAS char* v1 = v0 - 2 * KA_QROW;
      const u32x4 a = *(LAS u32x4*)(v0), b = *(LAS u32x4*)(v1); const unsigned a4 = *(LAS unsigned*)(v0 + 16), b4 = *(LAS unsigned*)(v1 + 16);
      const unsigned av[5] = {a[0], a[1], a[2], a[3], a4}, bv[5] = {b[0], b[1], b[2], b[3], b4};
#pragma unroll
      for (int i = 0; i < 5; ++i) {
        o[2 * i]     = dot2bf(pp, __builtin_amdgcn_perm(bv[i], av[i], 0x05040100u), o[2 * i]);
        o[2 * i + 1] = dot2bf(pp, __builtin_amdgcn_perm(bv[i], av[i], 0x07060302u), o[2 * i + 1]);
      }
    }
    LAS char* op = lds + KA_OOFF + t * 80 + hd * 36 + hf * 16;
#pragma unroll
    for (int i = 0; i < 5; ++i) *(LAS unsigned*)(op + 4 * i) = pk_bf16(o[2 * i], o[2 * i + 1]);
    if (hd == 0) *(LAS unsigned*)(lds + KA_OOFF + t * 80 + 72 + hf * 4) = 0u;
  }
  WAITL0();
  BAR(); asm volatile("" ::: "memory");
  STAMP(5);
#pragma unroll
  for (int i = 0; i < 2; ++i) {
    const int id = tid + i * 512;
    if (id < 640) {
      const int mb = (id * 820) >> 16, rem = id - mb * 80;
      const u32x4 v = *(LAS u32x4*)(lds + KA_OOFF + (mb * 16 + (rem & 15)) * 80 + (rem >> 4) * 16);
      ST16(rs, WS_O + ((mt * 8 + mb) * 80 + hp * 5) * 256 + rem * 16, v);
    }
  }
  WAITV(0);
  BAR(); asm volatile("" ::: "memory");
  STAMP(6);
  GAS unsigned* cnt = (GAS unsigned*)(ws + WS_CNT) + (256 + mt) * 64;
  if (tid == 0) __hip_atomic_fetch_add(cnt, 1u, __ATOMIC_RELAXED, __HIP_MEMORY_SCOPE_AGENT);
  u32x4 wf[4][5];
#define KA_P2R(set, i) do { _Pragma("unroll") for (int _b = 0; _b < 4; ++_b) wf[set][_b] = tr_frag(lds + pa[(i) % 5] + ((i) / 5) * (288 * 144) + _b * 32, 576); \
      wf[set][4] = tr_frag2(lds + pm4[(i) % 5] + ((i) / 5) * p4s, lds + pm4[(i) % 5] + ((i) / 5) * p4s + p4t); } while (0)
  KA_P2R(0, 0); KA_P2R(1, 1); KA_P2R(2, 2); KA_P2R(3, 3);
  SCHED0();
  if (w == 0) {
    unsigned spins = 0;
    while (__builtin_amdgcn_readfirstlane(__hip_atomic_load(cnt, __ATOMIC_RELAXED, __HIP_MEMORY_SCOPE_AGENT)) < 16u) {
      if (++spins > 2000000u) break;
      __builtin_amdgcn_s_sleep(1);
    }
  }
  asm volatile("" ::: "memory");
  BAR(); asm volatile("" ::: "memory");
  STAMP(7);
  {
    const int n8 = hp >> 1;
    const int oo = (int)(WS_O + ((tok >> 4) * 80 + 4 * wn) * 256 + lane * 16);
    u32x4 of[10];
#pragma unroll
    for (int i = 0; i < 10; ++i) of[i] = __builtin_amdgcn_raw_buffer_load_b128(rs, oo + i * 2048, 0, 16);
    f32x4 c[5];
#pragma unroll
    for (int b = 0; b < 5; ++b) c[b] = (f32x4){0.f, 0.f, 0.f, 0.f};
#define KA_P2M(set, i) do { _Pragma("unroll") for (int _b = 0; _b < 5; ++_b) c[_b] = MFMA16(BF8(wf[set][_b]), BF8(of[i]), c[_b]); } while (0)
    SCHED0();
#pragma unroll
    for (int i = 0; i < 10; ++i) {
      KA_P2M(i & 3, i); SCHED0();
      if (i + 4 < 10) KA_P2R(i & 3, i + 4);
      SCHED0();
    }
    STAMP(8);
    if (wn == 1) {
#pragma unroll
      for (int b = 0; b < 5; ++b) *(LAS f32x4*)(lds + KA_RED + (wm * 5 + b) * 1024 + lane * 16) = c[b];
    }
    WAITL0();
    BAR(); asm volatile("" ::: "memory");
    if (wn == 0) {
#pragma unroll
      for (int b = 0; b < 5; ++b) c[b] += *(LAS f32x4*)(lds + KA_RED + (wm * 5 + b) * 1024 + lane * 16);
      const float mean = xm + __shfl(c[4][0] + c[4][1], 32 + r), mb = rbf(mean);
      LAS char* xq = lds + KA_XR + (wm * 16 + r) * 144 + g * 8;
      unsigned hw_[10], cw_[10];
      f32x2 qv = {0.f, 0.f};
#pragma unroll
      for (int b = 0; b < 5; ++b) {
        const u32x2 xv = *(LAS u32x2*)(xq + ((b < 4 || g < 2) ? b * 32 : 0));
#pragma unroll
        for (int e = 0; e < 2; ++e) {
          const unsigned a = pk_bf16(c[b][2 * e], c[b][2 * e + 1]);
          const unsigned hh = pk_bf16(bf_lo(xv[e]) + bf_lo(a), bf_hi(xv[e]) + bf_hi(a));
          const f32x2 hv = {bf_lo(hh), bf_hi(hh)};
          const f32x2 d = hv - mean, dc = hv - mb;
          hw_[2 * b + e] = hh; cw_[2 * b + e] = pk_bf16(dc[0], dc[1]);
          if (b < 4) qv += d * d; else qv += g < 2 ? d * d : (f32x2){0.f, 0.f};
        }
      }
      const int ho = ((tok >> 4) * 72 + n8 * 9 + 2 * (g & 1) + (g >> 1)) * 256 + r * 16;
#pragma unroll
      for (int b = 0; b < 4; b += 2) {
        const auto c0 = __builtin_amdgcn_permlane16_swap(cw_[2 * b], cw_[2 * b + 2], false, false), c1 = __builtin_amdgcn_permlane16_swap(cw_[2 * b + 1], cw_[2 * b + 3], false, false);
        const auto h0 = __builtin_amdgcn_permlane16_swap(hw_[2 * b], hw_[2 * b + 2], false, false), h1 = __builtin_amdgcn_permlane16_swap(hw_[2 * b + 1], hw_[2 * b + 3], false, false);
        ST16(rs, WS_HC + ho + b * 512, ((u32x4){c0[0], c1[0], c0[1], c1[1]}));
        ST16(rs, WS_H + ho + b * 512, ((u32x4){h0[0], h1[0], h0[1], h1[1]}));
      }
      const auto s0 = __builtin_amdgcn_permlane32_swap(cw_[8], hw_[8], false, false), s1 = __builtin_amdgcn_permlane32_swap(cw_[9], hw_[9], false, false);
      ST8(rs, (g < 2 ? WS_HC : WS_H) + ((tok >> 4) * 72 + n8 * 9 + 8) * 256 + r * 16 + (g & 1) * 8, ((u32x2){s0[0], s1[0]}));
      float q = qv[0] + qv[1];
      q += __shfl_xor(q, 16); q += __shfl_xor(q, 32);
      if (g == 0) ST4(rs, WS_ST + (tok * 8 + n8) * 4, __float_as_uint(q));
    }
  }
  STAMP(9); STAMP_OUT(0);
}

constexpr int KC_UPOFF = 16384 + 9216 + 128;
constexpr int KC_STAGE = 35072;
constexpr int KC_STOFF = 4 * KC_STAGE;
constexpr int KC_SCR = KC_STOFF + 8192;
constexpr int KC_LDS = KC_SCR + 256;

DI f32x2 geglu2(f32x2 a, f32x2 b, f32x2 k0, f32x2 k1, f32x2 ik2) {
  const f32x2 tt = a * __builtin_elementwise_fma(a * a, k1, k0);
  const f32x2 e = {__builtin_amdgcn_exp2f(tt[0]), __builtin_amdgcn_exp2f(tt[1])};
  const f32x2 d = __builtin_elementwise_fma(e, ik2, ik2);
  const f32x2 rr = {__builtin_amdgcn_rcpf(d[0]), __builtin_amdgcn_rcpf(d[1])};
  return (a * b) * rr;
}
DI void ln_consts(const LAS char* sp_, f32x2& k0, f32x2& k1, f32x2& ik2) {
  const LAS f32x4* sp = (const LAS f32x4*)sp_;
  const f32x4 s4 = sp[0] + sp[1];
  const float var = rbf(((s4[0] + s4[1]) + (s4[2] + s4[3])) * (1.0f / D_));
  const float sd = rbf(__builtin_amdgcn_sqrtf(rbf(var + 1e-5f)));
  const float rs = __builtin_amdgcn_rcpf(sd);
  const float k0s = -2.3022081981f * rs;
  const float k1s = -0.1029432397f * rs * rs * rs;
  const float ik2s = sd * sd;
  k0 = (f32x2){k0s, k0s}; k1 = (f32x2){k1s, k1s}; ik2 = (f32x2){ik2s, ik2s};
}

extern "C" __global__ __launch_bounds__(768, 1) void kC(const u16* __restrict__ wg, const u16* __restrict__ wu, const u16* __restrict__ wd, char* __restrict__ ws) {
  extern __shared__ __attribute__((aligned(16))) char smem[];
  LAS char* lds = (LAS char*)smem;
  const int tid = threadIdx.x, lane = tid & 63, w = __builtin_amdgcn_readfirstlane(tid >> 6);
  const int bx = blockIdx.x, xcd = bx & 7, jj = bx >> 3;
  const int pm = (xcd & 1) * 4 + (jj & 3), pn = (xcd >> 1) * 8 + (jj >> 2);
  const int wm = w & 3, wn = w >> 2;

  STAMP_DECL STAMP(0);
  const char* src[3]; unsigned dst[3]; unsigned step[3];
  src[0] = ws + WS_HC + (size_t)(pm * 16 + w) * (72 * 256) + lane * 16; dst[0] = w * 1024; step[0] = 1024;
#define KC_G(i, st) do { glds16(src[i], lds + ((st) & 3) * KC_STAGE + dst[i]); src[i] += step[i]; } while (0)
  KC_G(0, 0);
#pragma unroll
  for (int i = 1; i < 3; ++i) {
    int sl = i * 12 + w; sl = sl > 33 ? 33 : sl;
    if (sl < 16) {
      src[i] = ws + WS_HC + (size_t)(pm * 16 + sl) * (72 * 256) + lane * 16;
      dst[i] = sl * 1024; step[i] = 1024;
    } else {
      const int c = sl - 16, m = c >= 9 ? 1 : 0, cc = c - 9 * m, q = cc * 64 + lane, rr = (q * 3641) >> 16, ch = q - rr * 18;
      const int kr = (rr & 19) | ((rr & 4) << 1) | ((rr & 8) >> 1);
      src[i] = (const char*)(m ? wu : wg) + pn * 288 + (unsigned)(kr * (F_ * 2) + ch * 16);
      dst[i] = (m ? KC_UPOFF : 16384) + cc * 1024; step[i] = 32 * F_ * 2;
    }
    KC_G(i, 0);
  }
#define KC_ISSUE(st) do { KC_G(0, st); KC_G(1, st); KC_G(2, st); } while (0)
  KC_ISSUE(1); KC_ISSUE(2);
  glds16(ws + WS_ST + (size_t)pm * 8192 + (w & 7) * 1024 + lane * 16, lds + KC_STOFF + (w & 7) * 1024);
  const auto ars = __builtin_amdgcn_make_buffer_rsrc((void*)(ws + WS_ACT), 0, S_ * F_ * 2, 0x00020000);
#define WDW 6
#define KC_TOUCH_WD() do { if (w < WDW) {     \
    int ln = w * 64 + lane; ln = ln < 324 ? ln : 323; \
    touch4((const char*)wd + (size_t)(xcd >> 1) * (1152 * D_ * 2) + (size_t)(jj * 324 + ln) * 128, lds + KC_SCR); } \
    else if (w == 6) touch_code(CODE_ADDR("kD"), CODE_ADDR("kD_end_lbl"), lane, lds + KC_SCR); } while (0)

  const int g = lane >> 4, r = lane & 15;
  f32x4 acc[6][4];
#pragma unroll
  for (int a = 0; a < 6; ++a)
#pragma unroll
    for (int b = 0; b < 4; ++b) acc[a][b] = (f32x4){0.f, 0.f, 0.f, 0.f};
  const unsigned xadr = (unsigned)(wm * 4096 + lane * 16);
  const unsigned wadr = (unsigned)(16384 + (16 * (g >> 1) + 4 * (g & 1) + (r >> 2)) * 288 + wn * 96 + (r & 3) * 8);
  u32x4 xf[4], wA[3], wB[3];
#define KC_WOFF(j) ((((j) & 1) ? (KC_UPOFF - 16384) : 0) + ((j) >> 1) * 32)
#define KC_RW(WF, i, j, base) WF[i] = tr_frag((base) + wadr + KC_WOFF(j), 2304)
#define KC_RX(c, base) xf[c] = *(LAS u32x4*)((base) + xadr + (c) * 1024)
#define KC_M3(WF, j0, c) do { acc[j0][c] = MFMA16(BF8(WF[0]), BF8(xf[c]), acc[j0][c]); acc[j0 + 1][c] = MFMA16(BF8(WF[1]), BF8(xf[c]), acc[j0 + 1][c]); \
    acc[j0 + 2][c] = MFMA16(BF8(WF[2]), BF8(xf[c]), acc[j0 + 2][c]); } while (0)
#define KC_HALFA(base, G0, G1) do { \
    KC_M3(wA, 0, 0); SCHED0(); KC_RW(wB, 0, 3, base); SCHED0(); \
    KC_M3(wA, 0, 1); SCHED0(); KC_RW(wB, 1, 4, base); G0; SCHED0(); \
    KC_M3(wA, 0, 2); SCHED0(); KC_RW(wB, 2, 5, base); SCHED0(); \
    KC_M3(wA, 0, 3); SCHED0(); G1; SCHED0(); } while (0)
#define KC_HALFB(nb, G0) do { \
    KC_M3(wB, 3, 0); SCHED0(); KC_RX(0, nb); KC_RW(wA, 0, 0, nb); SCHED0(); \
    KC_M3(wB, 3, 1); SCHED0(); KC_RX(1, nb); KC_RW(wA, 1, 1, nb); SCHED0(); \
    KC_M3(wB, 3, 2); SCHED0(); KC_RX(2, nb); KC_RW(wA, 2, 2, nb); G0; SCHED0(); \
    KC_M3(wB, 3, 3); SCHED0(); KC_RX(3, nb); SCHED0(); } while (0)
  STAMP(1);
  WAITV(7); BAR(); asm volatile("" ::: "memory");
  STAMP(2);
  KC_RX(0, lds); KC_RW(wA, 0, 0, lds); KC_RW(wA, 1, 1, lds); KC_RW(wA, 2, 2, lds); KC_RX(1, lds); KC_RX(2, lds); KC_RX(3, lds);
  KC_ISSUE(3);
  KC_HALFA(lds, (void)0, (void)0);
  STAMP(3);
#pragma unroll 1
  for (int t = 1; t < 15; ++t) {
    WAITVL(6);
    BAR(); asm volatile("" ::: "memory");
    LAS char* sb = lds + (t & 3) * KC_STAGE;
    KC_HALFB(sb, KC_G(0, t + 3));
    KC_HALFA(sb, KC_G(1, t + 3), KC_G(2, t + 3));
  }
  WAITVL(0);
  BAR(); asm volatile("" ::: "memory");
  KC_TOUCH_WD();
#define KC_T0 15
#pragma unroll 1
  for (int t = KC_T0; t < 18; ++t) {
    LAS char* sb = lds + (t & 3) * KC_STAGE;
    KC_HALFB(sb, (void)0);
    KC_HALFA(sb, (void)0, (void)0);
  }
  KC_M3(wB, 3, 0); KC_M3(wB, 3, 1); KC_M3(wB, 3, 2); KC_M3(wB, 3, 3);
  STAMP(4);
#pragma unroll
  for (int cp = 0; cp < 2; ++cp) {
    f32x2 k0[2], k1[2], ik2[2];
#pragma unroll
    for (int e = 0; e < 2; ++e) ln_consts(lds + KC_STOFF + (wm * 64 + cp * 32 + e * 16 + r) * 32, k0[e], k1[e], ik2[e]);
#pragma unroll
    for (int f = 0; f < 3; ++f) {
      u32x2 pc[2];
#pragma unroll
      for (int e = 0; e < 2; ++e) {
        const f32x4 a = acc[2 * f][2 * cp + e], b = acc[2 * f + 1][2 * cp + e];
        const f32x2 o0 = geglu2((f32x2){a[0], a[1]}, (f32x2){b[0], b[1]}, k0[e], k1[e], ik2[e]);
        const f32x2 o1 = geglu2((f32x2){a[2], a[3]}, (f32x2){b[2], b[3]}, k0[e], k1[e], ik2[e]);
        pc[e] = (u32x2){pk_bf16(o0[0], o0[1]), pk_bf16(o1[0], o1[1])};
      }
      const auto s0 = __builtin_amdgcn_permlane32_swap(pc[0][0], pc[1][0], false, false);
      const auto s1 = __builtin_amdgcn_permlane32_swap(pc[0][1], pc[1][1], false, false);
      const u32x4 v = {s0[0], s1[0], s0[1], s1[1]};
      __builtin_amdgcn_raw_buffer_store_b128(v, ars, ((pm * 8 + wm * 2 + cp) * 288 + pn * 9 + wn * 3 + f) * 1024 + ((g & 1) * 32 + (g >> 1) * 16 + r) * 16, 0, 16);
    }
  }
  STAMP(5); STAMP_OUT(2);
}

constexpr int KD_STAGE = 16384 + 18432;
constexpr int KD_LDS = 2 * KD_STAGE + 72 * 1024;

template <int NB, int NG>
DI void kD_loop(LAS char* lds, const char* srcA, const char* const (&srcW)[3], int w, int lane, f32x4 (&acc)[4][5]) {
  const int gk = w >> 2, wm = w & 1, g = lane >> 4, r = lane & 15;
#define KD_GA(i, st) glds16(srcA + (size_t)(i) * (2 * 288 * 1024) + (st) * 4096, lds + ((st) & 3) * KD_STAGE + (i) * 8192 + w * 1024)
#define KD_GW(i, st) glds16(srcW[i] + (size_t)(st) * (64 * D_ * 2), lds + ((st) & 3) * KD_STAGE + 16384 + (i) * 8192 + w * 1024)
#define KD_GW2(st) do { if (NG == 5) glds16(srcW[2] + (size_t)(st) * (64 * D_ * 2), lds + ((st) & 3) * KD_STAGE + 32768 + (w & 1) * 1024); } while (0)
#define KD_WAIT2() do { if (NG == 5) WAITVL(10); else WAITVL(8); } while (0)
  const unsigned xadr = (unsigned)(wm * 8192 + (gk * 2 + (g >> 1)) * 1024 + ((g & 1) * 32 + r) * 16);
  const unsigned wadr = (unsigned)(16384 + (gk * 32 + 16 * (g >> 1) + 4 * (g & 1) + (r >> 2)) * 288 + (NB == 5 ? 0 : 160) + (r & 3) * 8);
  u32x4 xP[4], wP[5], xQ[4], wQ[5];
#define KD_RW(WF, b, base) WF[b] = tr_frag((base) + wadr + (b) * 32, 2304)
#define KD_RX(XF, a, base) XF[a] = *(LAS u32x4*)((base) + xadr + ((a) >> 1) * 4096 + ((a) & 1) * 256)
#define KD_M(XC, WC, b) do { _Pragma("unroll") for (int _a = 0; _a < 4; ++_a) acc[_a][b] = MFMA16(BF8(WC[b]), BF8(XC[_a]), acc[_a][b]); } while (0)
#define KD_STEP(XC, WC, XN, WN, nb, G0, G1, G2, G3) do { \
    KD_M(XC, WC, 0); SCHED0(); KD_RW(WN, 0, nb); KD_RX(XN, 0, nb); KD_RX(XN, 1, nb); G0; SCHED0(); \
    KD_M(XC, WC, 1); SCHED0(); KD_RX(XN, 2, nb); KD_RX(XN, 3, nb); KD_RW(WN, 1, nb); G1; SCHED0(); \
    KD_M(XC, WC, 2); SCHED0(); KD_RW(WN, 2, nb); KD_RW(WN, 3, nb); G2; SCHED0(); \
    KD_M(XC, WC, 3); SCHED0(); if (NB == 5) KD_RW(WN, 4, nb); G3; SCHED0(); \
    if (NB == 5) { KD_M(XC, WC, 4); SCHED0(); } } while (0)
  KD_RW(wP, 0, lds); KD_RX(xP, 0, lds); KD_RX(xP, 1, lds); KD_RX(xP, 2, lds); KD_RX(xP, 3, lds); KD_RW(wP, 1, lds); KD_RW(wP, 2, lds); KD_RW(wP, 3, lds);
  if (NB == 5) KD_RW(wP, 4, lds);
  KD_GA(0, 3); KD_GA(1, 3); KD_GW(0, 3); KD_GW(1, 3); KD_GW2(3);
#pragma unroll 1
  for (int t = 1; t < 15; t += 2) {
    KD_WAIT2();
    BAR(); asm volatile("" ::: "memory");
    KD_STEP(xP, wP, xQ, wQ, lds + (t & 3) * KD_STAGE, do { KD_GA(0, t + 3); KD_GW2(t + 3); } while (0), KD_GA(1, t + 3), KD_GW(0, t + 3), KD_GW(1, t + 3));
    KD_WAIT2();
    BAR(); asm volatile("" ::: "memory");
    KD_STEP(xQ, wQ, xP, wP, lds + ((t + 1) & 3) * KD_STAGE, do { KD_GA(0, t + 4); KD_GW2(t + 4); } while (0), KD_GA(1, t + 4), KD_GW(0, t + 4), KD_GW(1, t + 4));
  }
  KD_WAIT2(); BAR(); asm volatile("" ::: "memory");
  KD_STEP(xP, wP, xQ, wQ, lds + 3 * KD_STAGE, (void)0, (void)0, (void)0, (void)0);
  if (NG == 5) WAITVL(5); else WAITVL(4);
  BAR(); asm volatile("" ::: "memory");
  KD_STEP(xQ, wQ, xP, wP, lds, (void)0, (void)0, (void)0, (void)0);
  WAITVL(0); BAR(); asm volatile("" ::: "memory");
  KD_STEP(xP, wP, xQ, wQ, lds + KD_STAGE, (void)0, (void)0, (void)0, (void)0);
  KD_M(xQ, wQ, 0); KD_M(xQ, wQ, 1); KD_M(xQ, wQ, 2); KD_M(xQ, wQ, 3);
  if (NB == 5) KD_M(xQ, wQ, 4);
}

extern "C" __global__ __launch_bounds__(512, 1) void kD(const u16* __restrict__ wd, char* __restrict__ ws, u16* __restrict__ out) {
  extern __shared__ __attribute__((aligned(16))) char smem[];
  LAS char* lds = (LAS char*)smem;
  const int tid = threadIdx.x, lane = tid & 63, w = __builtin_amdgcn_readfirstlane(tid >> 6);
  const int bx = blockIdx.x, xcd = bx & 7, jj = bx >> 3;
  const int ks = xcd >> 1, mt = (xcd & 1) * 8 + (jj & 7), nt = jj >> 3;
  const int gk = w >> 2, wm = w & 1, wn = ((w >> 1) & 1) ^ gk;
  const int g = lane >> 4, r = lane & 15;

  STAMP_DECL STAMP(0);
  const char* srcA = ws + WS_ACT + (size_t)((mt * 4 + (w >> 2)) * 288 + ks * 72 + (w & 3)) * 1024 + lane * 16;
  KD_GA(0, 0); KD_GA(1, 0);
  const char* srcW[3];
#pragma unroll
  for (int i = 0; i < 3; ++i) {
    const int cc = i < 2 ? i * 8 + w : 16 + (w & 1);
    const int q = cc * 64 + lane, rr = (q * 3641) >> 16, ch = q - rr * 18;
    srcW[i] = (const char*)wd + (size_t)(ks * 1152 * D_ + nt * 144) * 2 + (unsigned)(rr * (D_ * 2) + ch * 16);
  }
  const bool extra = (w == 2 || w == 3);
#define KD_GX(st) do { if (extra) glds16(srcW[2] + (size_t)(st) * (64 * D_ * 2), lds + ((st) & 3) * KD_STAGE + 32768 + (w & 1) * 1024); } while (0)
  KD_GW(0, 0); KD_GW(1, 0); KD_GX(0);
  KD_GA(0, 1); KD_GA(1, 1); KD_GW(0, 1); KD_GW(1, 1); KD_GX(1);
  KD_GA(0, 2); KD_GA(1, 2); KD_GW(0, 2); KD_GW(1, 2); KD_GX(2);
  const char* hb = ws + WS_H + (size_t)((mt * 8 + ks * 2 + (g >> 1)) * 72 + nt * 18) * 256 + r * 16 + (g & 1) * 8;
  const u32x2 ha0 = *(const u32x2*)(hb + w * 512), ha1 = *(const u32x2*)(hb + w * 512 + 256), hb0 = *(const u32x2*)(hb + 4096), hb1 = *(const u32x2*)(hb + 4096 + 256);
  const u32x4 hres0 = {ha0[0], ha0[1], ha1[0], ha1[1]}, hres1 = {hb0[0], hb0[1], hb1[0], hb1[1]};
  asm volatile("" ::: "memory");
  f32x4 acc[4][5];
#pragma unroll
  for (int a = 0; a < 4; ++a)
#pragma unroll
    for (int b = 0; b < 5; ++b) acc[a][b] = (f32x4){0.f, 0.f, 0.f, 0.f};
  STAMP(1);
  if (extra) WAITVL(10); else WAITVL(8);
  BAR(); asm volatile("" ::: "memory");
  STAMP(2);
  if (wn == 0) kD_loop<5, 4>(lds, srcA, srcW, w, lane, acc);
  else if (extra) kD_loop<4, 5>(lds, srcA, srcW, w, lane, acc);
  else kD_loop<4, 4>(lds, srcA, srcW, w, lane, acc);
  STAMP(3);
  const int qk = 2 * wm + gk;
  {
    LAS char* xw = lds + 2 * KD_STAGE + ((qk ^ 1) * 18 + wn * 5) * 1024 + lane * 16;
#pragma unroll
    for (int sb = 0; sb < 2; ++sb)
#pragma unroll
      for (int b = 0; b < 5; ++b)
        if (b < 4 || wn == 0) *(LAS f32x4*)(xw + (sb * 9 + b) * 1024) = gk ? acc[sb][b] : acc[2 + sb][b];
  }
  WAITL0();
  BAR(); asm volatile("" ::: "memory");
  STAMP(4);
  const int tile = mt * 4 + nt;
  const auto prs = __builtin_amdgcn_make_buffer_rsrc((void*)(ws + WS_PART), 0, (int)(64 * 4 * 3 * PART_SLAB), 0x00020000);
  {
    LAS char* xr = lds + 2 * KD_STAGE + (qk * 18 + wn * 5) * 1024 + lane * 16;
    const int sidx = ks - (ks > qk ? 1 : 0);
    const int po = (int)(((tile * 4 + qk) * 3 + sidx) * PART_SLAB) + wn * 5120 + lane * 16;
#pragma unroll
    for (int b = 0; b < 5; ++b)
      if (b < 4 || wn == 0) {
        const f32x4 v0 = (gk ? acc[2][b] : acc[0][b]) + *(LAS f32x4*)(xr + b * 1024);
        const f32x4 v1 = (gk ? acc[3][b] : acc[1][b]) + *(LAS f32x4*)(xr + (9 + b) * 1024);
        if (qk != ks) {
          const auto s0 = __builtin_amdgcn_permlane32_swap(pk_bf16(v0[0], v0[1]), pk_bf16(v1[0], v1[1]), false, false);
          const auto s1 = __builtin_amdgcn_permlane32_swap(pk_bf16(v0[2], v0[3]), pk_bf16(v1[2], v1[3]), false, false);
          __builtin_amdgcn_raw_buffer_store_b128((u32x4){s0[0], s1[0], s0[1], s1[1]}, prs, po + b * 1024, 0, 16);
        } else {
          *(LAS f32x4*)(lds + (wn * 5 + b) * 2048 + lane * 16) = v0;
          *(LAS f32x4*)(lds + (wn * 5 + b) * 2048 + 1024 + lane * 16) = v1;
        }
      }
  }
  STAMP(5);
  WAITVL(0);
  BAR(); asm volatile("" ::: "memory");
  STAMP(6);
  GAS unsigned* cnt = (GAS unsigned*)(ws + WS_CNT);
  if (tid < 4 && tid != ks) __hip_atomic_fetch_add(cnt + (tile * 4 + tid) * 64, 1u, __ATOMIC_RELAXED, __HIP_MEMORY_SCOPE_AGENT);
  if (w == 0) {
    unsigned spins = 0;
    while (__builtin_amdgcn_readfirstlane(__hip_atomic_load(cnt + (tile * 4 + ks) * 64, __ATOMIC_RELAXED, __HIP_MEMORY_SCOPE_AGENT)) < 3u) {
      if (++spins > 2000000u) break;
      __builtin_amdgcn_s_sleep(1);
    }
  }
  asm volatile("" ::: "memory");
  BAR(); asm volatile("" ::: "memory");
  STAMP(7);
  u32x4 pp[2][3];
#pragma unroll
  for (int i = 0; i < 2; ++i)
#pragma unroll
    for (int q = 0; q < 3; ++q)
      if (i == 0 || w == 1) pp[i][q] = __builtin_amdgcn_raw_buffer_load_b128(prs, (int)(((tile * 4 + ks) * 3 + q) * PART_SLAB) + (i ? 8 : w) * 1024 + lane * 16, 0, 16);
#pragma unroll
  for (int i = 0; i < 2; ++i) {
    if (i == 1 && w != 1) break;
    const int grp = i ? 8 : w;
    const u32x4 hr = i ? hres1 : hres0;
    u16* op = out + (size_t)(mt * 128 + ks * 32 + (g >> 1) * 16 + r) * D_ + nt * 144 + grp * 16 + 4 * (g & 1);
#pragma unroll
    for (int hf = 0; hf < 2; ++hf) {
      f32x4 v = *(LAS f32x4*)(lds + grp * 2048 + (g >> 1) * 1024 + (((g & 1) + 2 * hf) * 16 + r) * 16);
#pragma unroll
      for (int q = 0; q < 3; ++q) v += (f32x4){bf_lo(pp[i][q][2 * hf]), bf_hi(pp[i][q][2 * hf]), bf_lo(pp[i][q][2 * hf + 1]), bf_hi(pp[i][q][2 * hf + 1])};
      const unsigned a0 = pk_bf16(v[0], v[1]), a1 = pk_bf16(v[2], v[3]);
      const unsigned o0 = pk_bf16(bf_lo(hr[2 * hf]) + bf_lo(a0), bf_hi(hr[2 * hf]) + bf_hi(a0));
      const unsigned o1 = pk_bf16(bf_lo(hr[2 * hf + 1]) + bf_lo(a1), bf_hi(hr[2 * hf + 1]) + bf_hi(a1));
      *(u32x2*)(op + 8 * hf) = (u32x2){o0, o1};
    }
  }
  STAMP(8); STAMP_OUT(3);
  asm volatile("kD_end_lbl:");
}

extern "C" void kernel_launch(void* const* d_in, const int* in_sizes, int n_in, void* d_out, int out_size, void* d_ws, size_t ws_size,
                              hipStream_t stream) {
  const u16* x = (const u16*)d_in[0];
  const u16* wqkv = (const u16*)d_in[1];
  const u16* wo = (const u16*)d_in[2];
  const u16* wg = (const u16*)d_in[4];
  const u16* wu = (const u16*)d_in[5];
  const u16* wd = (const u16*)d_in[6];
  char* ws = (char*)d_ws;
  (void)hipFuncSetAttribute((const void*)kA, hipFuncAttributeMaxDynamicSharedMemorySize, KA_LDS);
  (void)hipFuncSetAttribute((const void*)kC, hipFuncAttributeMaxDynamicSharedMemorySize, KC_LDS);
  (void)hipFuncSetAttribute((const void*)kD, hipFuncAttributeMaxDynamicSharedMemorySize, KD_LDS);
  k0<<<PFMODE == 3 ? 800 : 544, 512, K0_LDS, stream>>>(x, wqkv, wo, wg, wu, ws);
  kA<<<256, 576, KA_LDS, stream>>>(x, wo, wg, wu, ws);
  kC<<<256, 768, KC_LDS, stream>>>(wg, wu, wd, ws);
  kD<<<256, 512, KD_LDS, stream>>>(wd, ws, (u16*)d_out);
}
```

```cpp
#include <hip/hip_runtime.h>
#include <hip/hip_bf16.h>
#include <stdint.h>

#define NEWLAY 1
#define LAS __attribute__((address_space(3)))
#define GAS __attribute__((address_space(1)))
#define DI __device__ __forceinline__
typedef unsigned short u16;
typedef short s16x4 __attribute__((ext_vector_type(4)));
typedef short s16x8 __attribute__((ext_vector_type(8)));
typedef __bf16 bf16x2 __attribute__((ext_vector_type(2)));
typedef __bf16 bf16x8 __attribute__((ext_vector_type(8)));
typedef float f32x2 __attribute__((ext_vector_type(2)));
typedef float f32x4 __attribute__((ext_vector_type(4)));
typedef float f32x16 __attribute__((ext_vector_type(16)));
typedef unsigned u32x2 __attribute__((ext_vector_type(2)));
typedef unsigned u32x4 __attribute__((ext_vector_type(4)));

#define KC_PF 0
#define PFMODE 0
constexpr int S_ = 2048, D_ = 576, F_ = 4608;

constexpr size_t WS_O    = 0;
constexpr size_t WS_XN   = WS_O + (size_t)S_ * 640 * 2;
constexpr size_t WS_WQ   = WS_XN + (size_t)S_ * D_ * 2;
constexpr size_t WS_HC   = WS_WQ + (size_t)16 * 18 * 7 * 1024;
constexpr size_t WS_H    = WS_HC + (size_t)S_ * D_ * 2;
constexpr size_t WS_ST   = WS_H + (size_t)S_ * D_ * 2;
constexpr size_t WS_XM   = WS_ST + (size_t)S_ * 16 * 4;
constexpr size_t WS_WM   = WS_XM + (size_t)S_ * 4;
constexpr size_t WS_CNT  = WS_WM + 8192;
constexpr size_t WS_ACT  = WS_CNT + 512 * 256;
constexpr size_t WS_PART = WS_ACT + (size_t)S_ * F_ * 2;
constexpr size_t PART_SLAB = 9 * 1024;
static_assert(64 * 4 * 3 * PART_SLAB <= WS_H, "partial slabs must not reach h");

DI float bf_lo(unsigned v) { return __uint_as_float(v << 16); }
DI float bf_hi(unsigned v) { return __uint_as_float(v & 0xffff0000u); }
DI unsigned pk_bf16(float a, float b) { f32x2 v = {a, b}; return __builtin_bit_cast(unsigned, __builtin_convertvector(v, bf16x2)); }
DI float rbf(float a) { return bf_lo(pk_bf16(a, 0.f)); }
DI float dot2bf(unsigned a, unsigned b, float c) { return __builtin_amdgcn_fdot2_f32_bf16(__builtin_bit_cast(bf16x2, a), __builtin_bit_cast(bf16x2, b), c, false); }
DI s16x4 tr_read(LAS char* p) { return __builtin_amdgcn_ds_read_tr16_b64_v4i16((LAS s16x4*)p); }
DI bf16x8 cat8(s16x4 a, s16x4 b) { return __builtin_bit_cast(bf16x8, __builtin_shufflevector(a, b, 0, 1, 2, 3, 4, 5, 6, 7)); }
DI void glds16(const void* g, LAS char* l) {
  unsigned keep; const unsigned la = __builtin_amdgcn_readfirstlane((unsigned)(size_t)l);
  asm volatile("s_mov_b32 %0, m0\n\ts_mov_b32 m0, %2\n\ts_nop 0\n\tglobal_load_lds_dwordx4 %1, off\n\ts_mov_b32 m0, %0" : "=&s"(keep) : "v"(g), "s"(la) : "memory");
}
DI u32x4 tr_frag(LAS char* p, int second) {
  const u32x2 a = __builtin_bit_cast(u32x2, tr_read(p)), b = __builtin_bit_cast(u32x2, tr_read(p + second));
  return (u32x4){a[0], a[1], b[0], b[1]};
}
DI u32x4 tr_frag2(LAS char* p0, LAS char* p1) {
  const u32x2 a = __builtin_bit_cast(u32x2, tr_read(p0)), b = __builtin_bit_cast(u32x2, tr_read(p1));
  return (u32x4){a[0], a[1], b[0], b[1]};
}
#define BF8(v) __builtin_bit_cast(bf16x8, (v))
DI void touch4(const void* g, LAS char* l) {
  unsigned keep; const unsigned la = __builtin_amdgcn_readfirstlane((unsigned)(size_t)l);
  asm volatile("s_mov_b32 %0, m0\n\ts_mov_b32 m0, %2\n\ts_nop 0\n\tglobal_load_lds_dword %1, off\n\ts_mov_b32 m0, %0" : "=&s"(keep) : "v"(g), "s"(la) : "memory");
}
DI float wave_sum(float v) {
#define DPP_ADD(ctrl) v += __builtin_bit_cast(float, __builtin_amdgcn_mov_dpp(__builtin_bit_cast(int, v), ctrl, 0xF, 0xF, true))
  DPP_ADD(0xB1); DPP_ADD(0x4E); DPP_ADD(0x141); DPP_ADD(0x140);
#undef DPP_ADD
  const int iv = __builtin_bit_cast(int, v);
  return (__builtin_bit_cast(float, __builtin_amdgcn_readlane(iv, 0)) + __builtin_bit_cast(float, __builtin_amdgcn_readlane(iv, 16))) +
         (__builtin_bit_cast(float, __builtin_amdgcn_readlane(iv, 32)) + __builtin_bit_cast(float, __builtin_amdgcn_readlane(iv, 48)));
}
#define CODE_ADDR(sym) ({ unsigned long long _pc; int _off; asm volatile("s_getpc_b64 %0\n\ts_mov_b32 %1, " sym "@rel32@lo+4" : "=s"(_pc), "=s"(_off)); (const char*)(_pc + (long long)_off); })
DI void touch_code(const char* lo, const char* hi, int lane, LAS char* scr) {
  const long long n = (hi - lo) >> 7;
  if (n > 0 && n <= 512) {
#pragma unroll 1
    for (int i = 0; i < (int)n; i += 64) { const int l = i + lane < (int)n ? i + lane : (int)n - 1; touch4(lo + (size_t)l * 128, scr); }
  }
}
#define MFMA16(a, b, c) __builtin_amdgcn_mfma_f32_16x16x32_bf16((a), (b), (c), 0, 0, 0)
#define MFMA32(a, b, c) __builtin_amdgcn_mfma_f32_32x32x16_bf16((a), (b), (c), 0, 0, 0)
#define BAR() __builtin_amdgcn_s_barrier()
#define SCHED0() __builtin_amdgcn_sched_barrier(0)
#define WAITV(n) asm volatile("s_waitcnt vmcnt(" #n ")" ::: "memory")
#define WAITVL(n) asm volatile("s_waitcnt vmcnt(" #n ") lgkmcnt(0)" ::: "memory")
#define WAITL0() asm volatile("s_waitcnt lgkmcnt(0)" ::: "memory")

#define STAMP_DECL
#define STAMP(i) do {} while (0)
#define STAMP_OUT(kid) do {} while (0)
DI unsigned fm32_piece(int tok, int c4, int KB16) {
  const int pi = (c4 & 15) >> 2;
  return (unsigned)(((tok >> 5) * KB16 + (c4 >> 4)) * 1024 + (((pi & 1) * 32 + (tok & 31)) * 16) + (pi >> 1) * 8);
}

#define WS_RSRC(ws) __builtin_amdgcn_make_buffer_rsrc((void*)(ws), 0, (int)WS_ACT, 0x00020000)
#define ST16(rs, off, v) __builtin_amdgcn_raw_buffer_store_b128((v), (rs), (int)(off), 0, 16)
#define ST8(rs, off, v) __builtin_amdgcn_raw_buffer_store_b64((v), (rs), (int)(off), 0, 16)
#define ST4(rs, off, v) __builtin_amdgcn_raw_buffer_store_b32((v), (rs), (int)(off), 0, 16)

constexpr int K0_SCR = 9216;
constexpr int K0_LDS = K0_SCR + 256;

extern "C" __global__ __launch_bounds__(512, 2) void k0(const u16* __restrict__ x, const u16* __restrict__ wqkv, const u16* __restrict__ wo,
                                                        const u16* __restrict__ wg, const u16* __restrict__ wu, char* __restrict__ ws) {
  extern __shared__ __attribute__((aligned(16))) char smem[];
  LAS char* lds = (LAS char*)smem;
  const int tid = threadIdx.x, lane = tid & 63, w = __builtin_amdgcn_readfirstlane(tid >> 6);
  const int bx = blockIdx.x;
  const auto rs = WS_RSRC(ws);
  STAMP_DECL STAMP(0);
  if (bx < 256) {
    const int row = bx * 8 + w, l36 = lane < 36 ? lane : 35;
    const char* xp = (const char*)x + (size_t)row * (D_ * 2) + l36 * 32;
    const u32x4 va = *(const u32x4*)xp, vb = *(const u32x4*)(xp + 16);
    f32x2 v[8];
#pragma unroll
    for (int e = 0; e < 4; ++e) { v[e] = (f32x2){bf_lo(va[e]), bf_hi(va[e])}; v[4 + e] = (f32x2){bf_lo(vb[e]), bf_hi(vb[e])}; }
    const f32x2 s2 = ((v[0] + v[1]) + (v[2] + v[3])) + ((v[4] + v[5]) + (v[6] + v[7]));
    const float mean = wave_sum(lane < 36 ? s2[0] + s2[1] : 0.f) * (1.0f / D_), mb = rbf(mean);
    f32x2 q2 = {0.f, 0.f};
    unsigned pk[8];
#pragma unroll
    for (int e = 0; e < 8; ++e) { const f32x2 d = v[e] - mb; q2 = __builtin_elementwise_fma(d, d, q2); pk[e] = pk_bf16(d[0], d[1]); }
    const float dm = mean - mb;
    const float var = rbf(fmaxf(wave_sum(lane < 36 ? q2[0] + q2[1] : 0.f) * (1.0f / D_) - dm * dm, 0.f));
    const float rsd = __builtin_amdgcn_rcpf(rbf(__builtin_amdgcn_sqrtf(rbf(var + 1e-5f))));
#pragma unroll
    for (int e = 0; e < 8; ++e) pk[e] = pk_bf16(bf_lo(pk[e]) * rsd, bf_hi(pk[e]) * rsd);
    if (lane < 36) {
      *(LAS u32x4*)(lds + lane * 256 + w * 16) = (u32x4){pk[0], pk[1], pk[4], pk[5]};
      *(LAS u32x4*)(lds + lane * 256 + 128 + w * 16) = (u32x4){pk[2], pk[3], pk[6], pk[7]};
    }
    if (lane == 0) ST4(rs, WS_XM + row * 4, __float_as_uint(mean));
    WAITL0();
    STAMP(1);
    BAR(); asm volatile("" ::: "memory");
    STAMP(2);
#pragma unroll
    for (int i = 0; i < 2; ++i) {
      const int t = tid + i * 512;
      if (t < 576) {
        const u32x4 c = *(LAS u32x4*)(lds + t * 16);
        ST16(rs, WS_XN + ((bx >> 2) * 36 + (t >> 4)) * 1024 + (((t >> 3) & 1) * 32 + (bx & 3) * 8 + (t & 7)) * 16, c);
      }
    }
    STAMP(3); STAMP_OUT(4);
  } else if (bx < 472) {
    const int u = bx - 256, ks = (u * 5462) >> 16, rem = u - ks * 12, seg = rem >> 2, hq = rem & 3;
#pragma unroll
    for (int i = 0; i < 2; ++i) {
      const int cc = i * 8 + w;
      if (cc < 9) {
        const int q = cc * 64 + lane, rr = (q * 3641) >> 16, ch = q - rr * 18;
        glds16((const char*)wqkv + (size_t)(ks * 32) * 3456 + seg * 1152 + hq * 288 + (unsigned)(rr * 3456 + ch * 16), lds + cc * 1024);
      }
    }
    const int g = lane >> 4, r = lane & 15;
    const unsigned adr = (unsigned)((16 * (g >> 1) + 4 * (g & 1) + (r >> 2)) * 288 + (r & 3) * 8);
    int dsto[2];
#pragma unroll
    for (int i = 0; i < 2; ++i) {
      const int c = (i * 8 + w) * 16 + r, hl = (c * 1821) >> 16, pcol = seg * 36 + c - hl * 36;
      dsto[i] = (int)(WS_WQ + (((hq * 4 + hl) * 18 + ks) * 7 + (pcol >> 4)) * 1024 + (g * 16 + (pcol & 15)) * 16);
    }
    WAITV(0);
    BAR(); asm volatile("" ::: "memory");
    ST16(rs, dsto[0], tr_frag(lds + adr + w * 32, 2304));
    if (w == 0) ST16(rs, dsto[1], tr_frag(lds + adr + 256, 2304));
  } else if (bx >= 544) {
    const int b2 = bx - 544, xcd = b2 & 7, jj = b2 >> 3;
    if (w < 6) { int ln = w * 64 + lane; ln = ln < 324 ? ln : 323; const int k18 = (ln * 3641) >> 16;
      touch4((const char*)((xcd & 1) ? wu : wg) + (size_t)(k18 * 32 + jj) * (F_ * 2) + (xcd >> 1) * 2304 + (ln - k18 * 18) * 128, lds + K0_SCR); }
  } else {
    const int wrow = (bx - 472) * 8 + w, l36 = lane < 36 ? lane : 35;
    const char* rp = (const char*)wo + (size_t)wrow * (D_ * 2) + l36 * 32;
    const u32x4 wa = *(const u32x4*)rp, wb = *(const u32x4*)(rp + 16);
    if (bx == 472 && tid < 272) ST4(rs, WS_CNT + tid * 256, 0u);
    if (bx < 480 + 8 && w == 7) touch_code(CODE_ADDR("k0"), CODE_ADDR("kC"), lane, lds + K0_SCR);
    float t = 0.f;
#pragma unroll
    for (int e = 0; e < 4; ++e) { t = dot2bf(wa[e], 0x3f803f80u, t); t = dot2bf(wb[e], 0x3f803f80u, t); }
    t = wave_sum(lane < 36 ? t : 0.f) * (1.0f / D_);
    const float hi = rbf(t);
    if (lane == 0) ST8(rs, WS_WM + wrow * 8, ((u32x2){pk_bf16(hi, t - hi), 0u}));
  }
}

constexpr int KA_STAGE = 16384;
constexpr int KA_QROW = 304;
constexpr int KA_OOFF = 144 * KA_QROW;
constexpr int KA_WO = 54272;
constexpr int KA_WM = KA_WO + 82944;
constexpr int KA_XR = KA_WM + 5120;
constexpr int KA_SCR = KA_XR + 9216;
constexpr int KA_LDS = KA_SCR + 256;
constexpr int KA_RED = 0;

template <int NB>
DI void kA_loop(LAS char* lds, const char* s0, const char* s1, unsigned st0, unsigned st1, int w, int lane, f32x4 (&acc)[2][4], f32x4& acch) {
  const int g = lane >> 4, r = lane & 15, wm = w & 3;
#define KA_G0(sl) do { glds16(s0, lds + (sl) * KA_STAGE + w * 1024); s0 += st0; } while (0)
#define KA_G1(sl) do { glds16(s1, lds + (sl) * KA_STAGE + 8192 + w * 1024); s1 += st1; } while (0)
  const unsigned xadr = (unsigned)(wm * 2048 + (g >> 1) * 1024 + ((g & 1) * 32 + r) * 16);
  const unsigned hadr = (unsigned)(8192 + lane * 16);
  const unsigned wadr = (unsigned)(9216 + (NB == 4 ? 0 : 4096) + lane * 16);
  const unsigned whadr = wadr + (unsigned)((NB == 3 && wm == 3 ? 2 : wm) * 1024);
  u32x4 xP[3], wP[5], xQ[3], wQ[5];
#define KA_RD(dst, base, off) dst = *(LAS u32x4*)((base) + (off))
#define KA_M(XC, WC, b) do { acc[0][b] = MFMA16(BF8(WC[b]), BF8(XC[0]), acc[0][b]); acc[1][b] = MFMA16(BF8(WC[b]), BF8(XC[1]), acc[1][b]); } while (0)
#define KA_STEP(XC, WC, XN, WN, nb, G0, G1) do { \
    KA_M(XC, WC, 0); SCHED0(); KA_RD(WN[0], nb, wadr); KA_RD(XN[0], nb, xadr); KA_RD(XN[1], nb, xadr + 256); G0; SCHED0(); \
    KA_M(XC, WC, 1); SCHED0(); KA_RD(WN[1], nb, wadr + 1024); KA_RD(WN[2], nb, wadr + 2048); SCHED0(); \
    KA_M(XC, WC, 2); SCHED0(); if (NB == 4) KA_RD(WN[3], nb, wadr + 3072); KA_RD(XN[2], nb, hadr); G1; SCHED0(); \
    if (NB == 4) KA_M(XC, WC, 3); \
    acch = MFMA16(BF8(WC[4]), BF8(XC[2]), acch); SCHED0(); KA_RD(WN[4], nb, whadr); SCHED0(); } while (0)
  KA_RD(wP[0], lds, wadr); KA_RD(xP[0], lds, xadr); KA_RD(xP[1], lds, xadr + 256); KA_RD(wP[1], lds, wadr + 1024); KA_RD(wP[2], lds, wadr + 2048);
  if (NB == 4) KA_RD(wP[3], lds, wadr + 3072);
  KA_RD(xP[2], lds, hadr); KA_RD(wP[4], lds, whadr);
  KA_STEP(xP, wP, xQ, wQ, lds + KA_STAGE, KA_G0(5), KA_G1(5));
  int cur = 2, fre = 0;
#pragma unroll 1
  for (int t = 2; t < 14; t += 2) {
    WAITVL(4);
    BAR(); asm volatile("" ::: "memory");
    LAS char* sb = lds + cur * KA_STAGE;
    KA_STEP(xQ, wQ, xP, wP, sb, KA_G0(fre), KA_G1(fre));
    KA_STEP(xP, wP, xQ, wQ, sb + KA_STAGE, KA_G0(fre + 1), KA_G1(fre + 1));
    fre = cur; cur = cur == 4 ? 0 : cur + 2;
  }
#pragma unroll 1
  for (int t = 14; t < 18; t += 2) {
    if (t == 14) WAITVL(4); else WAITVL(0);
    BAR(); asm volatile("" ::: "memory");
    LAS char* sb = lds + cur * KA_STAGE;
    KA_STEP(xQ, wQ, xP, wP, sb, (void)0, (void)0);
    KA_STEP(xP, wP, xQ, wQ, sb + KA_STAGE, (void)0, (void)0);
    cur = cur == 4 ? 0 : cur + 2;
  }
  KA_M(xQ, wQ, 0); KA_M(xQ, wQ, 1); KA_M(xQ, wQ, 2);
  if (NB == 4) KA_M(xQ, wQ, 3);
  acch = MFMA16(BF8(wQ[4]), BF8(xQ[2]), acch);
}

extern "C" __global__ __launch_bounds__(640, 1) void kA(const u16* __restrict__ x, const u16* __restrict__ wo, const u16* __restrict__ wg,
                                                        const u16* __restrict__ wu, const u16* __restrict__ wd, char* __restrict__ ws) {
  extern __shared__ __attribute__((aligned(16))) char smem[];
  LAS char* lds = (LAS char*)smem;
  const int tid = threadIdx.x, lane = tid & 63, w = __builtin_amdgcn_readfirstlane(tid >> 6);
  const int bx = blockIdx.x, xcd = bx & 7, jj = bx >> 3;
  const int mt = (xcd >> 1) * 4 + (jj & 3), hp = (xcd & 1) * 8 + (jj >> 2);
  const int g = lane >> 4, r = lane & 15, wm = w & 3, wn = w >> 2;
  const auto rs = WS_RSRC(ws);
  STAMP_DECL STAMP(0);
#define PFB 7
#define PFD 99
  if (w == 9) {
#pragma unroll 1
    for (int i = 0; i < 15; ++i) {
      BAR();
      if (i == PFB) {
#pragma unroll
        for (int j = 0; j < 6; ++j) {
          int ln = j * 64 + lane; ln = ln < 324 ? ln : 323;
          const int k18 = (ln * 3641) >> 16;
          touch4((const char*)((xcd & 1) ? wu : wg) + (size_t)(k18 * 32 + jj) * (F_ * 2) + (xcd >> 1) * 2304 + (ln - k18 * 18) * 128, lds + KA_SCR);
        }
      }
      if (i == PFD) {
#pragma unroll
        for (int j = 0; j < 3; ++j) { int ln = j * 64 + lane; ln = ln < 162 ? ln : 161; touch4((const char*)wd + (size_t)(bx * 162 + ln) * 128, lds + KA_SCR); }
      }
    }
    return;
  }
  if (w == 8) {
#define KA_PF(m, i0, i1) do { _Pragma("unroll") for (int _i = i0; _i < i1; ++_i) { int ln = _i * 64 + lane; ln = ln < 324 ? ln : 323; const int k18 = (ln * 3641) >> 16; \
      touch4((const char*)(((xcd ^ (m)) & 1) ? wu : wg) + (size_t)(k18 * 32 + jj) * (F_ * 2) + (xcd >> 1) * 2304 + (ln - k18 * 18) * 128, lds + KA_SCR); } } while (0)
    const int th = hp & 1, n8 = hp >> 1;
    unsigned po[9];
#pragma unroll
    for (int j = 0; j < 9; ++j) {
      const int q = j * 64 + lane, k = (q * 7282) >> 16;
      po[j] = (unsigned)(k * (D_ * 2) + (q - k * 9) * 16 + n8 * 144);
    }
#define KA_WOG(gi) do { _Pragma("unroll") for (int _j = 0; _j < 9; ++_j) glds16((const char*)wo + (size_t)(gi) * (64 * D_ * 2) + po[_j], lds + KA_WO + ((gi) * 9 + _j) * 1024); } while (0)
#pragma unroll 1
    for (int i = 0; i < 9; ++i) {
      BAR();
      if (i >= 1 && i < 5) KA_WOG(i + 4);
      else if (i == 5) {
#pragma unroll
        for (int j = 0; j < 9; ++j) glds16((const char*)x + (size_t)(mt * 128 + th * 64) * (D_ * 2) + po[j], lds + KA_XR + j * 1024);
      } else if (i == 6) {
#pragma unroll
        for (int j = 0; j < 5; ++j) { const int o = j * 1024 + lane * 16; glds16(ws + WS_WM + (o < 4608 ? o : 0), lds + KA_WM + j * 1024); }
      }
    }
    BAR();
    KA_WOG(0); KA_WOG(1);
    BAR();
    KA_WOG(2); KA_WOG(3); KA_WOG(4);
    WAITV(0);
    touch_code(CODE_ADDR("kC"), CODE_ADDR("kD"), lane, lds + KA_SCR);
#define PFROWS 1
#pragma unroll
    for (int i = 0; i < 2 * PFROWS; ++i) {
      const int l54 = lane < 54 ? lane : 53, k3 = (l54 * 3641) >> 16;
      touch4((const char*)((i & 1) ? wu : wg) + (size_t)((i >> 1) * 96 + k3 * 32 + jj) * (F_ * 2) + (xcd >> 1) * 2304 + (l54 - k3 * 18) * 128, lds + KA_SCR);
    }
    BAR();
    BAR(); BAR(); BAR();
    STAMP(9); STAMP_OUT(0);
    return;
  }
  const char* s0 = ws + WS_XN + (size_t)((mt * 4 + (w >> 1)) * 36 + (w & 1)) * 1024 + lane * 16;
  const char* s1 = w ? ws + WS_WQ + (size_t)(hp * 126 + (w - 1)) * 1024 + lane * 16
                     : ws + WS_XN + (size_t)((mt ? mt * 4 - 1 : 0) * 36 + (g >> 1)) * 1024 + ((g & 1) * 32 + 16 + r) * 16;
  const unsigned st0 = 2048, st1 = w ? 7168u : 2048u;
#pragma unroll
  for (int st = 0; st < 5; ++st) { KA_G0(st); KA_G1(st); }
  STAMP(1);
  unsigned sto[5][2];
#pragma unroll
  for (int b = 0; b < 5; ++b) {
    const int pd = 4 * (4 * wn + (b < 4 ? b : wm)) + g, sg = (pd * 57) >> 9, c0 = (pd - sg * 9) * 4;
#pragma unroll
    for (int e = 0; e < 2; ++e) {
      const int c = c0 + 2 * e, hd = c >= 18 ? 1 : 0, d = c - 18 * hd;
      sto[b][e] = (unsigned)(r * KA_QROW + (pd < 27 ? (sg * 2 + hd) * 48 + d * 2 : 288 + 4 * e));
    }
  }
  f32x4 acc[2][4], acch = {0.f, 0.f, 0.f, 0.f};
#pragma unroll
  for (int a = 0; a < 2; ++a)
#pragma unroll
    for (int b = 0; b < 4; ++b) acc[a][b] = (f32x4){0.f, 0.f, 0.f, 0.f};
  WAITV(6);
  BAR(); asm volatile("" ::: "memory");
  STAMP(2);
  if (wn == 0) kA_loop<4>(lds, s0, s1, st0, st1, w, lane, acc, acch);
  else kA_loop<3>(lds, s0, s1, st0, st1, w, lane, acc, acch);
  STAMP(3);
  WAITL0();
  BAR(); asm volatile("" ::: "memory");
  const int tok = mt * 128 + (hp & 1) * 64 + wm * 16 + r;
  const float xm = *(const float*)(ws + WS_XM + (size_t)tok * 4);
  asm volatile("" ::: "memory");
  unsigned pa[5], pm4[5];
  const unsigned p4s = (r & 3) < 2 ? 288u * 144u : 288u * 8u, p4t = (r & 3) < 2 ? 576u : 32u;
#pragma unroll
  for (int i = 0; i < 5; ++i) {
    const int c = 8 * i + 4 * wn + g, h5 = (c * 205) >> 10, krow = 36 * h5 + 8 * (c - 5 * h5) + (r >> 2), p = r & 3;
    pa[i] = (unsigned)(KA_WO + krow * 144 + p * 8);
    pm4[i] = p < 2 ? (unsigned)(KA_WO + krow * 144 + 128 + p * 8) : (unsigned)(KA_WM + krow * 8);
  }
  {
    LAS char* row0 = lds + (2 * wm + 1) * 16 * KA_QROW;
#pragma unroll
    for (int b = 0; b < 4; ++b)
#pragma unroll
      for (int a = 0; a < 2; ++a) {
        *(LAS unsigned*)(row0 + a * 16 * KA_QROW + sto[b][0]) = pk_bf16(acc[a][b][0], acc[a][b][1]);
        *(LAS unsigned*)(row0 + a * 16 * KA_QROW + sto[b][1]) = pk_bf16(acc[a][b][2], acc[a][b][3]);
      }
    *(LAS unsigned*)(lds + sto[4][0]) = pk_bf16(acch[0], acch[1]); *(LAS unsigned*)(lds + sto[4][1]) = pk_bf16(acch[2], acch[3]);
  }
  WAITL0();
  BAR(); asm volatile("" ::: "memory");
  STAMP(4);
  {
    const int hf = tid & 1, t = (tid >> 1) & 127, hd = tid >> 8, T = mt * 128 + t;
    LAS char* base = lds + (t + 16) * KA_QROW + hd * 48;
    const u32x4 qa = *(LAS u32x4*)(base), qb = *(LAS u32x4*)(base + 16); const unsigned qc = *(LAS unsigned*)(base + 32);
    float lg[8];
#pragma unroll
    for (int i = 0; i < 4; ++i) {
      const int wv = hf * 4 + i;
      LAS char* kp = base - wv * 2 * KA_QROW + 96;
      const u32x4 a = *(LAS u32x4*)(kp), b = *(LAS u32x4*)(kp + 16); const unsigned c = *(LAS unsigned*)(kp + 32);
      float s = 0.f;
      s = dot2bf(qa[0], a[0], s); s = dot2bf(qa[1], a[1], s); s = dot2bf(qa[2], a[2], s); s = dot2bf(qa[3], a[3], s);
      s = dot2bf(qb[0], b[0], s); s = dot2bf(qb[1], b[1], s); s = dot2bf(qb[2], b[2], s); s = dot2bf(qb[3], b[3], s);
      s = dot2bf(qc, c, s);
      s = rbf(rbf(s) * 0.23570226039551584f);
      s = (T - 2 * wv >= 0) ? s : -1.0e9f;
      const float o = __builtin_bit_cast(float, __builtin_amdgcn_mov_dpp(__builtin_bit_cast(int, s), 0xB1, 0xF, 0xF, true));
      lg[i] = hf ? o : s; lg[4 + i] = hf ? s : o;
    }
    const float mx = fmaxf(fmaxf(fmaxf(lg[0], lg[1]), fmaxf(lg[2], lg[3])), fmaxf(fmaxf(lg[4], lg[5]), fmaxf(lg[6], lg[7])));
    float sm = 0.f;
#pragma unroll
    for (int wv = 0; wv < 8; ++wv) { lg[wv] = __builtin_amdgcn_exp2f((lg[wv] - mx) * 1.4426950408889634f); sm += lg[wv]; }
    const float inv = 1.0f / sm;
    float o[10];
#pragma unroll
    for (int d = 0; d < 10; ++d) o[d] = 0.f;
#pragma unroll
    for (int wp = 0; wp < 4; ++wp) {
      const unsigned pp = pk_bf16(lg[2 * wp] * inv, lg[2 * wp + 1] * inv);
      LAS char* v0 = base - (2 * wp) * 2 * KA_QROW + 192 + hf * 16;
      LAS char* v1 = v0 - 2 * KA_QROW;
      const u32x4 a = *(LAS u32x4*)(v0), b = *(LAS u32x4*)(v1); const unsigned a4 = *(LAS unsigned*)(v0 + 16), b4 = *(LAS unsigned*)(v1 + 16);
      const unsigned av[5] = {a[0], a[1], a[2], a[3], a4}, bv[5] = {b[0], b[1], b[2], b[3], b4};
#pragma unroll
      for (int i = 0; i < 5; ++i) {
        o[2 * i]     = dot2bf(pp, __builtin_amdgcn_perm(bv[i], av[i], 0x05040100u), o[2 * i]);
        o[2 * i + 1] = dot2bf(pp, __builtin_amdgcn_perm(bv[i], av[i], 0x07060302u), o[2 * i + 1]);
      }
    }
    LAS char* op = lds + KA_OOFF + t * 80 + hd * 36 + hf * 16;
#pragma unroll
    for (int i = 0; i < 5; ++i) *(LAS unsigned*)(op + 4 * i) = pk_bf16(o[2 * i], o[2 * i + 1]);
    if (hd == 0) *(LAS unsigned*)(lds + KA_OOFF + t * 80 + 72 + hf * 4) = 0u;
  }
  WAITL0();
  BAR(); asm volatile("" ::: "memory");
  STAMP(5);
#pragma unroll
  for (int i = 0; i < 2; ++i) {
    const int id = tid + i * 512;
    if (id < 640) {
      const int mb = (id * 820) >> 16, rem = id - mb * 80;
      const u32x4 v = *(LAS u32x4*)(lds + KA_OOFF + (mb * 16 + (rem & 15)) * 80 + (rem >> 4) * 16);
      ST16(rs, WS_O + ((mt * 8 + mb) * 80 + hp * 5) * 256 + rem * 16, v);
    }
  }
  WAITV(0);
  BAR(); asm volatile("" ::: "memory");
  STAMP(6);
  GAS unsigned* cnt = (GAS unsigned*)(ws + WS_CNT) + (256 + mt) * 64;
  if (tid == 0) __hip_atomic_fetch_add(cnt, 1u, __ATOMIC_RELAXED, __HIP_MEMORY_SCOPE_AGENT);
  u32x4 wf[4][5];
#define KA_P2R(set, i) do { _Pragma("unroll") for (int _b = 0; _b < 4; ++_b) wf[set][_b] = tr_frag(lds + pa[(i) % 5] + ((i) / 5) * (288 * 144) + _b * 32, 576); \
      wf[set][4] = tr_frag2(lds + pm4[(i) % 5] + ((i) / 5) * p4s, lds + pm4[(i) % 5] + ((i) / 5) * p4s + p4t); } while (0)
  KA_P2R(0, 0); KA_P2R(1, 1); KA_P2R(2, 2); KA_P2R(3, 3);
  SCHED0();
  if (w == 0) {
    unsigned spins = 0;
    while (__builtin_amdgcn_readfirstlane(__hip_atomic_load(cnt, __ATOMIC_RELAXED, __HIP_MEMORY_SCOPE_AGENT)) < 16u) {
      if (++spins > 2000000u) break;
      __builtin_amdgcn_s_sleep(1);
    }
  }
  asm volatile("" ::: "memory");
  BAR(); asm volatile("" ::: "memory");
  STAMP(7);
  {
    const int n8 = hp >> 1;
    const int oo = (int)(WS_O + ((tok >> 4) * 80 + 4 * wn) * 256 + lane * 16);
    u32x4 of[10];
#pragma unroll
    for (int i = 0; i < 10; ++i) of[i] = __builtin_amdgcn_raw_buffer_load_b128(rs, oo + i * 2048, 0, 16);
    f32x4 c[5];
#pragma unroll
    for (int b = 0; b < 5; ++b) c[b] = (f32x4){0.f, 0.f, 0.f, 0.f};
#define KA_P2M(set, i) do { _Pragma("unroll") for (int _b = 0; _b < 5; ++_b) c[_b] = MFMA16(BF8(wf[set][_b]), BF8(of[i]), c[_b]); } while (0)
    SCHED0();
#pragma unroll
    for (int i = 0; i < 10; ++i) {
      KA_P2M(i & 3, i); SCHED0();
      if (i + 4 < 10) KA_P2R(i & 3, i + 4);
      SCHED0();
    }
    STAMP(8);
    if (wn == 1) {
#pragma unroll
      for (int b = 0; b < 5; ++b) *(LAS f32x4*)(lds + KA_RED + (wm * 5 + b) * 1024 + lane * 16) = c[b];
    }
    WAITL0();
    BAR(); asm volatile("" ::: "memory");
    if (wn == 0) {
#pragma unroll
      for (int b = 0; b < 5; ++b) c[b] += *(LAS f32x4*)(lds + KA_RED + (wm * 5 + b) * 1024 + lane * 16);
      const float mean = xm + __shfl(c[4][0] + c[4][1], 32 + r), mb = rbf(mean);
      LAS char* xq = lds + KA_XR + (wm * 16 + r) * 144 + g * 8;
      unsigned hw_[10], cw_[10];
      f32x2 qv = {0.f, 0.f};
#pragma unroll
      for (int b = 0; b < 5; ++b) {
        const u32x2 xv = *(LAS u32x2*)(xq + ((b < 4 || g < 2) ? b * 32 : 0));
#pragma unroll
        for (int e = 0; e < 2; ++e) {
          const unsigned a = pk_bf16(c[b][2 * e], c[b][2 * e + 1]);
          const unsigned hh = pk_bf16(bf_lo(xv[e]) + bf_lo(a), bf_hi(xv[e]) + bf_hi(a));
          const f32x2 hv = {bf_lo(hh), bf_hi(hh)};
          const f32x2 d = hv - mean, dc = hv - mb;
          hw_[2 * b + e] = hh; cw_[2 * b + e] = pk_bf16(dc[0], dc[1]);
          if (b < 4) qv += d * d; else qv += g < 2 ? d * d : (f32x2){0.f, 0.f};
        }
      }
      const int ho = ((tok >> 4) * 72 + n8 * 9 + 2 * (g & 1) + (g >> 1)) * 256 + r * 16;
#pragma unroll
      for (int b = 0; b < 4; b += 2) {
        const auto c0 = __builtin_amdgcn_permlane16_swap(cw_[2 * b], cw_[2 * b + 2], false, false), c1 = __builtin_amdgcn_permlane16_swap(cw_[2 * b + 1], cw_[2 * b + 3], false, false);
        const auto h0 = __builtin_amdgcn_permlane16_swap(hw_[2 * b], hw_[2 * b + 2], false, false), h1 = __builtin_amdgcn_permlane16_swap(hw_[2 * b + 1], hw_[2 * b + 3], false, false);
        ST16(rs, WS_HC + ho + b * 512, ((u32x4){c0[0], c1[0], c0[1], c1[1]}));
        ST16(rs, WS_H + ho + b * 512, ((u32x4){h0[0], h1[0], h0[1], h1[1]}));
      }
      const auto s0 = __builtin_amdgcn_permlane32_swap(cw_[8], hw_[8], false, false), s1 = __builtin_amdgcn_permlane32_swap(cw_[9], hw_[9], false, false);
      ST8(rs, (g < 2 ? WS_HC : WS_H) + ((tok >> 4) * 72 + n8 * 9 + 8) * 256 + r * 16 + (g & 1) * 8, ((u32x2){s0[0], s1[0]}));
      float q = qv[0] + qv[1];
      q += __shfl_xor(q, 16); q += __shfl_xor(q, 32);
      if (g == 0) ST4(rs, WS_ST + (tok * 8 + n8) * 4, __float_as_uint(q));
    }
  }
  STAMP(9); STAMP_OUT(0);
}

constexpr int KC_UPOFF = 16384 + 9216 + 128;
constexpr int KC_STAGE = 35072;
constexpr int KC_STOFF = 4 * KC_STAGE;
constexpr int KC_SCR = KC_STOFF + 8192;
constexpr int KC_LDS = KC_SCR + 256;

DI f32x2 geglu2(f32x2 a, f32x2 b, f32x2 k0, f32x2 k1, f32x2 ik2) {
  const f32x2 tt = a * __builtin_elementwise_fma(a * a, k1, k0);
  const f32x2 e = {__builtin_amdgcn_exp2f(tt[0]), __builtin_amdgcn_exp2f(tt[1])};
  const f32x2 d = __builtin_elementwise_fma(e, ik2, ik2);
  const f32x2 rr = {__builtin_amdgcn_rcpf(d[0]), __builtin_amdgcn_rcpf(d[1])};
  return (a * b) * rr;
}
DI void ln_consts(const LAS char* sp_, f32x2& k0, f32x2& k1, f32x2& ik2) {
  const LAS f32x4* sp = (const LAS f32x4*)sp_;
  const f32x4 s4 = sp[0] + sp[1];
  const float var = rbf(((s4[0] + s4[1]) + (s4[2] + s4[3])) * (1.0f / D_));
  const float sd = rbf(__builtin_amdgcn_sqrtf(rbf(var + 1e-5f)));
  const float rs = __builtin_amdgcn_rcpf(sd);
  const float k0s = -2.3022081981f * rs;
  const float k1s = -0.1029432397f * rs * rs * rs;
  const float ik2s = sd * sd;
  k0 = (f32x2){k0s, k0s}; k1 = (f32x2){k1s, k1s}; ik2 = (f32x2){ik2s, ik2s};
}

extern "C" __global__ __launch_bounds__(768, 1) void kC(const u16* __restrict__ wg, const u16* __restrict__ wu, const u16* __restrict__ wd, char* __restrict__ ws) {
  extern __shared__ __attribute__((aligned(16))) char smem[];
  LAS char* lds = (LAS char*)smem;
  const int tid = threadIdx.x, lane = tid & 63, w = __builtin_amdgcn_readfirstlane(tid >> 6);
  const int bx = blockIdx.x, xcd = bx & 7, jj = bx >> 3;
  const int pm = (xcd & 1) * 4 + (jj & 3), pn = (xcd >> 1) * 8 + (jj >> 2);
  const int wm = w & 3, wn = w >> 2;

  STAMP_DECL STAMP(0);
  const char* src[3]; unsigned dst[3]; unsigned step[3];
  src[0] = ws + WS_HC + (size_t)(pm * 16 + w) * (72 * 256) + lane * 16; dst[0] = w * 1024; step[0] = 1024;
#define KC_G(i, st) do { glds16(src[i], lds + ((st) & 3) * KC_STAGE + dst[i]); src[i] += step[i]; } while (0)
  KC_G(0, 0);
#pragma unroll
  for (int i = 1; i < 3; ++i) {
    int sl = i * 12 + w; sl = sl > 33 ? 33 : sl;
    if (sl < 16) {
      src[i] = ws + WS_HC + (size_t)(pm * 16 + sl) * (72 * 256) + lane * 16;
      dst[i] = sl * 1024; step[i] = 1024;
    } else {
      const int c = sl - 16, m = c >= 9 ? 1 : 0, cc = c - 9 * m, q = cc * 64 + lane, rr = (q * 3641) >> 16, ch = q - rr * 18;
      const int kr = (rr & 19) | ((rr & 4) << 1) | ((rr & 8) >> 1);
      src[i] = (const char*)(m ? wu : wg) + pn * 288 + (unsigned)(kr * (F_ * 2) + ch * 16);
      dst[i] = (m ? KC_UPOFF : 16384) + cc * 1024; step[i] = 32 * F_ * 2;
    }
    KC_G(i, 0);
  }
#define KC_ISSUE(st) do { KC_G(0, st); KC_G(1, st); KC_G(2, st); } while (0)
  KC_ISSUE(1); KC_ISSUE(2);
  glds16(ws + WS_ST + (size_t)pm * 8192 + (w & 7) * 1024 + lane * 16, lds + KC_STOFF + (w & 7) * 1024);
  const auto ars = __builtin_amdgcn_make_buffer_rsrc((void*)(ws + WS_ACT), 0, S_ * F_ * 2, 0x00020000);
#define WDW 6
#define KC_TOUCH_WD() do { if (w < WDW) {     \
    int ln = w * 64 + lane; ln = ln < 324 ? ln : 323; \
    touch4((const char*)wd + (size_t)(xcd >> 1) * (1152 * D_ * 2) + (size_t)(jj * 324 + ln) * 128, lds + KC_SCR); } \
    else if (w == 6) touch_code(CODE_ADDR("kD"), CODE_ADDR("kD_end_lbl"), lane, lds + KC_SCR); } while (0)

  const int g = lane >> 4, r = lane & 15;
  f32x4 acc[6][4];
#pragma unroll
  for (int a = 0; a < 6; ++a)
#pragma unroll
    for (int b = 0; b < 4; ++b) acc[a][b] = (f32x4){0.f, 0.f, 0.f, 0.f};
  const unsigned xadr = (unsigned)(wm * 4096 + lane * 16);
  const unsigned wadr = (unsigned)(16384 + (16 * (g >> 1) + 4 * (g & 1) + (r >> 2)) * 288 + wn * 96 + (r & 3) * 8);
  u32x4 xf[4], wA[3], wB[3];
#define KC_WOFF(j) ((((j) & 1) ? (KC_UPOFF - 16384) : 0) + ((j) >> 1) * 32)
#define KC_RW(WF, i, j, base) WF[i] = tr_frag((base) + wadr + KC_WOFF(j), 2304)
#define KC_RX(c, base) xf[c] = *(LAS u32x4*)((base) + xadr + (c) * 1024)
#define KC_M3(WF, j0, c) do { acc[j0][c] = MFMA16(BF8(WF[0]), BF8(xf[c]), acc[j0][c]); acc[j0 + 1][c] = MFMA16(BF8(WF[1]), BF8(xf[c]), acc[j0 + 1][c]); \
    acc[j0 + 2][c] = MFMA16(BF8(WF[2]), BF8(xf[c]), acc[j0 + 2][c]); } while (0)
#define KC_HALFA(base, G0, G1) do { \
    KC_M3(wA, 0, 0); SCHED0(); KC_RW(wB, 0, 3, base); SCHED0(); \
    KC_M3(wA, 0, 1); SCHED0(); KC_RW(wB, 1, 4, base); G0; SCHED0(); \
    KC_M3(wA, 0, 2); SCHED0(); KC_RW(wB, 2, 5, base); SCHED0(); \
    KC_M3(wA, 0, 3); SCHED0(); G1; SCHED0(); } while (0)
#define KC_HALFB(nb, G0) do { \
    KC_M3(wB, 3, 0); SCHED0(); KC_RX(0, nb); KC_RW(wA, 0, 0, nb); SCHED0(); \
    KC_M3(wB, 3, 1); SCHED0(); KC_RX(1, nb); KC_RW(wA, 1, 1, nb); SCHED0(); \
    KC_M3(wB, 3, 2); SCHED0(); KC_RX(2, nb); KC_RW(wA, 2, 2, nb); G0; SCHED0(); \
    KC_M3(wB, 3, 3); SCHED0(); KC_RX(3, nb); SCHED0(); } while (0)
  STAMP(1);
  WAITV(7); BAR(); asm volatile("" ::: "memory");
  STAMP(2);
  KC_RX(0, lds); KC_RW(wA, 0, 0, lds); KC_RW(wA, 1, 1, lds); KC_RW(wA, 2, 2, lds); KC_RX(1, lds); KC_RX(2, lds); KC_RX(3, lds);
  KC_ISSUE(3);
  KC_HALFA(lds, (void)0, (void)0);
  STAMP(3);
#pragma unroll 1
  for (int t = 1; t < 15; ++t) {
    WAITVL(6);
    BAR(); asm volatile("" ::: "memory");
    LAS char* sb = lds + (t & 3) * KC_STAGE;
    KC_HALFB(sb, KC_G(0, t + 3));
    KC_HALFA(sb, KC_G(1, t + 3), KC_G(2, t + 3));
  }
  WAITVL(0);
  BAR(); asm volatile("" ::: "memory");
  KC_TOUCH_WD();
#define KC_T0 15
#pragma unroll 1
  for (int t = KC_T0; t < 18; ++t) {
    LAS char* sb = lds + (t & 3) * KC_STAGE;
    KC_HALFB(sb, (void)0);
    KC_HALFA(sb, (void)0, (void)0);
  }
  KC_M3(wB, 3, 0); KC_M3(wB, 3, 1); KC_M3(wB, 3, 2); KC_M3(wB, 3, 3);
  STAMP(4);
#pragma unroll
  for (int cp = 0; cp < 2; ++cp) {
    f32x2 k0[2], k1[2], ik2[2];
#pragma unroll
    for (int e = 0; e < 2; ++e) ln_consts(lds + KC_STOFF + (wm * 64 + cp * 32 + e * 16 + r) * 32, k0[e], k1[e], ik2[e]);
#pragma unroll
    for (int f = 0; f < 3; ++f) {
      u32x2 pc[2];
#pragma unroll
      for (int e = 0; e < 2; ++e) {
        const f32x4 a = acc[2 * f][2 * cp + e], b = acc[2 * f + 1][2 * cp + e];
        const f32x2 o0 = geglu2((f32x2){a[0], a[1]}, (f32x2){b[0], b[1]}, k0[e], k1[e], ik2[e]);
        const f32x2 o1 = geglu2((f32x2){a[2], a[3]}, (f32x2){b[2], b[3]}, k0[e], k1[e], ik2[e]);
        pc[e] = (u32x2){pk_bf16(o0[0], o0[1]), pk_bf16(o1[0], o1[1])};
      }
      const auto s0 = __builtin_amdgcn_permlane32_swap(pc[0][0], pc[1][0], false, false);
      const auto s1 = __builtin_amdgcn_permlane32_swap(pc[0][1], pc[1][1], false, false);
      const u32x4 v = {s0[0], s1[0], s0[1], s1[1]};
      __builtin_amdgcn_raw_buffer_store_b128(v, ars, ((pm * 8 + wm * 2 + cp) * 288 + pn * 9 + wn * 3 + f) * 1024 + ((g & 1) * 32 + (g >> 1) * 16 + r) * 16, 0, 16);
    }
  }
  STAMP(5); STAMP_OUT(2);
}

constexpr int KD_STAGE = 16384 + 18432;
constexpr int KD_LDS = 2 * KD_STAGE + 72 * 1024;

template <int NB, int NG>
DI void kD_loop(LAS char* lds, const char* srcA, const char* const (&srcW)[3], int w, int lane, f32x4 (&acc)[4][5]) {
  const int gk = w >> 2, wm = w & 1, g = lane >> 4, r = lane & 15;
#define KD_GA(i, st) glds16(srcA + (size_t)(i) * (2 * 288 * 1024) + (st) * 4096, lds + ((st) & 3) * KD_STAGE + (i) * 8192 + w * 1024)
#define KD_GW(i, st) glds16(srcW[i] + (size_t)(st) * (64 * D_ * 2), lds + ((st) & 3) * KD_STAGE + 16384 + (i) * 8192 + w * 1024)
#define KD_GW2(st) do { if (NG == 5) glds16(srcW[2] + (size_t)(st) * (64 * D_ * 2), lds + ((st) & 3) * KD_STAGE + 32768 + (w & 1) * 1024); } while (0)
#define KD_WAIT2() do { if (NG == 5) WAITVL(10); else WAITVL(8); } while (0)
  const unsigned xadr = (unsigned)(wm * 8192 + (gk * 2 + (g >> 1)) * 1024 + ((g & 1) * 32 + r) * 16);
  const unsigned wadr = (unsigned)(16384 + (gk * 32 + 16 * (g >> 1) + 4 * (g & 1) + (r >> 2)) * 288 + (NB == 5 ? 0 : 160) + (r & 3) * 8);
  u32x4 xP[4], wP[5], xQ[4], wQ[5];
#define KD_RW(WF, b, base) WF[b] = tr_frag((base) + wadr + (b) * 32, 2304)
#define KD_RX(XF, a, base) XF[a] = *(LAS u32x4*)((base) + xadr + ((a) >> 1) * 4096 + ((a) & 1) * 256)
#define KD_M(XC, WC, b) do { _Pragma("unroll") for (int _a = 0; _a < 4; ++_a) acc[_a][b] = MFMA16(BF8(WC[b]), BF8(XC[_a]), acc[_a][b]); } while (0)
#define KD_STEP(XC, WC, XN, WN, nb, G0, G1, G2, G3) do { \
    KD_M(XC, WC, 0); SCHED0(); KD_RW(WN, 0, nb); KD_RX(XN, 0, nb); KD_RX(XN, 1, nb); G0; SCHED0(); \
    KD_M(XC, WC, 1); SCHED0(); KD_RX(XN, 2, nb); KD_RX(XN, 3, nb); KD_RW(WN, 1, nb); G1; SCHED0(); \
    KD_M(XC, WC, 2); SCHED0(); KD_RW(WN, 2, nb); KD_RW(WN, 3, nb); G2; SCHED0(); \
    KD_M(XC, WC, 3); SCHED0(); if (NB == 5) KD_RW(WN, 4, nb); G3; SCHED0(); \
    if (NB == 5) { KD_M(XC, WC, 4); SCHED0(); } } while (0)
  KD_RW(wP, 0, lds); KD_RX(xP, 0, lds); KD_RX(xP, 1, lds); KD_RX(xP, 2, lds); KD_RX(xP, 3, lds); KD_RW(wP, 1, lds); KD_RW(wP, 2, lds); KD_RW(wP, 3, lds);
  if (NB == 5) KD_RW(wP, 4, lds);
  KD_GA(0, 3); KD_GA(1, 3); KD_GW(0, 3); KD_GW(1, 3); KD_GW2(3);
#pragma unroll 1
  for (int t = 1; t < 15; t += 2) {
    KD_WAIT2();
    BAR(); asm volatile("" ::: "memory");
    KD_STEP(xP, wP, xQ, wQ, lds + (t & 3) * KD_STAGE, do { KD_GA(0, t + 3); KD_GW2(t + 3); } while (0), KD_GA(1, t + 3), KD_GW(0, t + 3), KD_GW(1, t + 3));
    KD_WAIT2();
    BAR(); asm volatile("" ::: "memory");
    KD_STEP(xQ, wQ, xP, wP, lds + ((t + 1) & 3) * KD_STAGE, do { KD_GA(0, t + 4); KD_GW2(t + 4); } while (0), KD_GA(1, t + 4), KD_GW(0, t + 4), KD_GW(1, t + 4));
  }
  KD_WAIT2(); BAR(); asm volatile("" ::: "memory");
  KD_STEP(xP, wP, xQ, wQ, lds + 3 * KD_STAGE, (void)0, (void)0, (void)0, (void)0);
  if (NG == 5) WAITVL(5); else WAITVL(4);
  BAR(); asm volatile("" ::: "memory");
  KD_STEP(xQ, wQ, xP, wP, lds, (void)0, (void)0, (void)0, (void)0);
  WAITVL(0); BAR(); asm volatile("" ::: "memory");
  KD_STEP(xP, wP, xQ, wQ, lds + KD_STAGE, (void)0, (void)0, (void)0, (void)0);
  KD_M(xQ, wQ, 0); KD_M(xQ, wQ, 1); KD_M(xQ, wQ, 2); KD_M(xQ, wQ, 3);
  if (NB == 5) KD_M(xQ, wQ, 4);
}

extern "C" __global__ __launch_bounds__(512, 1) void kD(const u16* __restrict__ wd, char* __restrict__ ws, u16* __restrict__ out) {
  extern __shared__ __attribute__((aligned(16))) char smem[];
  LAS char* lds = (LAS char*)smem;
  const int tid = threadIdx.x, lane = tid & 63, w = __builtin_amdgcn_readfirstlane(tid >> 6);
  const int bx = blockIdx.x, xcd = bx & 7, jj = bx >> 3;
  const int ks = xcd >> 1, mt = (xcd & 1) * 8 + (jj & 7), nt = jj >> 3;
  const int gk = w >> 2, wm = w & 1, wn = ((w >> 1) & 1) ^ gk;
  const int g = lane >> 4, r = lane & 15;

  STAMP_DECL STAMP(0);
  const char* srcA = ws + WS_ACT + (size_t)((mt * 4 + (w >> 2)) * 288 + ks * 72 + (w & 3)) * 1024 + lane * 16;
  KD_GA(0, 0); KD_GA(1, 0);
  const char* srcW[3];
#pragma unroll
  for (int i = 0; i < 3; ++i) {
    const int cc = i < 2 ? i * 8 + w : 16 + (w & 1);
    const int q = cc * 64 + lane, rr = (q * 3641) >> 16, ch = q - rr * 18;
    srcW[i] = (const char*)wd + (size_t)(ks * 1152 * D_ + nt * 144) * 2 + (unsigned)(rr * (D_ * 2) + ch * 16);
  }
  const bool extra = (w == 2 || w == 3);
#define KD_GX(st) do { if (extra) glds16(srcW[2] + (size_t)(st) * (64 * D_ * 2), lds + ((st) & 3) * KD_STAGE + 32768 + (w & 1) * 1024); } while (0)
  KD_GW(0, 0); KD_GW(1, 0); KD_GX(0);
  KD_GA(0, 1); KD_GA(1, 1); KD_GW(0, 1); KD_GW(1, 1); KD_GX(1);
  KD_GA(0, 2); KD_GA(1, 2); KD_GW(0, 2); KD_GW(1, 2); KD_GX(2);
  const char* hb = ws + WS_H + (size_t)((mt * 8 + ks * 2 + (g >> 1)) * 72 + nt * 18) * 256 + r * 16 + (g & 1) * 8;
  const u32x2 ha0 = *(const u32x2*)(hb + w * 512), ha1 = *(const u32x2*)(hb + w * 512 + 256), hb0 = *(const u32x2*)(hb + 4096), hb1 = *(const u32x2*)(hb + 4096 + 256);
  const u32x4 hres0 = {ha0[0], ha0[1], ha1[0], ha1[1]}, hres1 = {hb0[0], hb0[1], hb1[0], hb1[1]};
  asm volatile("" ::: "memory");
  f32x4 acc[4][5];
#pragma unroll
  for (int a = 0; a < 4; ++a)
#pragma unroll
    for (int b = 0; b < 5; ++b) acc[a][b] = (f32x4){0.f, 0.f, 0.f, 0.f};
  STAMP(1);
  if (extra) WAITVL(10); else WAITVL(8);
  BAR(); asm volatile("" ::: "memory");
  STAMP(2);
  if (wn == 0) kD_loop<5, 4>(lds, srcA, srcW, w, lane, acc);
  else if (extra) kD_loop<4, 5>(lds, srcA, srcW, w, lane, acc);
  else kD_loop<4, 4>(lds, srcA, srcW, w, lane, acc);
  STAMP(3);
  const int qk = 2 * wm + gk;
  {
    LAS char* xw = lds + 2 * KD_STAGE + ((qk ^ 1) * 18 + wn * 5) * 1024 + lane * 16;
#pragma unroll
    for (int sb = 0; sb < 2; ++sb)
#pragma unroll
      for (int b = 0; b < 5; ++b)
        if (b < 4 || wn == 0) *(LAS f32x4*)(xw + (sb * 9 + b) * 1024) = gk ? acc[sb][b] : acc[2 + sb][b];
  }
  WAITL0();
  BAR(); asm volatile("" ::: "memory");
  STAMP(4);
  const int tile = mt * 4 + nt;
  const auto prs = __builtin_amdgcn_make_buffer_rsrc((void*)(ws + WS_PART), 0, (int)(64 * 4 * 3 * PART_SLAB), 0x00020000);
  {
    LAS char* xr = lds + 2 * KD_STAGE + (qk * 18 + wn * 5) * 1024 + lane * 16;
    const int sidx = ks - (ks > qk ? 1 : 0);
    const int po = (int)(((tile * 4 + qk) * 3 + sidx) * PART_SLAB) + wn * 5120 + lane * 16;
#pragma unroll
    for (int b = 0; b < 5; ++b)
      if (b < 4 || wn == 0) {
        const f32x4 v0 = (gk ? acc[2][b] : acc[0][b]) + *(LAS f32x4*)(xr + b * 1024);
        const f32x4 v1 = (gk ? acc[3][b] : acc[1][b]) + *(LAS f32x4*)(xr + (9 + b) * 1024);
        if (qk != ks) {
          const auto s0 = __builtin_amdgcn_permlane32_swap(pk_bf16(v0[0], v0[1]), pk_bf16(v1[0], v1[1]), false, false);
          const auto s1 = __builtin_amdgcn_permlane32_swap(pk_bf16(v0[2], v0[3]), pk_bf16(v1[2], v1[3]), false, false);
          __builtin_amdgcn_raw_buffer_store_b128((u32x4){s0[0], s1[0], s0[1], s1[1]}, prs, po + b * 1024, 0, 16);
        } else {
          *(LAS f32x4*)(lds + (wn * 5 + b) * 2048 + lane * 16) = v0;
          *(LAS f32x4*)(lds + (wn * 5 + b) * 2048 + 1024 + lane * 16) = v1;
        }
      }
  }
  STAMP(5);
  WAITVL(0);
  BAR(); asm volatile("" ::: "memory");
  STAMP(6);
  GAS unsigned* cnt = (GAS unsigned*)(ws + WS_CNT);
  if (tid < 4 && tid != ks) __hip_atomic_fetch_add(cnt + (tile * 4 + tid) * 64, 1u, __ATOMIC_RELAXED, __HIP_MEMORY_SCOPE_AGENT);
  if (w == 0) {
    unsigned spins = 0;
    while (__builtin_amdgcn_readfirstlane(__hip_atomic_load(cnt + (tile * 4 + ks) * 64, __ATOMIC_RELAXED, __HIP_MEMORY_SCOPE_AGENT)) < 3u) {
      if (++spins > 2000000u) break;
      __builtin_amdgcn_s_sleep(1);
    }
  }
  asm volatile("" ::: "memory");
  BAR(); asm volatile("" ::: "memory");
  STAMP(7);
  u32x4 pp[2][3];
#pragma unroll
  for (int i = 0; i < 2; ++i)
#pragma unroll
    for (int q = 0; q < 3; ++q)
      if (i == 0 || w == 1) pp[i][q] = __builtin_amdgcn_raw_buffer_load_b128(prs, (int)(((tile * 4 + ks) * 3 + q) * PART_SLAB) + (i ? 8 : w) * 1024 + lane * 16, 0, 16);
#pragma unroll
  for (int i = 0; i < 2; ++i) {
    if (i == 1 && w != 1) break;
    const int grp = i ? 8 : w;
    const u32x4 hr = i ? hres1 : hres0;
    u16* op = out + (size_t)(mt * 128 + ks * 32 + (g >> 1) * 16 + r) * D_ + nt * 144 + grp * 16 + 4 * (g & 1);
#pragma unroll
    for (int hf = 0; hf < 2; ++hf) {
      f32x4 v = *(LAS f32x4*)(lds + grp * 2048 + (g >> 1) * 1024 + (((g & 1) + 2 * hf) * 16 + r) * 16);
#pragma unroll
      for (int q = 0; q < 3; ++q) v += (f32x4){bf_lo(pp[i][q][2 * hf]), bf_hi(pp[i][q][2 * hf]), bf_lo(pp[i][q][2 * hf + 1]), bf_hi(pp[i][q][2 * hf + 1])};
      const unsigned a0 = pk_bf16(v[0], v[1]), a1 = pk_bf16(v[2], v[3]);
      const unsigned o0 = pk_bf16(bf_lo(hr[2 * hf]) + bf_lo(a0), bf_hi(hr[2 * hf]) + bf_hi(a0));
      const unsigned o1 = pk_bf16(bf_lo(hr[2 * hf + 1]) + bf_lo(a1), bf_hi(hr[2 * hf + 1]) + bf_hi(a1));
      *(u32x2*)(op + 8 * hf) = (u32x2){o0, o1};
    }
  }
  STAMP(8); STAMP_OUT(3);
  asm volatile("kD_end_lbl:");
}

extern "C" void kernel_launch(void* const* d_in, const int* in_sizes, int n_in, void* d_out, int out_size, void* d_ws, size_t ws_size,
                              hipStream_t stream) {
  const u16* x = (const u16*)d_in[0];
  const u16* wqkv = (const u16*)d_in[1];
  const u16* wo = (const u16*)d_in[2];
  const u16* wg = (const u16*)d_in[4];
  const u16* wu = (const u16*)d_in[5];
  const u16* wd = (const u16*)d_in[6];
  char* ws = (char*)d_ws;
  (void)hipFuncSetAttribute((const void*)kA, hipFuncAttributeMaxDynamicSharedMemorySize, KA_LDS);
  (void)hipFuncSetAttribute((const void*)kC, hipFuncAttributeMaxDynamicSharedMemorySize, KC_LDS);
  (void)hipFuncSetAttribute((const void*)kD, hipFuncAttributeMaxDynamicSharedMemorySize, KD_LDS);
  k0<<<PFMODE == 3 ? 800 : 544, 512, K0_LDS, stream>>>(x, wqkv, wo, wg, wu, ws);
  kA<<<256, 640, KA_LDS, stream>>>(x, wo, wg, wu, wd, ws);
  kC<<<256, 768, KC_LDS, stream>>>(wg, wu, wd, ws);
  kD<<<256, 512, KD_LDS, stream>>>(wd, ws, (u16*)d_out);
}
```

```cpp
#include <hip/hip_runtime.h>
#include <hip/hip_bf16.h>
#include <stdint.h>

#define LAS __attribute__((address_space(3)))
#define GAS __attribute__((address_space(1)))
#define DI __device__ __forceinline__
typedef unsigned short u16;
typedef short s16x4 __attribute__((ext_vector_type(4)));
typedef __bf16 bf16x2 __attribute__((ext_vector_type(2)));
typedef __bf16 bf16x8 __attribute__((ext_vector_type(8)));
typedef float f32x2 __attribute__((ext_vector_type(2)));
typedef float f32x4 __attribute__((ext_vector_type(4)));
typedef unsigned u32x2 __attribute__((ext_vector_type(2)));
typedef unsigned u32x4 __attribute__((ext_vector_type(4)));

constexpr int S_ = 2048, D_ = 576, F_ = 4608;

constexpr size_t WS_O    = 0;
constexpr size_t WS_XN   = WS_O + (size_t)S_ * 640 * 2;
constexpr size_t WS_WQ   = WS_XN + (size_t)S_ * D_ * 2;
constexpr size_t WS_HC   = WS_WQ + (size_t)16 * 18 * 7 * 1024;
constexpr size_t WS_H    = WS_HC + (size_t)S_ * D_ * 2;
constexpr size_t WS_ST   = WS_H + (size_t)S_ * D_ * 2;
constexpr size_t WS_XM   = WS_ST + (size_t)S_ * 16 * 4;
constexpr size_t WS_WM   = WS_XM + (size_t)S_ * 4;
constexpr size_t WS_CNT  = WS_WM + 8192;
constexpr size_t WS_ACT  = WS_CNT + 512 * 256;
constexpr size_t WS_PART = WS_ACT + (size_t)S_ * F_ * 2;
constexpr size_t PART_SLAB = 9 * 1024;

DI float bf_lo(unsigned v) { return __uint_as_float(v << 16); }
DI float bf_hi(unsigned v) { return __uint_as_float(v & 0xffff0000u); }
DI unsigned pk_bf16(float a, float b) { f32x2 v = {a, b}; return __builtin_bit_cast(unsigned, __builtin_convertvector(v, bf16x2)); }
DI float rbf(float a) { return bf_lo(pk_bf16(a, 0.f)); }
DI float dot2bf(unsigned a, unsigned b, float c) { return __builtin_amdgcn_fdot2_f32_bf16(__builtin_bit_cast(bf16x2, a), __builtin_bit_cast(bf16x2, b), c, false); }
DI s16x4 tr_read(LAS char* p) { return __builtin_amdgcn_ds_read_tr16_b64_v4i16((LAS s16x4*)p); }
DI void glds16(const void* g, LAS char* l) {
  unsigned keep; const unsigned la = __builtin_amdgcn_readfirstlane((unsigned)(size_t)l);
  asm volatile("s_mov_b32 %0, m0\n\ts_mov_b32 m0, %2\n\ts_nop 0\n\tglobal_load_lds_dwordx4 %1, off\n\ts_mov_b32 m0, %0" : "=&s"(keep) : "v"(g), "s"(la) : "memory");
}
DI u32x4 tr_frag(LAS char* p, int second) {
  const u32x2 a = __builtin_bit_cast(u32x2, tr_read(p)), b = __builtin_bit_cast(u32x2, tr_read(p + second));
  return (u32x4){a[0], a[1], b[0], b[1]};
}
DI u32x4 tr_frag2(LAS char* p0, LAS char* p1) {
  const u32x2 a = __builtin_bit_cast(u32x2, tr_read(p0)), b = __builtin_bit_cast(u32x2, tr_read(p1));
  return (u32x4){a[0], a[1], b[0], b[1]};
}
#define BF8(v) __builtin_bit_cast(bf16x8, (v))
DI void touch4(const void* g, LAS char* l) {
  unsigned keep; const unsigned la = __builtin_amdgcn_readfirstlane((unsigned)(size_t)l);
  asm volatile("s_mov_b32 %0, m0\n\ts_mov_b32 m0, %2\n\ts_nop 0\n\tglobal_load_lds_dword %1, off\n\ts_mov_b32 m0, %0" : "=&s"(keep) : "v"(g), "s"(la) : "memory");
}
DI float wave_sum(float v) {
#define DPP_ADD(ctrl) v += __builtin_bit_cast(float, __builtin_amdgcn_mov_dpp(__builtin_bit_cast(int, v), ctrl, 0xF, 0xF, true))
  DPP_ADD(0xB1); DPP_ADD(0x4E); DPP_ADD(0x141); DPP_ADD(0x140);
#undef DPP_ADD
  const int iv = __builtin_bit_cast(int, v);
  return (__builtin_bit_cast(float, __builtin_amdgcn_readlane(iv, 0)) + __builtin_bit_cast(float, __builtin_amdgcn_readlane(iv, 16))) +
         (__builtin_bit_cast(float, __builtin_amdgcn_readlane(iv, 32)) + __builtin_bit_cast(float, __builtin_amdgcn_readlane(iv, 48)));
}
#define CODE_ADDR(sym) ({ unsigned long long _pc; int _off; asm volatile("s_getpc_b64 %0\n\ts_mov_b32 %1, " sym "@rel32@lo+4" : "=s"(_pc), "=s"(_off)); (const char*)(_pc + (long long)_off); })
DI void touch_code(const char* lo, const char* hi, int lane, LAS char* scr) {
  const long long n = (hi - lo) >> 7;
  if (n > 0 && n <= 512) {
#pragma unroll 1
    for (int i = 0; i < (int)n; i += 64) { const int l = i + lane < (int)n ? i + lane : (int)n - 1; touch4(lo + (size_t)l * 128, scr); }
  }
}
#define MFMA16(a, b, c) __builtin_amdgcn_mfma_f32_16x16x32_bf16((a), (b), (c), 0, 0, 0)
#define BAR() __builtin_amdgcn_s_barrier()
#define SCHED0() __builtin_amdgcn_sched_barrier(0)
#define WAITV(n) asm volatile("s_waitcnt vmcnt(" #n ")" ::: "memory")
#define WAITVL(n) asm volatile("s_waitcnt vmcnt(" #n ") lgkmcnt(0)" ::: "memory")
#define WAITL0() asm volatile("s_waitcnt lgkmcnt(0)" ::: "memory")


#define WS_RSRC(ws) __builtin_amdgcn_make_buffer_rsrc((void*)(ws), 0, (int)WS_ACT, 0x00020000)
#define ST16(rs, off, v) __builtin_amdgcn_raw_buffer_store_b128((v), (rs), (int)(off), 0, 16)
#define ST8(rs, off, v) __builtin_amdgcn_raw_buffer_store_b64((v), (rs), (int)(off), 0, 16)
#define ST4(rs, off, v) __builtin_amdgcn_raw_buffer_store_b32((v), (rs), (int)(off), 0, 16)

constexpr int K0_SCR = 9216;
constexpr int K0_LDS = K0_SCR + 256;

extern "C" __global__ __launch_bounds__(512, 2) void k0(const u16* __restrict__ x, const u16* __restrict__ wqkv, const u16* __restrict__ wo,
                                                        char* __restrict__ ws) {
  extern __shared__ __attribute__((aligned(16))) char smem[];
  LAS char* lds = (LAS char*)smem;
  const int tid = threadIdx.x, lane = tid & 63, w = __builtin_amdgcn_readfirstlane(tid >> 6);
  const int bx = blockIdx.x;
  const auto rs = WS_RSRC(ws);
  if (bx < 256) {
    const int row = bx * 8 + w, l36 = lane < 36 ? lane : 35;
    const char* xp = (const char*)x + (size_t)row * (D_ * 2) + l36 * 32;
    const u32x4 va = *(const u32x4*)xp, vb = *(const u32x4*)(xp + 16);
    f32x2 v[8];
#pragma unroll
    for (int e = 0; e < 4; ++e) { v[e] = (f32x2){bf_lo(va[e]), bf_hi(va[e])}; v[4 + e] = (f32x2){bf_lo(vb[e]), bf_hi(vb[e])}; }
    const f32x2 s2 = ((v[0] + v[1]) + (v[2] + v[3])) + ((v[4] + v[5]) + (v[6] + v[7]));
    const float mean = wave_sum(lane < 36 ? s2[0] + s2[1] : 0.f) * (1.0f / D_), mb = rbf(mean);
    f32x2 q2 = {0.f, 0.f};
    unsigned pk[8];
#pragma unroll
    for (int e = 0; e < 8; ++e) { const f32x2 d = v[e] - mb; q2 = __builtin_elementwise_fma(d, d, q2); pk[e] = pk_bf16(d[0], d[1]); }
    const float dm = mean - mb;
    const float var = rbf(fmaxf(wave_sum(lane < 36 ? q2[0] + q2[1] : 0.f) * (1.0f / D_) - dm * dm, 0.f));
    const float rsd = __builtin_amdgcn_rcpf(rbf(__builtin_amdgcn_sqrtf(rbf(var + 1e-5f))));
#pragma unroll
    for (int e = 0; e < 8; ++e) pk[e] = pk_bf16(bf_lo(pk[e]) * rsd, bf_hi(pk[e]) * rsd);
    if (lane < 36) {
      *(LAS u32x4*)(lds + lane * 256 + w * 16) = (u32x4){pk[0], pk[1], pk[4], pk[5]};
      *(LAS u32x4*)(lds + lane * 256 + 128 + w * 16) = (u32x4){pk[2], pk[3], pk[6], pk[7]};
    }
    if (lane == 0) ST4(rs, WS_XM + row * 4, __float_as_uint(mean));
    WAITL0();
    BAR(); asm volatile("" ::: "memory");
#pragma unroll
    for (int i = 0; i < 2; ++i) {
      const int t = tid + i * 512;
      if (t < 576) {
        const u32x4 c = *(LAS u32x4*)(lds + t * 16);
        ST16(rs, WS_XN + ((bx >> 2) * 36 + (t >> 4)) * 1024 + (((t >> 3) & 1) * 32 + (bx & 3) * 8 + (t & 7)) * 16, c);
      }
    }
  } else if (bx < 472) {
    const int u = bx - 256, ks = (u * 5462) >> 16, rem = u - ks * 12, seg = rem >> 2, hq = rem & 3;
#pragma unroll
    for (int i = 0; i < 2; ++i) {
      const int cc = i * 8 + w;
      if (cc < 9) {
        const int q = cc * 64 + lane, rr = (q * 3641) >> 16, ch = q - rr * 18;
        glds16((const char*)wqkv + (size_t)(ks * 32) * 3456 + seg * 1152 + hq * 288 + (unsigned)(rr * 3456 + ch * 16), lds + cc * 1024);
      }
    }
    const int g = lane >> 4, r = lane & 15;
    const unsigned adr = (unsigned)((16 * (g >> 1) + 4 * (g & 1) + (r >> 2)) * 288 + (r & 3) * 8);
    int dsto[2];
#pragma unroll
    for (int i = 0; i < 2; ++i) {
      const int c = (i * 8 + w) * 16 + r, hl = (c * 1821) >> 16, pcol = seg * 36 + c - hl * 36;
      dsto[i] = (int)(WS_WQ + (((hq * 4 + hl) * 18 + ks) * 7 + (pcol >> 4)) * 1024 + (g * 16 + (pcol & 15)) * 16);
    }
    if (seg == 2 && w == 1)
      ST16(rs, WS_WQ + (((hq * 4 + (lane >> 4)) * 18 + ks) * 7 + 6) * 1024 + (((lane >> 2) & 3) * 16 + 12 + (lane & 3)) * 16, ((u32x4){0u, 0u, 0u, 0u}));
    WAITV(0);
    BAR(); asm volatile("" ::: "memory");
    ST16(rs, dsto[0], tr_frag(lds + adr + w * 32, 2304));
    if (w == 0) ST16(rs, dsto[1], tr_frag(lds + adr + 256, 2304));
  } else {
    const int wrow = (bx - 472) * 8 + w, l36 = lane < 36 ? lane : 35;
    const char* rp = (const char*)wo + (size_t)wrow * (D_ * 2) + l36 * 32;
    const u32x4 wa = *(const u32x4*)rp, wb = *(const u32x4*)(rp + 16);
    if (bx == 472 && tid < 272) ST4(rs, WS_CNT + tid * 256, 0u);
    if (bx < 480 + 8 && w == 7) touch_code(CODE_ADDR("k0"), CODE_ADDR("kC"), lane, lds + K0_SCR);
    float t = 0.f;
#pragma unroll
    for (int e = 0; e < 4; ++e) { t = dot2bf(wa[e], 0x3f803f80u, t); t = dot2bf(wb[e], 0x3f803f80u, t); }
    t = wave_sum(lane < 36 ? t : 0.f) * (1.0f / D_);
    const float hi = rbf(t);
    if (lane == 0) ST8(rs, WS_WM + wrow * 8, ((u32x2){pk_bf16(hi, t - hi), 0u}));
  }
}

constexpr int KA_STAGE = 16384;
constexpr int KA_QROW = 304;
constexpr int KA_OOFF = 144 * KA_QROW;
constexpr int KA_WO = 54272;
constexpr int KA_WM = KA_WO + 82944;
constexpr int KA_XR = KA_WM + 5120;
constexpr int KA_SCR = KA_XR + 9216;
constexpr int KA_LDS = KA_SCR + 256;
constexpr int KA_RED = 0;

template <int NB>
DI void kA_loop(LAS char* lds, const char* s0, const char* s1, unsigned st0, unsigned st1, int w, int lane, f32x4 (&acc)[2][4], f32x4& acch) {
  const int g = lane >> 4, r = lane & 15, wm = w & 3;
#define KA_G0(sl) do { glds16(s0, lds + (sl) * KA_STAGE + w * 1024); s0 += st0; } while (0)
#define KA_G1(sl) do { glds16(s1, lds + (sl) * KA_STAGE + 8192 + w * 1024); s1 += st1; } while (0)
  const unsigned xadr = (unsigned)(wm * 2048 + (g >> 1) * 1024 + ((g & 1) * 32 + r) * 16);
  const unsigned hadr = (unsigned)(8192 + lane * 16);
  const unsigned wadr = (unsigned)(9216 + (NB == 4 ? 0 : 4096) + lane * 16);
  const unsigned whadr = wadr + (unsigned)((NB == 3 && wm == 3 ? 2 : wm) * 1024);
  u32x4 xP[3], wP[5], xQ[3], wQ[5];
#define KA_RD(dst, base, off) dst = *(LAS u32x4*)((base) + (off))
#define KA_M(XC, WC, b) do { acc[0][b] = MFMA16(BF8(WC[b]), BF8(XC[0]), acc[0][b]); acc[1][b] = MFMA16(BF8(WC[b]), BF8(XC[1]), acc[1][b]); } while (0)
#define KA_STEP(XC, WC, XN, WN, nb, G0, G1) do { \
    KA_M(XC, WC, 0); SCHED0(); KA_RD(WN[0], nb, wadr); KA_RD(XN[0], nb, xadr); KA_RD(XN[1], nb, xadr + 256); G0; SCHED0(); \
    KA_M(XC, WC, 1); SCHED0(); KA_RD(WN[1], nb, wadr + 1024); KA_RD(WN[2], nb, wadr + 2048); SCHED0(); \
    KA_M(XC, WC, 2); SCHED0(); if (NB == 4) KA_RD(WN[3], nb, wadr + 3072); KA_RD(XN[2], nb, hadr); G1; SCHED0(); \
    if (NB == 4) KA_M(XC, WC, 3); \
    acch = MFMA16(BF8(WC[4]), BF8(XC[2]), acch); SCHED0(); KA_RD(WN[4], nb, whadr); SCHED0(); } while (0)
  KA_RD(wP[0], lds, wadr); KA_RD(xP[0], lds, xadr); KA_RD(xP[1], lds, xadr + 256); KA_RD(wP[1], lds, wadr + 1024); KA_RD(wP[2], lds, wadr + 2048);
  if (NB == 4) KA_RD(wP[3], lds, wadr + 3072);
  KA_RD(xP[2], lds, hadr); KA_RD(wP[4], lds, whadr);
  KA_STEP(xP, wP, xQ, wQ, lds + KA_STAGE, KA_G0(5), KA_G1(5));
  int cur = 2, fre = 0;
#pragma unroll 1
  for (int t = 2; t < 14; t += 2) {
    WAITVL(4);
    BAR(); asm volatile("" ::: "memory");
    LAS char* sb = lds + cur * KA_STAGE;
    KA_STEP(xQ, wQ, xP, wP, sb, KA_G0(fre), KA_G1(fre));
    KA_STEP(xP, wP, xQ, wQ, sb + KA_STAGE, KA_G0(fre + 1), KA_G1(fre + 1));
    fre = cur; cur = cur == 4 ? 0 : cur + 2;
  }
#pragma unroll 1
  for (int t = 14; t < 18; t += 2) {
    if (t == 14) WAITVL(4); else WAITVL(0);
    BAR(); asm volatile("" ::: "memory");
    LAS char* sb = lds + cur * KA_STAGE;
    KA_STEP(xQ, wQ, xP, wP, sb, (void)0, (void)0);
    KA_STEP(xP, wP, xQ, wQ, sb + KA_STAGE, (void)0, (void)0);
    cur = cur == 4 ? 0 : cur + 2;
  }
  KA_M(xQ, wQ, 0); KA_M(xQ, wQ, 1); KA_M(xQ, wQ, 2);
  if (NB == 4) KA_M(xQ, wQ, 3);
  acch = MFMA16(BF8(wQ[4]), BF8(xQ[2]), acch);
}

extern "C" __global__ __launch_bounds__(576, 1) void kA(const u16* __restrict__ x, const u16* __restrict__ wo, const u16* __restrict__ wg,
                                                        const u16* __restrict__ wu, char* __restrict__ ws) {
  extern __shared__ __attribute__((aligned(16))) char smem[];
  LAS char* lds = (LAS char*)smem;
  const int tid = threadIdx.x, lane = tid & 63, w = __builtin_amdgcn_readfirstlane(tid >> 6);
  const int bx = blockIdx.x, xcd = bx & 7, jj = bx >> 3;
  const int mt = (xcd >> 1) * 4 + (jj & 3), hp = (xcd & 1) * 8 + (jj >> 2);
  const int g = lane >> 4, r = lane & 15, wm = w & 3, wn = w >> 2;
  const auto rs = WS_RSRC(ws);
  if (w == 8) {
    const int th = hp & 1, n8 = hp >> 1;
    unsigned po[9];
#pragma unroll
    for (int j = 0; j < 9; ++j) {
      const int q = j * 64 + lane, k = (q * 7282) >> 16;
      po[j] = (unsigned)(k * (D_ * 2) + (q - k * 9) * 16 + n8 * 144);
    }
#define KA_WOG(gi) do { _Pragma("unroll") for (int _j = 0; _j < 9; ++_j) glds16((const char*)wo + (size_t)(gi) * (64 * D_ * 2) + po[_j], lds + KA_WO + ((gi) * 9 + _j) * 1024); } while (0)
#pragma unroll 1
    for (int i = 0; i < 9; ++i) {
      BAR();
      if (i >= 1 && i < 5) KA_WOG(i + 4);
      else if (i == 5) {
#pragma unroll
        for (int j = 0; j < 9; ++j) glds16((const char*)x + (size_t)(mt * 128 + th * 64) * (D_ * 2) + po[j], lds + KA_XR + j * 1024);
      } else if (i == 6) {
#pragma unroll
        for (int j = 0; j < 5; ++j) { const int o = j * 1024 + lane * 16; glds16(ws + WS_WM + (o < 4608 ? o : 0), lds + KA_WM + j * 1024); }
      }
    }
    BAR();
    KA_WOG(0); KA_WOG(1);
    BAR();
    KA_WOG(2); KA_WOG(3); KA_WOG(4);
    WAITV(0);
    touch_code(CODE_ADDR("kC"), CODE_ADDR("kD"), lane, lds + KA_SCR);
#pragma unroll
    for (int i = 0; i < 2; ++i) {
      const int l54 = lane < 54 ? lane : 53, k3 = (l54 * 3641) >> 16;
      touch4((const char*)((i & 1) ? wu : wg) + (size_t)(k3 * 32 + jj) * (F_ * 2) + (xcd >> 1) * 2304 + (l54 - k3 * 18) * 128, lds + KA_SCR);
    }
    BAR();
    BAR(); BAR(); BAR();
    return;
  }
  const char* s0 = ws + WS_XN + (size_t)((mt * 4 + (w >> 1)) * 36 + (w & 1)) * 1024 + lane * 16;
  const char* s1 = w ? ws + WS_WQ + (size_t)(hp * 126 + (w - 1)) * 1024 + lane * 16
                     : ws + WS_XN + (size_t)((mt ? mt * 4 - 1 : 0) * 36 + (g >> 1)) * 1024 + ((g & 1) * 32 + 16 + r) * 16;
  const unsigned st0 = 2048, st1 = w ? 7168u : 2048u;
#pragma unroll
  for (int st = 0; st < 5; ++st) { KA_G0(st); KA_G1(st); }
  unsigned sto[5][2];
#pragma unroll
  for (int b = 0; b < 5; ++b) {
    const int pd = 4 * (4 * wn + (b < 4 ? b : wm)) + g, sg = (pd * 57) >> 9, c0 = (pd - sg * 9) * 4;
#pragma unroll
    for (int e = 0; e < 2; ++e) {
      const int c = c0 + 2 * e, hd = c >= 18 ? 1 : 0, d = c - 18 * hd;
      sto[b][e] = (unsigned)(r * KA_QROW + (pd < 27 ? (sg * 2 + hd) * 48 + d * 2 : 288 + 4 * e));
    }
  }
  f32x4 acc[2][4], acch = {0.f, 0.f, 0.f, 0.f};
#pragma unroll
  for (int a = 0; a < 2; ++a)
#pragma unroll
    for (int b = 0; b < 4; ++b) acc[a][b] = (f32x4){0.f, 0.f, 0.f, 0.f};
  WAITV(6);
  BAR(); asm volatile("" ::: "memory");
  if (wn == 0) kA_loop<4>(lds, s0, s1, st0, st1, w, lane, acc, acch);
  else kA_loop<3>(lds, s0, s1, st0, st1, w, lane, acc, acch);
  WAITL0();
  BAR(); asm volatile("" ::: "memory");
  const int tok = mt * 128 + (hp & 1) * 64 + wm * 16 + r;
  const float xm = *(const float*)(ws + WS_XM + (size_t)tok * 4);
  asm volatile("" ::: "memory");
  unsigned pa[5], pm4[5];
  const unsigned p4s = (r & 3) < 2 ? 288u * 144u : 288u * 8u, p4t = (r & 3) < 2 ? 576u : 32u;
#pragma unroll
  for (int i = 0; i < 5; ++i) {
    const int c = 8 * i + 4 * wn + g, h5 = (c * 205) >> 10, krow = 36 * h5 + 8 * (c - 5 * h5) + (r >> 2), p = r & 3;
    pa[i] = (unsigned)(KA_WO + krow * 144 + p * 8);
    pm4[i] = p < 2 ? (unsigned)(KA_WO + krow * 144 + 128 + p * 8) : (unsigned)(KA_WM + krow * 8);
  }
  {
    LAS char* row0 = lds + (2 * wm + 1) * 16 * KA_QROW;
#pragma unroll
    for (int b = 0; b < 4; ++b)
#pragma unroll
      for (int a = 0; a < 2; ++a) {
        *(LAS unsigned*)(row0 + a * 16 * KA_QROW + sto[b][0]) = pk_bf16(acc[a][b][0], acc[a][b][1]);
        *(LAS unsigned*)(row0 + a * 16 * KA_QROW + sto[b][1]) = pk_bf16(acc[a][b][2], acc[a][b][3]);
      }
    *(LAS unsigned*)(lds + sto[4][0]) = pk_bf16(acch[0], acch[1]); *(LAS unsigned*)(lds + sto[4][1]) = pk_bf16(acch[2], acch[3]);
  }
  WAITL0();
  BAR(); asm volatile("" ::: "memory");
  {
    const int hf = tid & 1, t = (tid >> 1) & 127, hd = tid >> 8, T = mt * 128 + t;
    LAS char* base = lds + (t + 16) * KA_QROW + hd * 48;
    const u32x4 qa = *(LAS u32x4*)(base), qb = *(LAS u32x4*)(base + 16); const unsigned qc = *(LAS unsigned*)(base + 32);
#define XOR1(v) __builtin_amdgcn_mov_dpp((v), 0xB1, 0xF, 0xF, true)
#define XOR1F(v) __builtin_bit_cast(float, XOR1(__builtin_bit_cast(int, (v))))
    float lg[4];
#pragma unroll
    for (int i = 0; i < 4; ++i) {
      const int wv = hf * 4 + i;
      LAS char* kp = base - wv * 2 * KA_QROW + 96;
      const u32x4 a = *(LAS u32x4*)(kp), b = *(LAS u32x4*)(kp + 16); const unsigned c = *(LAS unsigned*)(kp + 32);
      float s = 0.f;
      s = dot2bf(qa[0], a[0], s); s = dot2bf(qa[1], a[1], s); s = dot2bf(qa[2], a[2], s); s = dot2bf(qa[3], a[3], s);
      s = dot2bf(qb[0], b[0], s); s = dot2bf(qb[1], b[1], s); s = dot2bf(qb[2], b[2], s); s = dot2bf(qb[3], b[3], s);
      s = dot2bf(qc, c, s);
      s = rbf(rbf(s) * 0.23570226039551584f);
      lg[i] = (T - 2 * wv >= 0) ? s : -1.0e9f;
    }
    float mx = fmaxf(fmaxf(lg[0], lg[1]), fmaxf(lg[2], lg[3]));
    mx = fmaxf(mx, XOR1F(mx));
    float sm = 0.f;
#pragma unroll
    for (int i = 0; i < 4; ++i) { lg[i] = __builtin_amdgcn_exp2f((lg[i] - mx) * 1.4426950408889634f); sm += lg[i]; }
    sm += XOR1F(sm);
    const float inv = __builtin_amdgcn_rcpf(sm);
    const unsigned p0 = pk_bf16(lg[0] * inv, lg[1] * inv), p1 = pk_bf16(lg[2] * inv, lg[3] * inv);
    const unsigned q0 = (unsigned)XOR1((int)p0), q1 = (unsigned)XOR1((int)p1);
    const unsigned pw[4] = {hf ? q0 : p0, hf ? q1 : p1, hf ? p0 : q0, hf ? p1 : q1};
    float o[10];
#pragma unroll
    for (int d = 0; d < 10; ++d) o[d] = 0.f;
#pragma unroll
    for (int wp = 0; wp < 4; ++wp) {
      const unsigned pp = pw[wp];
      LAS char* v0 = base - (2 * wp) * 2 * KA_QROW + 192 + hf * 16;
      LAS char* v1 = v0 - 2 * KA_QROW;
      const u32x4 a = *(LAS u32x4*)(v0), b = *(LAS u32x4*)(v1); const unsigned a4 = *(LAS unsigned*)(v0 + 16), b4 = *(LAS unsigned*)(v1 + 16);
      const unsigned av[5] = {a[0], a[1], a[2], a[3], a4}, bv[5] = {b[0], b[1], b[2], b[3], b4};
#pragma unroll
      for (int i = 0; i < 5; ++i) {
        o[2 * i]     = dot2bf(pp, __builtin_amdgcn_perm(bv[i], av[i], 0x05040100u), o[2 * i]);
        o[2 * i + 1] = dot2bf(pp, __builtin_amdgcn_perm(bv[i], av[i], 0x07060302u), o[2 * i + 1]);
      }
    }
    LAS char* op = lds + KA_OOFF + t * 80 + hd * 36 + hf * 16;
#pragma unroll
    for (int i = 0; i < 5; ++i) *(LAS unsigned*)(op + 4 * i) = pk_bf16(o[2 * i], o[2 * i + 1]);
    if (hd == 0) *(LAS unsigned*)(lds + KA_OOFF + t * 80 + 72 + hf * 4) = 0u;
  }
  WAITL0();
  BAR(); asm volatile("" ::: "memory");
#pragma unroll
  for (int i = 0; i < 2; ++i) {
    const int id = tid + i * 512;
    if (id < 640) {
      const int mb = (id * 820) >> 16, rem = id - mb * 80;
      const u32x4 v = *(LAS u32x4*)(lds + KA_OOFF + (mb * 16 + (rem & 15)) * 80 + (rem >> 4) * 16);
      ST16(rs, WS_O + ((mt * 8 + mb) * 80 + hp * 5) * 256 + rem * 16, v);
    }
  }
  WAITV(0);
  BAR(); asm volatile("" ::: "memory");
  GAS unsigned* cnt = (GAS unsigned*)(ws + WS_CNT) + (256 + mt) * 64;
  if (tid == 0) __hip_atomic_fetch_add(cnt, 1u, __ATOMIC_RELAXED, __HIP_MEMORY_SCOPE_AGENT);
  u32x4 wf[4][5];
#define KA_P2R(set, i) do { _Pragma("unroll") for (int _b = 0; _b < 4; ++_b) wf[set][_b] = tr_frag(lds + pa[(i) % 5] + ((i) / 5) * (288 * 144) + _b * 32, 576); \
      wf[set][4] = tr_frag2(lds + pm4[(i) % 5] + ((i) / 5) * p4s, lds + pm4[(i) % 5] + ((i) / 5) * p4s + p4t); } while (0)
  KA_P2R(0, 0); KA_P2R(1, 1); KA_P2R(2, 2); KA_P2R(3, 3);
  SCHED0();
  if (w == 0) {
    unsigned spins = 0;
    while (__builtin_amdgcn_readfirstlane(__hip_atomic_load(cnt, __ATOMIC_RELAXED, __HIP_MEMORY_SCOPE_AGENT)) < 16u) {
      if (++spins > 400000u) break;
      __builtin_amdgcn_s_sleep(1);
    }
  }
  asm volatile("" ::: "memory");
  BAR(); asm volatile("" ::: "memory");
  {
    const int n8 = hp >> 1;
    const int oo = (int)(WS_O + ((tok >> 4) * 80 + 4 * wn) * 256 + lane * 16);
    u32x4 of[10];
#pragma unroll
    for (int i = 0; i < 10; ++i) of[i] = __builtin_amdgcn_raw_buffer_load_b128(rs, oo + i * 2048, 0, 16);
    f32x4 c[5];
#pragma unroll
    for (int b = 0; b < 5; ++b) c[b] = (f32x4){0.f, 0.f, 0.f, 0.f};
#define KA_P2M(set, i) do { _Pragma("unroll") for (int _b = 0; _b < 5; ++_b) c[_b] = MFMA16(BF8(wf[set][_b]), BF8(of[i]), c[_b]); } while (0)
    SCHED0();
#pragma unroll
    for (int i = 0; i < 10; ++i) {
      KA_P2M(i & 3, i); SCHED0();
      if (i + 4 < 10) KA_P2R(i & 3, i + 4);
      SCHED0();
    }
    if (wn == 1) {
#pragma unroll
      for (int b = 0; b < 5; ++b) *(LAS f32x4*)(lds + KA_RED + (wm * 5 + b) * 1024 + lane * 16) = c[b];
    }
    WAITL0();
    BAR(); asm volatile("" ::: "memory");
    if (wn == 0) {
#pragma unroll
      for (int b = 0; b < 5; ++b) c[b] += *(LAS f32x4*)(lds + KA_RED + (wm * 5 + b) * 1024 + lane * 16);
      const float mean = xm + __shfl(c[4][0] + c[4][1], 32 + r), mb = rbf(mean);
      LAS char* xq = lds + KA_XR + (wm * 16 + r) * 144 + g * 8;
      unsigned hw_[10], cw_[10];
      f32x2 qv = {0.f, 0.f};
#pragma unroll
      for (int b = 0; b < 5; ++b) {
        const u32x2 xv = *(LAS u32x2*)(xq + ((b < 4 || g < 2) ? b * 32 : 0));
#pragma unroll
        for (int e = 0; e < 2; ++e) {
          const unsigned a = pk_bf16(c[b][2 * e], c[b][2 * e + 1]);
          const unsigned hh = pk_bf16(bf_lo(xv[e]) + bf_lo(a), bf_hi(xv[e]) + bf_hi(a));
          const f32x2 hv = {bf_lo(hh), bf_hi(hh)};
          const f32x2 d = hv - mean, dc = hv - mb;
          hw_[2 * b + e] = hh; cw_[2 * b + e] = pk_bf16(dc[0], dc[1]);
          if (b < 4) qv += d * d; else qv += g < 2 ? d * d : (f32x2){0.f, 0.f};
        }
      }
      const int ho = ((tok >> 4) * 72 + n8 * 9 + 2 * (g & 1) + (g >> 1)) * 256 + r * 16;
#pragma unroll
      for (int b = 0; b < 4; b += 2) {
        const auto c0 = __builtin_amdgcn_permlane16_swap(cw_[2 * b], cw_[2 * b + 2], false, false), c1 = __builtin_amdgcn_permlane16_swap(cw_[2 * b + 1], cw_[2 * b + 3], false, false);
        const auto h0 = __builtin_amdgcn_permlane16_swap(hw_[2 * b], hw_[2 * b + 2], false, false), h1 = __builtin_amdgcn_permlane16_swap(hw_[2 * b + 1], hw_[2 * b + 3], false, false);
        ST16(rs, WS_HC + ho + b * 512, ((u32x4){c0[0], c1[0], c0[1], c1[1]}));
        ST16(rs, WS_H + ho + b * 512, ((u32x4){h0[0], h1[0], h0[1], h1[1]}));
      }
      const auto s0 = __builtin_amdgcn_permlane32_swap(cw_[8], hw_[8], false, false), s1 = __builtin_amdgcn_permlane32_swap(cw_[9], hw_[9], false, false);
      ST8(rs, (g < 2 ? WS_HC : WS_H) + ((tok >> 4) * 72 + n8 * 9 + 8) * 256 + r * 16 + (g & 1) * 8, ((u32x2){s0[0], s1[0]}));
      float q = qv[0] + qv[1];
      q += __shfl_xor(q, 16); q += __shfl_xor(q, 32);
      if (g == 0) ST4(rs, WS_ST + (tok * 8 + n8) * 4, __float_as_uint(q));
    }
  }
}

constexpr int KC_UPOFF = 16384 + 9216 + 128;
constexpr int KC_STAGE = 35072;
constexpr int KC_STOFF = 4 * KC_STAGE;
constexpr int KC_SCR = KC_STOFF + 8192;
constexpr int KC_LDS = KC_SCR + 256;

DI f32x2 geglu2(f32x2 a, f32x2 b, f32x2 k0, f32x2 k1, f32x2 ik2) {
  const f32x2 tt = a * __builtin_elementwise_fma(a * a, k1, k0);
  const f32x2 e = {__builtin_amdgcn_exp2f(tt[0]), __builtin_amdgcn_exp2f(tt[1])};
  const f32x2 d = __builtin_elementwise_fma(e, ik2, ik2);
  const f32x2 rr = {__builtin_amdgcn_rcpf(d[0]), __builtin_amdgcn_rcpf(d[1])};
  return (a * b) * rr;
}
DI void ln_consts(const LAS char* sp_, f32x2& k0, f32x2& k1, f32x2& ik2) {
  const LAS f32x4* sp = (const LAS f32x4*)sp_;
  const f32x4 s4 = sp[0] + sp[1];
  const float var = rbf(((s4[0] + s4[1]) + (s4[2] + s4[3])) * (1.0f / D_));
  const float sd = rbf(__builtin_amdgcn_sqrtf(rbf(var + 1e-5f)));
  const float rs = __builtin_amdgcn_rcpf(sd);
  const float k0s = -2.3022081981f * rs;
  const float k1s = -0.1029432397f * rs * rs * rs;
  const float ik2s = sd * sd;
  k0 = (f32x2){k0s, k0s}; k1 = (f32x2){k1s, k1s}; ik2 = (f32x2){ik2s, ik2s};
}

extern "C" __global__ __launch_bounds__(768, 1) void kC(const u16* __restrict__ wg, const u16* __restrict__ wu, const u16* __restrict__ wd, char* __restrict__ ws) {
  extern __shared__ __attribute__((aligned(16))) char smem[];
  LAS char* lds = (LAS char*)smem;
  const int tid = threadIdx.x, lane = tid & 63, w = __builtin_amdgcn_readfirstlane(tid >> 6);
  const int bx = blockIdx.x, xcd = bx & 7, jj = bx >> 3;
  const int pm = (xcd & 1) * 4 + (jj & 3), pn = (xcd >> 1) * 8 + (jj >> 2);
  const int wm = w & 3, wn = w >> 2;

  const char* src[3]; unsigned dst[3]; unsigned step[3];
  src[0] = ws + WS_HC + (size_t)(pm * 16 + w) * (72 * 256) + lane * 16; dst[0] = w * 1024; step[0] = 1024;
#define KC_G(i, st) do { glds16(src[i], lds + ((st) & 3) * KC_STAGE + dst[i]); src[i] += step[i]; } while (0)
  KC_G(0, 0);
#pragma unroll
  for (int i = 1; i < 3; ++i) {
    int sl = i * 12 + w; sl = sl > 33 ? 33 : sl;
    if (sl < 16) {
      src[i] = ws + WS_HC + (size_t)(pm * 16 + sl) * (72 * 256) + lane * 16;
      dst[i] = sl * 1024; step[i] = 1024;
    } else {
      const int c = sl - 16, m = c >= 9 ? 1 : 0, cc = c - 9 * m, q = cc * 64 + lane, rr = (q * 3641) >> 16, ch = q - rr * 18;
      const int kr = (rr & 19) | ((rr & 4) << 1) | ((rr & 8) >> 1);
      src[i] = (const char*)(m ? wu : wg) + pn * 288 + (unsigned)(kr * (F_ * 2) + ch * 16);
      dst[i] = (m ? KC_UPOFF : 16384) + cc * 1024; step[i] = 32 * F_ * 2;
    }
    KC_G(i, 0);
  }
#define KC_ISSUE(st) do { KC_G(0, st); KC_G(1, st); KC_G(2, st); } while (0)
  KC_ISSUE(1); KC_ISSUE(2);
  glds16(ws + WS_ST + (size_t)pm * 8192 + (w & 7) * 1024 + lane * 16, lds + KC_STOFF + (w & 7) * 1024);
  const auto ars = __builtin_amdgcn_make_buffer_rsrc((void*)(ws + WS_ACT), 0, S_ * F_ * 2, 0x00020000);
#define KC_TOUCH_WD() do { if (w < 6) {     \
    int ln = w * 64 + lane; ln = ln < 324 ? ln : 323; \
    touch4((const char*)wd + (size_t)(xcd >> 1) * (1152 * D_ * 2) + (size_t)(jj * 324 + ln) * 128, lds + KC_SCR); } \
    else if (w == 6) touch_code(CODE_ADDR("kD"), CODE_ADDR("kD_end_lbl"), lane, lds + KC_SCR); } while (0)

  const int g = lane >> 4, r = lane & 15;
  f32x4 acc[6][4];
#pragma unroll
  for (int a = 0; a < 6; ++a)
#pragma unroll
    for (int b = 0; b < 4; ++b) acc[a][b] = (f32x4){0.f, 0.f, 0.f, 0.f};
  const unsigned xadr = (unsigned)(wm * 4096 + lane * 16);
  const unsigned wadr = (unsigned)(16384 + (16 * (g >> 1) + 4 * (g & 1) + (r >> 2)) * 288 + wn * 96 + (r & 3) * 8);
  u32x4 xf[4], wA[3], wB[3];
#define KC_WOFF(j) ((((j) & 1) ? (KC_UPOFF - 16384) : 0) + ((j) >> 1) * 32)
#define KC_RW(WF, i, j, base) WF[i] = tr_frag((base) + wadr + KC_WOFF(j), 2304)
#define KC_RX(c, base) xf[c] = *(LAS u32x4*)((base) + xadr + (c) * 1024)
#define KC_M3(WF, j0, c) do { acc[j0][c] = MFMA16(BF8(WF[0]), BF8(xf[c]), acc[j0][c]); acc[j0 + 1][c] = MFMA16(BF8(WF[1]), BF8(xf[c]), acc[j0 + 1][c]); \
    acc[j0 + 2][c] = MFMA16(BF8(WF[2]), BF8(xf[c]), acc[j0 + 2][c]); } while (0)
#define KC_HALFA(base, G0, G1) do { \
    KC_M3(wA, 0, 0); SCHED0(); KC_RW(wB, 0, 3, base); SCHED0(); \
    KC_M3(wA, 0, 1); SCHED0(); KC_RW(wB, 1, 4, base); G0; SCHED0(); \
    KC_M3(wA, 0, 2); SCHED0(); KC_RW(wB, 2, 5, base); SCHED0(); \
    KC_M3(wA, 0, 3); SCHED0(); G1; SCHED0(); } while (0)
#define KC_HALFB(nb, G0) do { \
    KC_M3(wB, 3, 0); SCHED0(); KC_RX(0, nb); KC_RW(wA, 0, 0, nb); SCHED0(); \
    KC_M3(wB, 3, 1); SCHED0(); KC_RX(1, nb); KC_RW(wA, 1, 1, nb); SCHED0(); \
    KC_M3(wB, 3, 2); SCHED0(); KC_RX(2, nb); KC_RW(wA, 2, 2, nb); G0; SCHED0(); \
    KC_M3(wB, 3, 3); SCHED0(); KC_RX(3, nb); SCHED0(); } while (0)
  WAITV(7); BAR(); asm volatile("" ::: "memory");
  KC_RX(0, lds); KC_RW(wA, 0, 0, lds); KC_RW(wA, 1, 1, lds); KC_RW(wA, 2, 2, lds); KC_RX(1, lds); KC_RX(2, lds); KC_RX(3, lds);
  KC_ISSUE(3);
  KC_HALFA(lds, (void)0, (void)0);
#pragma unroll 1
  for (int t = 1; t < 15; ++t) {
    WAITVL(6);
    BAR(); asm volatile("" ::: "memory");
    LAS char* sb = lds + (t & 3) * KC_STAGE;
    KC_HALFB(sb, KC_G(0, t + 3));
    KC_HALFA(sb, KC_G(1, t + 3), KC_G(2, t + 3));
  }
  WAITVL(0);
  BAR(); asm volatile("" ::: "memory");
  KC_TOUCH_WD();
#pragma unroll 1
  for (int t = 15; t < 18; ++t) {
    LAS char* sb = lds + (t & 3) * KC_STAGE;
    KC_HALFB(sb, (void)0);
    KC_HALFA(sb, (void)0, (void)0);
  }
  KC_M3(wB, 3, 0); KC_M3(wB, 3, 1); KC_M3(wB, 3, 2); KC_M3(wB, 3, 3);
#pragma unroll
  for (int cp = 0; cp < 2; ++cp) {
    f32x2 k0[2], k1[2], ik2[2];
#pragma unroll
    for (int e = 0; e < 2; ++e) ln_consts(lds + KC_STOFF + (wm * 64 + cp * 32 + e * 16 + r) * 32, k0[e], k1[e], ik2[e]);
#pragma unroll
    for (int f = 0; f < 3; ++f) {
      u32x2 pc[2];
#pragma unroll
      for (int e = 0; e < 2; ++e) {
        const f32x4 a = acc[2 * f][2 * cp + e], b = acc[2 * f + 1][2 * cp + e];
        const f32x2 o0 = geglu2((f32x2){a[0], a[1]}, (f32x2){b[0], b[1]}, k0[e], k1[e], ik2[e]);
        const f32x2 o1 = geglu2((f32x2){a[2], a[3]}, (f32x2){b[2], b[3]}, k0[e], k1[e], ik2[e]);
        pc[e] = (u32x2){pk_bf16(o0[0], o0[1]), pk_bf16(o1[0], o1[1])};
      }
      const auto s0 = __builtin_amdgcn_permlane32_swap(pc[0][0], pc[1][0], false, false);
      const auto s1 = __builtin_amdgcn_permlane32_swap(pc[0][1], pc[1][1], false, false);
      const u32x4 v = {s0[0], s1[0], s0[1], s1[1]};
      __builtin_amdgcn_raw_buffer_store_b128(v, ars, ((pm * 8 + wm * 2 + cp) * 288 + pn * 9 + wn * 3 + f) * 1024 + ((g & 1) * 32 + (g >> 1) * 16 + r) * 16, 0, 16);
    }
  }
}

constexpr int KD_STAGE = 16384 + 18432;
constexpr int KD_LDS = 2 * KD_STAGE + 72 * 1024;

template <int NB, int NG>
DI void kD_loop(LAS char* lds, const char* srcA, const char* const (&srcW)[3], int w, int lane, f32x4 (&acc)[4][5]) {
  const int gk = w >> 2, wm = w & 1, g = lane >> 4, r = lane & 15;
#define KD_GA(i, st) glds16(srcA + (size_t)(i) * (2 * 288 * 1024) + (st) * 4096, lds + ((st) & 3) * KD_STAGE + (i) * 8192 + w * 1024)
#define KD_GW(i, st) glds16(srcW[i] + (size_t)(st) * (64 * D_ * 2), lds + ((st) & 3) * KD_STAGE + 16384 + (i) * 8192 + w * 1024)
#define KD_GW2(st) do { if (NG == 5) glds16(srcW[2] + (size_t)(st) * (64 * D_ * 2), lds + ((st) & 3) * KD_STAGE + 32768 + (w & 1) * 1024); } while (0)
#define KD_WAIT2() do { if (NG == 5) WAITVL(10); else WAITVL(8); } while (0)
  const unsigned xadr = (unsigned)(wm * 8192 + (gk * 2 + (g >> 1)) * 1024 + ((g & 1) * 32 + r) * 16);
  const unsigned wadr = (unsigned)(16384 + (gk * 32 + 16 * (g >> 1) + 4 * (g & 1) + (r >> 2)) * 288 + (NB == 5 ? 0 : 160) + (r & 3) * 8);
  u32x4 xP[4], wP[5], xQ[4], wQ[5];
#define KD_RW(WF, b, base) WF[b] = tr_frag((base) + wadr + (b) * 32, 2304)
#define KD_RX(XF, a, base) XF[a] = *(LAS u32x4*)((base) + xadr + ((a) >> 1) * 4096 + ((a) & 1) * 256)
#define KD_M(XC, WC, b) do { _Pragma("unroll") for (int _a = 0; _a < 4; ++_a) acc[_a][b] = MFMA16(BF8(WC[b]), BF8(XC[_a]), acc[_a][b]); } while (0)
#define KD_STEP(XC, WC, XN, WN, nb, G0, G1, G2, G3) do { \
    KD_M(XC, WC, 0); SCHED0(); KD_RW(WN, 0, nb); KD_RX(XN, 0, nb); KD_RX(XN, 1, nb); G0; SCHED0(); \
    KD_M(XC, WC, 1); SCHED0(); KD_RX(XN, 2, nb); KD_RX(XN, 3, nb); KD_RW(WN, 1, nb); G1; SCHED0(); \
    KD_M(XC, WC, 2); SCHED0(); KD_RW(WN, 2, nb); KD_RW(WN, 3, nb); G2; SCHED0(); \
    KD_M(XC, WC, 3); SCHED0(); if (NB == 5) KD_RW(WN, 4, nb); G3; SCHED0(); \
    if (NB == 5) { KD_M(XC, WC, 4); SCHED0(); } } while (0)
  KD_RW(wP, 0, lds); KD_RX(xP, 0, lds); KD_RX(xP, 1, lds); KD_RX(xP, 2, lds); KD_RX(xP, 3, lds); KD_RW(wP, 1, lds); KD_RW(wP, 2, lds); KD_RW(wP, 3, lds);
  if (NB == 5) KD_RW(wP, 4, lds);
  KD_GA(0, 3); KD_GA(1, 3); KD_GW(0, 3); KD_GW(1, 3); KD_GW2(3);
#pragma unroll 1
  for (int t = 1; t < 15; t += 2) {
    KD_WAIT2();
    BAR(); asm volatile("" ::: "memory");
    KD_STEP(xP, wP, xQ, wQ, lds + (t & 3) * KD_STAGE, do { KD_GA(0, t + 3); KD_GW2(t + 3); } while (0), KD_GA(1, t + 3), KD_GW(0, t + 3), KD_GW(1, t + 3));
    KD_WAIT2();
    BAR(); asm volatile("" ::: "memory");
    KD_STEP(xQ, wQ, xP, wP, lds + ((t + 1) & 3) * KD_STAGE, do { KD_GA(0, t + 4); KD_GW2(t + 4); } while (0), KD_GA(1, t + 4), KD_GW(0, t + 4), KD_GW(1, t + 4));
  }
  KD_WAIT2(); BAR(); asm volatile("" ::: "memory");
  KD_STEP(xP, wP, xQ, wQ, lds + 3 * KD_STAGE, (void)0, (void)0, (void)0, (void)0);
  if (NG == 5) WAITVL(5); else WAITVL(4);
  BAR(); asm volatile("" ::: "memory");
  KD_STEP(xQ, wQ, xP, wP, lds, (void)0, (void)0, (void)0, (void)0);
  WAITVL(0); BAR(); asm volatile("" ::: "memory");
  KD_STEP(xP, wP, xQ, wQ, lds + KD_STAGE, (void)0, (void)0, (void)0, (void)0);
  KD_M(xQ, wQ, 0); KD_M(xQ, wQ, 1); KD_M(xQ, wQ, 2); KD_M(xQ, wQ, 3);
  if (NB == 5) KD_M(xQ, wQ, 4);
}

extern "C" __global__ __launch_bounds__(512, 1) void kD(const u16* __restrict__ wd, char* __restrict__ ws, u16* __restrict__ out) {
  extern __shared__ __attribute__((aligned(16))) char smem[];
  LAS char* lds = (LAS char*)smem;
  const int tid = threadIdx.x, lane = tid & 63, w = __builtin_amdgcn_readfirstlane(tid >> 6);
  const int bx = blockIdx.x, xcd = bx & 7, jj = bx >> 3;
  const int ks = xcd >> 1, mt = (xcd & 1) * 8 + (jj & 7), nt = jj >> 3;
  const int gk = w >> 2, wm = w & 1, wn = ((w >> 1) & 1) ^ gk;
  const int g = lane >> 4, r = lane & 15;

  const char* srcA = ws + WS_ACT + (size_t)((mt * 4 + (w >> 2)) * 288 + ks * 72 + (w & 3)) * 1024 + lane * 16;
  KD_GA(0, 0); KD_GA(1, 0);
  const char* srcW[3];
#pragma unroll
  for (int i = 0; i < 3; ++i) {
    const int cc = i < 2 ? i * 8 + w : 16 + (w & 1);
    const int q = cc * 64 + lane, rr = (q * 3641) >> 16, ch = q - rr * 18;
    srcW[i] = (const char*)wd + (size_t)(ks * 1152 * D_ + nt * 144) * 2 + (unsigned)(rr * (D_ * 2) + ch * 16);
  }
  const bool extra = (w == 2 || w == 3);
#define KD_GX(st) do { if (extra) glds16(srcW[2] + (size_t)(st) * (64 * D_ * 2), lds + ((st) & 3) * KD_STAGE + 32768 + (w & 1) * 1024); } while (0)
  KD_GW(0, 0); KD_GW(1, 0); KD_GX(0);
  KD_GA(0, 1); KD_GA(1, 1); KD_GW(0, 1); KD_GW(1, 1); KD_GX(1);
  KD_GA(0, 2); KD_GA(1, 2); KD_GW(0, 2); KD_GW(1, 2); KD_GX(2);
  const char* hb = ws + WS_H + (size_t)((mt * 8 + ks * 2 + (g >> 1)) * 72 + nt * 18) * 256 + r * 16 + (g & 1) * 8;
  const u32x2 ha0 = *(const u32x2*)(hb + w * 512), ha1 = *(const u32x2*)(hb + w * 512 + 256), hb0 = *(const u32x2*)(hb + 4096), hb1 = *(const u32x2*)(hb + 4096 + 256);
  const u32x4 hres0 = {ha0[0], ha0[1], ha1[0], ha1[1]}, hres1 = {hb0[0], hb0[1], hb1[0], hb1[1]};
  asm volatile("" ::: "memory");
  f32x4 acc[4][5];
#pragma unroll
  for (int a = 0; a < 4; ++a)
#pragma unroll
    for (int b = 0; b < 5; ++b) acc[a][b] = (f32x4){0.f, 0.f, 0.f, 0.f};
  if (extra) WAITVL(10); else WAITVL(8);
  BAR(); asm volatile("" ::: "memory");
  if (wn == 0) kD_loop<5, 4>(lds, srcA, srcW, w, lane, acc);
  else if (extra) kD_loop<4, 5>(lds, srcA, srcW, w, lane, acc);
  else kD_loop<4, 4>(lds, srcA, srcW, w, lane, acc);
  const int qk = 2 * wm + gk;
  {
    LAS char* xw = lds + 2 * KD_STAGE + ((qk ^ 1) * 18 + wn * 5) * 1024 + lane * 16;
#pragma unroll
    for (int sb = 0; sb < 2; ++sb)
#pragma unroll
      for (int b = 0; b < 5; ++b)
        if (b < 4 || wn == 0) *(LAS f32x4*)(xw + (sb * 9 + b) * 1024) = gk ? acc[sb][b] : acc[2 + sb][b];
  }
  WAITL0();
  BAR(); asm volatile("" ::: "memory");
  const int tile = mt * 4 + nt;
  const auto prs = __builtin_amdgcn_make_buffer_rsrc((void*)(ws + WS_PART), 0, (int)(64 * 4 * 3 * PART_SLAB), 0x00020000);
  {
    LAS char* xr = lds + 2 * KD_STAGE + (qk * 18 + wn * 5) * 1024 + lane * 16;
    const int sidx = ks - (ks > qk ? 1 : 0);
    const int po = (int)(((tile * 4 + qk) * 3 + sidx) * PART_SLAB) + wn * 5120 + lane * 16;
#pragma unroll
    for (int b = 0; b < 5; ++b)
      if (b < 4 || wn == 0) {
        const f32x4 v0 = (gk ? acc[2][b] : acc[0][b]) + *(LAS f32x4*)(xr + b * 1024);
        const f32x4 v1 = (gk ? acc[3][b] : acc[1][b]) + *(LAS f32x4*)(xr + (9 + b) * 1024);
        if (qk != ks) {
          const auto s0 = __builtin_amdgcn_permlane32_swap(pk_bf16(v0[0], v0[1]), pk_bf16(v1[0], v1[1]), false, false);
          const auto s1 = __builtin_amdgcn_permlane32_swap(pk_bf16(v0[2], v0[3]), pk_bf16(v1[2], v1[3]), false, false);
          __builtin_amdgcn_raw_buffer_store_b128((u32x4){s0[0], s1[0], s0[1], s1[1]}, prs, po + b * 1024, 0, 16);
        } else {
          *(LAS f32x4*)(lds + (wn * 5 + b) * 2048 + lane * 16) = v0;
          *(LAS f32x4*)(lds + (wn * 5 + b) * 2048 + 1024 + lane * 16) = v1;
        }
      }
  }
  WAITVL(0);
  BAR(); asm volatile("" ::: "memory");
  GAS unsigned* cnt = (GAS unsigned*)(ws + WS_CNT);
  if (tid < 4 && tid != ks) __hip_atomic_fetch_add(cnt + (tile * 4 + tid) * 64, 1u, __ATOMIC_RELAXED, __HIP_MEMORY_SCOPE_AGENT);
  if (w == 0) {
    unsigned spins = 0;
    while (__builtin_amdgcn_readfirstlane(__hip_atomic_load(cnt + (tile * 4 + ks) * 64, __ATOMIC_RELAXED, __HIP_MEMORY_SCOPE_AGENT)) < 3u) {
      if (++spins > 400000u) break;
      __builtin_amdgcn_s_sleep(1);
    }
  }
  asm volatile("" ::: "memory");
  BAR(); asm volatile("" ::: "memory");
  u32x4 pp[2][3];
#pragma unroll
  for (int i = 0; i < 2; ++i)
#pragma unroll
    for (int q = 0; q < 3; ++q)
      if (i == 0 || w == 1) pp[i][q] = __builtin_amdgcn_raw_buffer_load_b128(prs, (int)(((tile * 4 + ks) * 3 + q) * PART_SLAB) + (i ? 8 : w) * 1024 + lane * 16, 0, 16);
#pragma unroll
  for (int i = 0; i < 2; ++i) {
    if (i == 1 && w != 1) break;
    const int grp = i ? 8 : w;
    const u32x4 hr = i ? hres1 : hres0;
    u16* op = out + (size_t)(mt * 128 + ks * 32 + (g >> 1) * 16 + r) * D_ + nt * 144 + grp * 16 + 4 * (g & 1);
#pragma unroll
    for (int hf = 0; hf < 2; ++hf) {
      f32x4 v = *(LAS f32x4*)(lds + grp * 2048 + (g >> 1) * 1024 + (((g & 1) + 2 * hf) * 16 + r) * 16);
#pragma unroll
      for (int q = 0; q < 3; ++q) v += (f32x4){bf_lo(pp[i][q][2 * hf]), bf_hi(pp[i][q][2 * hf]), bf_lo(pp[i][q][2 * hf + 1]), bf_hi(pp[i][q][2 * hf + 1])};
      const unsigned a0 = pk_bf16(v[0], v[1]), a1 = pk_bf16(v[2], v[3]);
      const unsigned o0 = pk_bf16(bf_lo(hr[2 * hf]) + bf_lo(a0), bf_hi(hr[2 * hf]) + bf_hi(a0));
      const unsigned o1 = pk_bf16(bf_lo(hr[2 * hf + 1]) + bf_lo(a1), bf_hi(hr[2 * hf + 1]) + bf_hi(a1));
      *(u32x2*)(op + 8 * hf) = (u32x2){o0, o1};
    }
  }
  asm volatile("kD_end_lbl:");
}

extern "C" void kernel_launch(void* const* d_in, const int* in_sizes, int n_in, void* d_out, int out_size, void* d_ws, size_t ws_size,
                              hipStream_t stream) {
  const u16* x = (const u16*)d_in[0];
  const u16* wqkv = (const u16*)d_in[1];
  const u16* wo = (const u16*)d_in[2];
  const u16* wg = (const u16*)d_in[4];
  const u16* wu = (const u16*)d_in[5];
  const u16* wd = (const u16*)d_in[6];
  char* ws = (char*)d_ws;
  (void)hipFuncSetAttribute((const void*)kA, hipFuncAttributeMaxDynamicSharedMemorySize, KA_LDS);
  (void)hipFuncSetAttribute((const void*)kC, hipFuncAttributeMaxDynamicSharedMemorySize, KC_LDS);
  (void)hipFuncSetAttribute((const void*)kD, hipFuncAttributeMaxDynamicSharedMemorySize, KD_LDS);
  k0<<<544, 512, K0_LDS, stream>>>(x, wqkv, wo, ws);
  kA<<<256, 576, KA_LDS, stream>>>(x, wo, wg, wu, ws);
  kC<<<256, 768, KC_LDS, stream>>>(wg, wu, wd, ws);
  kD<<<256, 512, KD_LDS, stream>>>(wd, ws, (u16*)d_out);
}
```
